```python
import torch

def _softmax(x, axis=-1):
    return torch.softmax(x, dim=axis)
def _gelu(x):
    return torch.nn.functional.gelu(x, approximate="tanh")
def _silu(x):
    return torch.nn.functional.silu(x)
def _sigmoid(x):
    return torch.sigmoid(x)
def _layernorm(x, axis=-1, eps=1e-5):
    m = x.mean(dim=axis, keepdim=True)
    v = x.var(dim=axis, keepdim=True, unbiased=False)
    return (x - m) / torch.sqrt(v + eps)
def _scatter_add(tgt, idx, src, axis=0):
    return tgt.clone().index_add_(axis, idx, src)
def _segment_sum(src, seg, n):
    out = torch.zeros((n,) + src.shape[1:], dtype=src.dtype)
    return out.index_add_(0, seg, src)
def _pad(x, pw):
    flat = tuple(v for pair in reversed(pw) for v in pair)
    return torch.nn.functional.pad(x, flat)

kernel_name = 'recurrent_scan__sliding_block_cached__mixer_channel_mlp__decode'

def setup_inputs():

    _w = (lambda a: (lambda t: t.to(torch.bfloat16) if t.is_floating_point() else t)(torch.as_tensor(a)))
    def _A_setup():
        B, Sq, D = 1, 1, 512
        Hq, Hkv, Sk, Dh = 32, 8, 65536, 128
        assert Hq % Hkv == 0, (Hq, Hkv)
        return {
            "x": _w((lambda *s: torch.randn(*s, dtype=torch.bfloat16) * 0.1)(B, Sq, D)),
            "k_cache": _w((lambda *s: torch.randn(*s, dtype=torch.bfloat16) * 0.8)(B, Hkv, Sk, Dh)),
            "v_cache": _w((lambda *s: torch.randn(*s, dtype=torch.bfloat16) * 3.2)(B, Hkv, Sk, Dh)),
            "wq": _w((lambda *s: torch.randn(*s, dtype=torch.bfloat16) * 0.8)(D, Hq * Dh) * (D ** -0.5)),
            "wo": _w((lambda *s: torch.randn(*s, dtype=torch.bfloat16) * 3.2)(Hq * Dh, D) * ((Hq * Dh) ** -0.5)),
        }
    _A_in = _A_setup()

    def _B_setup():
        B, S, D, F = 1, 1, 512, 1792
        return {
            "x": _w((lambda *s: torch.randn(*s, dtype=torch.bfloat16) * 0.1)(B, S, D)),
            "w1": _w((lambda *s: torch.randn(*s, dtype=torch.bfloat16) * 3.2)(D, F) * (D ** -0.5)),
            "b1": _w((lambda *s: torch.randn(*s, dtype=torch.bfloat16) * 3.2)(F)),
            "w2": _w((lambda *s: torch.randn(*s, dtype=torch.bfloat16) * 3.2)(F, D) * (F ** -0.5)),
            "b2": _w((lambda *s: torch.randn(*s, dtype=torch.bfloat16) * 3.2)(D)),
        }
    _B_in = _B_setup()

    return {
        "A_x": _A_in["x"],
        "A_k_cache": _A_in["k_cache"],
        "A_v_cache": _A_in["v_cache"],
        "A_wq": _A_in["wq"],
        "A_wo": _A_in["wo"],
        "B_x": _B_in["x"],
        "B_w1": _B_in["w1"],
        "B_b1": _B_in["b1"],
        "B_w2": _B_in["w2"],
        "B_b2": _B_in["b2"],
    }

def reference(A_x, A_k_cache, A_v_cache, A_wq, A_wo, B_x, B_w1, B_b1, B_w2, B_b2):
    def _A_ref(x, k_cache, v_cache, wq, wo):
        B, Sq, D = x.shape
        Hq, Dh = 32, 128
        Hkv, Sk = k_cache.shape[1], k_cache.shape[2]
        W = min(4096, max(256, Sk // 4))
        rep = Hq // Hkv
        q = (lambda x, *d: x.permute(*d))((x @ wq).reshape(B, Sq, Hq, Dh), 0, 2, 1, 3)
        k = (lambda x, n, axis: torch.repeat_interleave(x, n, dim=axis))(k_cache[:, :, Sk - W:, :], rep, axis=1)
        v = (lambda x, n, axis: torch.repeat_interleave(x, n, dim=axis))(v_cache[:, :, Sk - W:, :], rep, axis=1)
        scores = torch.einsum("bhqd,bhkd->bhqk", q, k) * (Dh ** -0.5)
        p = _softmax(scores, axis=-1)
        o = torch.einsum("bhqk,bhkd->bhqd", p, v)
        return (lambda x, *d: x.permute(*d))(o, 0, 2, 1, 3).reshape(B, Sq, Hq * Dh) @ wo

    def _B_ref(x, w1, b1, w2, b2):
        y = _layernorm(x, axis=-1)
        h = _gelu(torch.einsum("bsd,df->bsf", y, w1) + b1)
        return torch.einsum("bsf,fd->bsd", h, w2) + b2

    x = A_x
    xn = _layernorm(x, axis=-1)
    s = _A_ref(xn, A_k_cache, A_v_cache, A_wq, A_wo)
    g = _silu(_B_ref(xn, B_w1, B_b1, B_w2, B_b2))
    return x + s * g
```

```cpp
#include <hip/hip_runtime.h>
#include <hip/hip_bf16.h>
#include <stdint.h>

typedef unsigned short u16;
typedef unsigned u32x4 __attribute__((ext_vector_type(4)));

__device__ __forceinline__ float bf2f(u16 b) { return __uint_as_float((unsigned)b << 16); }
__device__ __forceinline__ float rbf(float f) {
  unsigned u = __float_as_uint(f);
  u += 0x7FFFu + ((u >> 16) & 1u);
  return __uint_as_float(u & 0xffff0000u);
}
__device__ __forceinline__ u16 f2bf(float f) { return (u16)(__float_as_uint(rbf(f)) >> 16); }
__device__ __forceinline__ float lo(unsigned w) { return __uint_as_float(w << 16); }
__device__ __forceinline__ float hi(unsigned w) { return __uint_as_float(w & 0xffff0000u); }

constexpr int D = 512, HQ = 32, HKV = 8, SK = 65536, DH = 128, W = 4096, F = 1792;

__device__ float block_sum(float v, float* red) {
  for (int o = 32; o; o >>= 1) v += __shfl_xor(v, o);
  __syncthreads();
  if ((threadIdx.x & 63) == 0) red[threadIdx.x >> 6] = v;
  __syncthreads();
  float s = 0;
  for (int i = 0; i < (int)(blockDim.x >> 6); ++i) s += red[i];
  return s;
}
__device__ void ln512(float* v, float* red) {
  int t = threadIdx.x;
  float a = v[t], b = v[t + 256];
  float m = block_sum(a + b, red) * (1.f / 512);
  float da = a - m, db = b - m;
  float var = block_sum(da * da + db * db, red) * (1.f / 512);
  float mb = rbf(m), vb = rbf(var);
  float sd = rbf(sqrtf(rbf(vb + 1e-5f)));
  __syncthreads();
  v[t] = rbf(rbf(a - mb) / sd);
  v[t + 256] = rbf(rbf(b - mb) / sd);
  __syncthreads();
}

__global__ void __launch_bounds__(256) k1(const u16* x, const u16* wq, const u16* w1, const u16* b1, float* q, float* h) {
  __shared__ float xs[512]; __shared__ float red[8]; __shared__ float part[128][17];
  int t = threadIdx.x;
  xs[t] = bf2f(x[t]); xs[t + 256] = bf2f(x[t + 256]);
  __syncthreads();
  ln512(xs, red);
  bool isq = blockIdx.x < 256;
  if (!isq) ln512(xs, red);
  const u16* wm = isq ? wq : w1; int ld = isq ? 4096 : F; int c0 = (isq ? blockIdx.x : blockIdx.x - 256) * 16;
  float acc[8] = {0};
  int half = t & 1, rg = t >> 1;
  for (int r = 0; r < 4; ++r) {
    int row = rg + 128 * r;
    u32x4 wv = *(const u32x4*)(wm + (size_t)row * ld + c0 + half * 8);
    float xv = xs[row];
    for (int k = 0; k < 4; ++k) { acc[2 * k] += xv * lo(wv[k]); acc[2 * k + 1] += xv * hi(wv[k]); }
  }
  for (int k = 0; k < 8; ++k) part[rg][half * 8 + k] = acc[k];
  __syncthreads();
  if (t < 16) {
    float s = 0; for (int i = 0; i < 128; ++i) s += part[i][t];
    if (isq) q[c0 + t] = rbf(s);
    else {
      float p = rbf(rbf(s) + bf2f(b1[c0 + t]));
      float u = 0.7978845608028654f * (p + 0.044715f * p * p * p);
      h[c0 + t] = rbf(0.5f * p * (1.f + tanhf(u)));
    }
  }
}

__global__ void __launch_bounds__(256) k2(const u16* kc, const u16* vc, const float* q, float* po, float* pml) {
  __shared__ float qs[512]; __shared__ float sc[4][128]; __shared__ float red[4][2]; __shared__ float oacc[8][4][128];
  int t = threadIdx.x, g = blockIdx.x & 7, s = blockIdx.x >> 3;
  qs[t] = q[g * 512 + t]; qs[t + 256] = q[g * 512 + t + 256];
  __syncthreads();
  const u16* kb = kc + ((size_t)g * SK + (SK - W) + s * 128) * DH;
  const u16* vb = vc + ((size_t)g * SK + (SK - W) + s * 128) * DH;
  {
    int key = t >> 1, half = t & 1;
    float acc[4] = {0, 0, 0, 0};
    for (int c = 0; c < 8; ++c) {
      u32x4 kv = *(const u32x4*)(kb + key * DH + half * 64 + c * 8);
      for (int k = 0; k < 4; ++k)
        for (int hh = 0; hh < 4; ++hh)
          acc[hh] += qs[hh * 128 + half * 64 + c * 8 + 2 * k] * lo(kv[k]) + qs[hh * 128 + half * 64 + c * 8 + 2 * k + 1] * hi(kv[k]);
    }
    for (int hh = 0; hh < 4; ++hh) { float v = acc[hh] + __shfl_xor(acc[hh], 1); if (!half) sc[hh][key] = rbf(rbf(v) * 0.08838834764831845f); }
  }
  __syncthreads();
  {
    int w = t >> 6, l = t & 63;
    float a = sc[w][l], b = sc[w][l + 64];
    float m = fmaxf(a, b); for (int o = 32; o; o >>= 1) m = fmaxf(m, __shfl_xor(m, o));
    a = __expf(a - m); b = __expf(b - m);
    float su = a + b; for (int o = 32; o; o >>= 1) su += __shfl_xor(su, o);
    sc[w][l] = a; sc[w][l + 64] = b;
    if (l == 0) { pml[((g * 4 + w) * 32 + s) * 2] = m; pml[((g * 4 + w) * 32 + s) * 2 + 1] = su; }
  }
  __syncthreads();
  {
    int c = t & 15, kg = t >> 4;
    float acc[4][8]; for (int i = 0; i < 4; ++i) for (int j = 0; j < 8; ++j) acc[i][j] = 0;
    for (int r = 0; r < 8; ++r) {
      int key = kg + 16 * r;
      u32x4 vv = *(const u32x4*)(vb + key * DH + c * 8);
      for (int hh = 0; hh < 4; ++hh) { float p = sc[hh][key];
        for (int k = 0; k < 4; ++k) { acc[hh][2 * k] += p * lo(vv[k]); acc[hh][2 * k + 1] += p * hi(vv[k]); } }
    }
    for (int hh = 0; hh < 4; ++hh) for (int j = 0; j < 8; ++j) { float v = acc[hh][j]; v += __shfl_xor(v, 16); v += __shfl_xor(v, 32); acc[hh][j] = v; }
    if ((t & 63) < 16) for (int hh = 0; hh < 4; ++hh) for (int j = 0; j < 8; ++j) oacc[t >> 6][hh][c * 8 + j] = acc[hh][j];
  }
  __syncthreads();
  for (int i = t; i < 512; i += 256) { int hh = i >> 7, d = i & 127;
    float v = oacc[0][hh][d] + oacc[1][hh][d] + oacc[2][hh][d] + oacc[3][hh][d];
    po[((size_t)(g * 4 + hh) * 32 + s) * 128 + d] = v; }
}

__global__ void __launch_bounds__(256) k3(const float* po, const float* pml, const u16* wo, const float* h, const u16* w2, float* sp, float* mp) {
  __shared__ float os[128]; __shared__ float part[32][65];
  int t = threadIdx.x;
  if (blockIdx.x < 256) {
    int hd = blockIdx.x >> 3, cc = blockIdx.x & 7;
    if (t < 128) {
      float M = -INFINITY; for (int s = 0; s < 32; ++s) M = fmaxf(M, pml[(hd * 32 + s) * 2]);
      float L = 0, o = 0;
      for (int s = 0; s < 32; ++s) { float w = __expf(pml[(hd * 32 + s) * 2] - M); L += w * pml[(hd * 32 + s) * 2 + 1]; o += w * po[((size_t)hd * 32 + s) * 128 + t]; }
      os[t] = rbf(o / L);
    }
    __syncthreads();
    int c = t & 7, rg = t >> 3;
    float acc[8] = {0};
    for (int r = 0; r < 4; ++r) { int row = rg + 32 * r;
      u32x4 wv = *(const u32x4*)(wo + (size_t)(hd * 128 + row) * D + cc * 64 + c * 8);
      float xv = os[row];
      for (int k = 0; k < 4; ++k) { acc[2 * k] += xv * lo(wv[k]); acc[2 * k + 1] += xv * hi(wv[k]); } }
    for (int k = 0; k < 8; ++k) part[rg][c * 8 + k] = acc[k];
    __syncthreads();
    if (t < 64) { float s = 0; for (int i = 0; i < 32; ++i) s += part[i][t]; sp[hd * 512 + cc * 64 + t] = s; }
  } else {
    int b = blockIdx.x - 256;
    int rc = b >> 3, cc = b & 7;
    int c = t & 7, rg = t >> 3;
    float acc[8] = {0};
    for (int r = 0; r < 2; ++r) { int row = rc * 64 + rg + 32 * r;
      u32x4 wv = *(const u32x4*)(w2 + (size_t)row * D + cc * 64 + c * 8);
      float xv = h[row];
      for (int k = 0; k < 4; ++k) { acc[2 * k] += xv * lo(wv[k]); acc[2 * k + 1] += xv * hi(wv[k]); } }
    for (int k = 0; k < 8; ++k) part[rg][c * 8 + k] = acc[k];
    __syncthreads();
    if (t < 64) { float s = 0; for (int i = 0; i < 32; ++i) s += part[i][t]; mp[rc * 512 + cc * 64 + t] = s; }
  }
}

__global__ void __launch_bounds__(512) k4(const u16* x, const float* sp, const float* mp, const u16* b2, u16* out) {
  int t = threadIdx.x;
  float s = 0; for (int i = 0; i < 32; ++i) s += sp[i * 512 + t];
  float m = 0; for (int i = 0; i < 28; ++i) m += mp[i * 512 + t];
  s = rbf(s);
  float B = rbf(rbf(m) + bf2f(b2[t]));
  float g = rbf(B / (1.f + __expf(-B)));
  out[t] = f2bf(bf2f(x[t]) + rbf(s * g));
}

extern "C" void kernel_launch(void* const* d_in, const int* in_sizes, int n_in, void* d_out, int out_size, void* d_ws, size_t ws_size, hipStream_t stream) {
  const u16* x = (const u16*)d_in[0]; const u16* kc = (const u16*)d_in[1]; const u16* vc = (const u16*)d_in[2];
  const u16* wq = (const u16*)d_in[3]; const u16* wo = (const u16*)d_in[4];
  const u16* w1 = (const u16*)d_in[6]; const u16* b1 = (const u16*)d_in[7]; const u16* w2 = (const u16*)d_in[8]; const u16* b2 = (const u16*)d_in[9];
  float* ws = (float*)d_ws;
  float* q = ws; float* h = ws + 4096; float* po = ws + 8192; float* pml = po + 32 * 32 * 128; float* sp = pml + 32 * 32 * 2; float* mp = sp + 32 * 512;
  k1<<<256 + 112, 256, 0, stream>>>(x, wq, w1, b1, q, h);
  k2<<<256, 256, 0, stream>>>(kc, vc, q, po, pml);
  k3<<<256 + 224, 256, 0, stream>>>(po, pml, wo, h, w2, sp, mp);
  k4<<<1, 512, 0, stream>>>(x, sp, mp, b2, (u16*)d_out);
}
```

```cpp
#include <hip/hip_runtime.h>
#include <stdint.h>

typedef unsigned short u16;
typedef unsigned u32;
typedef u32 u32x4 __attribute__((ext_vector_type(4)));
typedef u32 u32x2 __attribute__((ext_vector_type(2)));
typedef float f32x4 __attribute__((ext_vector_type(4)));
typedef float f32x2 __attribute__((ext_vector_type(2)));
typedef short s16x4 __attribute__((ext_vector_type(4)));
typedef __bf16 bf16x8 __attribute__((ext_vector_type(8)));
typedef __bf16 bf16x2 __attribute__((ext_vector_type(2)));
typedef __amdgpu_buffer_rsrc_t rsrc_t;

constexpr int SK = 65536, WIN = 4096, FF = 1792;
constexpr int NWG = 256, NT = 256;
constexpr int NM = 112;
constexpr int NC = 64;
constexpr u32 WS_EPOCH = 0;
constexpr u32 WS_QP = 4096;
constexpr u32 WS_ST = WS_QP + 8 * 4 * 512 * 8;
constexpr u32 WS_OP = WS_ST + 32 * 32 * 16;
constexpr u32 WS_HP = WS_OP + 256 * 32 * 128;
constexpr u32 WS_SP = WS_HP + NM * 32 * 128;
constexpr u32 WS_BP = WS_SP + NC * 256 * 64;
constexpr u32 WS_END = WS_BP + NC * NM * 64;
constexpr u32 OOB = 0x7ffffff0u;

constexpr int AUX_NT = 2, AUX_SC1 = 16;
constexpr int SPIN_CAP = 1 << 14;

#define KEEP(x) asm volatile("" :: "v"(x))
#define FENCE() __builtin_amdgcn_sched_barrier(0)
#define MEMCLOB() asm volatile("" ::: "memory")

#define STAMP(k) do { } while (0)

__device__ __forceinline__ rsrc_t mk(const void* p, u32 bytes) { return __builtin_amdgcn_make_buffer_rsrc((void*)p, 0, bytes, 0x00020000); }
__device__ __forceinline__ u32x4 ldnt(rsrc_t r, u32 voff, u32 soff) { return __builtin_amdgcn_raw_buffer_load_b128(r, voff, soff, AUX_NT); }
__device__ __forceinline__ u32x4 ldsc(rsrc_t r, u32 voff) { return __builtin_amdgcn_raw_buffer_load_b128(r, voff, 0, AUX_SC1); }
__device__ __forceinline__ void stsc(rsrc_t r, u32 voff, u32x4 v) { __builtin_amdgcn_raw_buffer_store_b128(v, r, voff, 0, AUX_SC1); }
__device__ __forceinline__ float f_of(u32 u) { return __builtin_bit_cast(float, u); }
__device__ __forceinline__ u32 u_of(float f) { return __builtin_bit_cast(u32, f); }
__device__ __forceinline__ float lo16(u32 w) { return f_of(w << 16); }
__device__ __forceinline__ float hi16(u32 w) { return f_of(w & 0xffff0000u); }
__device__ __forceinline__ u32 pk(float lo, float hi) {
  bf16x2 r = {(__bf16)lo, (__bf16)hi};
  return __builtin_bit_cast(u32, r);
}
__device__ __forceinline__ float rbf(float f) { return lo16(pk(f, f)); }
template <int CTRL> __device__ __forceinline__ float dpp(float x) {
  return f_of(__builtin_amdgcn_mov_dpp(u_of(x), CTRL, 0xf, 0xf, true));
}
constexpr int XOR1 = 0xB1, XOR2 = 0x4E, HMIR = 0x141, MIR = 0x140, ROR4 = 0x124, ROR8 = 0x128;
__device__ __forceinline__ float x16_sum(float x) { auto s = __builtin_amdgcn_permlane16_swap(u_of(x), u_of(x), false, false); return f_of(s[0]) + f_of(s[1]); }
__device__ __forceinline__ float x32_sum(float x) { auto s = __builtin_amdgcn_permlane32_swap(u_of(x), u_of(x), false, false); return f_of(s[0]) + f_of(s[1]); }
__device__ __forceinline__ float x16_max(float x) { auto s = __builtin_amdgcn_permlane16_swap(u_of(x), u_of(x), false, false); return fmaxf(f_of(s[0]), f_of(s[1])); }
__device__ __forceinline__ float x32_max(float x) { auto s = __builtin_amdgcn_permlane32_swap(u_of(x), u_of(x), false, false); return fmaxf(f_of(s[0]), f_of(s[1])); }
__device__ __forceinline__ float wave_sum(float x) {
  x += dpp<XOR1>(x); x += dpp<XOR2>(x); x += dpp<HMIR>(x); x += dpp<MIR>(x);
  return x32_sum(x16_sum(x));
}
__device__ __forceinline__ float dot2(u32 a, u32 b, float c) {
  return __builtin_amdgcn_fdot2_f32_bf16(__builtin_bit_cast(bf16x2, a), __builtin_bit_cast(bf16x2, b), c, false);
}
__device__ __forceinline__ u32 plo(u32 a, u32 b) { return __builtin_amdgcn_perm(b, a, 0x05040100u); }
__device__ __forceinline__ u32 phi(u32 a, u32 b) { return __builtin_amdgcn_perm(b, a, 0x07060302u); }
__device__ __forceinline__ f32x4 mfma32(u32x4 a, u32x4 b, f32x4 c) {
  return __builtin_amdgcn_mfma_f32_16x16x32_bf16(__builtin_bit_cast(bf16x8, a), __builtin_bit_cast(bf16x8, b), c, 0, 0, 0);
}
__device__ __forceinline__ f32x4 mfma16(u32x2 a, u32x2 b, f32x4 c) {
  return __builtin_amdgcn_mfma_f32_16x16x16bf16_1k(__builtin_bit_cast(s16x4, a), __builtin_bit_cast(s16x4, b), c, 0, 0, 0);
}
__device__ __forceinline__ void bar() {
  asm volatile("s_waitcnt lgkmcnt(0)\n\ts_barrier" ::: "memory");
}
__device__ __forceinline__ void ln8(float (&v)[8]) {
  float s = 0;
#pragma unroll
  for (int i = 0; i < 8; ++i) s += v[i];
  const float m = wave_sum(s) * (1.f / 512);
  float q = 0;
#pragma unroll
  for (int i = 0; i < 8; ++i) { const float d = v[i] - m; q = fmaf(d, d, q); }
  const float var = wave_sum(q) * (1.f / 512);
  const float mb = rbf(m);
  const float sd = rbf(__builtin_sqrtf(rbf(rbf(var) + 1e-5f)));
  const float inv = 1.f / sd;
#pragma unroll
  for (int i = 0; i < 8; ++i) v[i] = rbf(rbf(v[i] - mb) * inv);
}
__device__ __forceinline__ f32x2 slab_gemv(const u32x4 (&wv)[4], u32x2 a, int jj) {
  f32x4 d[8];
#pragma unroll
  for (int e = 0; e < 8; ++e) {
    const int dw = e >> 1;
    u32x2 b;
    if (e & 1) { b.x = phi(wv[0][dw], wv[1][dw]); b.y = phi(wv[2][dw], wv[3][dw]); }
    else       { b.x = plo(wv[0][dw], wv[1][dw]); b.y = plo(wv[2][dw], wv[3][dw]); }
    d[e] = mfma16(a, b, f32x4{0.f, 0.f, 0.f, 0.f});
  }
  const bool b0 = jj & 1, b1 = jj & 2;
  const float e0 = b1 ? (b0 ? d[6][0] : d[4][0]) : (b0 ? d[2][0] : d[0][0]);
  const float e1 = b1 ? (b0 ? d[7][0] : d[5][0]) : (b0 ? d[3][0] : d[1][0]);
  return f32x2{e0, e1};
}
__device__ __forceinline__ bool tags_ok(u32x4 v, u32 tag) { return v.y == tag && v.w == tag; }

struct __attribute__((aligned(16))) Lds {
  float xn[4][512];
  u32 yb[4][256];
  float qred[4][64];
  u32 qs[256];
  float mx[4][4];
  float ls[4][4];
  float om[4][4][128];
  u32 av[8];
  u32 hv[8];
  float fin[4][2][8];
};

__global__ void __launch_bounds__(NT, 1) fused_decode(const u16* __restrict__ x, const u16* __restrict__ kc, const u16* __restrict__ vc,
                                                      const u16* __restrict__ wq, const u16* __restrict__ wo, const u16* __restrict__ w1,
                                                      const u16* __restrict__ b1, const u16* __restrict__ w2, const u16* __restrict__ b2,
                                                      u16* __restrict__ out, unsigned char* __restrict__ ws) {
  __shared__ Lds L;
  STAMP(0);
  const int t = threadIdx.x, l = t & 63, w = __builtin_amdgcn_readfirstlane(t >> 6);
  const int b = blockIdx.x, g = b & 7, s = b >> 3;
  const int c16 = l & 15, j4 = l >> 4;

  const rsrc_t rws = mk(ws, WS_END);
  const u32 epoch = __builtin_amdgcn_raw_buffer_load_b32(rws, WS_EPOCH, 0, AUX_SC1);
  const u32x4 xr = __builtin_amdgcn_raw_buffer_load_b128(mk(x, 1024), l * 16, 0, 0);
  const rsrc_t rq = mk(wq, 512 * 4096 * 2);
  u32x4 wqv[4];
  {
    const u32 rowb = (s & 3) * 128 + 32 * w, colb = g * 512 + (s >> 2) * 64;
    const u32 voff = (l >> 3) * 8192 + (l & 7) * 16;
    const u32 soff = rowb * 8192 + colb * 2;
#pragma unroll
    for (int i = 0; i < 4; ++i) wqv[i] = ldnt(rq, voff, soff + i * 8 * 8192);
  }
  const size_t kvoff = ((size_t)g * SK + (SK - WIN) + s * 128 + 32 * w) * 256;
  const rsrc_t rk = mk((const char*)kc + kvoff, 8192), rv = mk((const char*)vc + kvoff, 8192);
  const u32 kvo = (c16 >> 1) * 256 + (c16 & 1) * 64 + j4 * 16;
  const u32 vvo = j4 * 512 + c16 * 16;
  u32x4 K[8], V[8];
#define LDK(i) K[i] = ldnt(rk, kvo, ((i) >> 1) * 2048 + ((i) & 1) * 128)
#define LDV(i) V[i] = ldnt(rv, vvo, ((i) >> 1) * 2048 + ((i) & 1) * 256)
  LDK(0); LDK(1);
  const bool isM1 = b < NM, isM2 = b >= NM && b < 2 * NM, isFin = b < NC;
  const int jm = isM1 ? b : b - NM;
  const int fbase = (jm >> 4) * 256 + ((jm >> 3) & 1) * 128 + (jm & 1) * 64 + ((jm >> 1) & 3) * 2;
  const int fcol = (b >> 4) * 128 + 8 * (4 * (b & 3) + (l & 3)) + 2 * ((b >> 2) & 3);
  u32 b1v = 0, b2v = 0, xfv = 0;
  if (isM2) b1v = *(const u32*)(b1 + fbase + 8 * (2 * w + (l & 1)));
  if (isFin) { b2v = *(const u32*)(b2 + fcol); xfv = *(const u32*)(x + fcol); }
  FENCE();
  STAMP(1);
  float xv[8];
#pragma unroll
  for (int i = 0; i < 4; ++i) { xv[2 * i] = lo16(xr[i]); xv[2 * i + 1] = hi16(xr[i]); }
  ln8(xv);
  *(f32x4*)&L.xn[w][8 * l] = f32x4{xv[0], xv[1], xv[2], xv[3]};
  *(f32x4*)&L.xn[w][8 * l + 4] = f32x4{xv[4], xv[5], xv[6], xv[7]};
  if (isM1) {
    ln8(xv);
    *(u32x4*)&L.yb[w][4 * l] = u32x4{pk(xv[0], xv[1]), pk(xv[2], xv[3]), pk(xv[4], xv[5]), pk(xv[6], xv[7])};
  }
  const u32 tag = (epoch + 1u) * 0x9E3779B1u ^ 0x5bd1e995u;
  float xrow[4];
  {
    const int rowb = (s & 3) * 128 + 32 * w + (l >> 3);
#pragma unroll
    for (int i = 0; i < 4; ++i) xrow[i] = L.xn[w][rowb + 8 * i];
  }
  FENCE();
  KEEP(wqv[3]);
  FENCE();
  LDK(2); LDK(3); LDK(4); LDK(5);
  FENCE();
  {
    const u32 x01 = pk(xrow[0], xrow[1]), x23 = pk(xrow[2], xrow[3]);
    float acc[8];
#pragma unroll
    for (int e = 0; e < 8; ++e) {
      const int dw = e >> 1;
      const u32 p01 = (e & 1) ? phi(wqv[0][dw], wqv[1][dw]) : plo(wqv[0][dw], wqv[1][dw]);
      const u32 p23 = (e & 1) ? phi(wqv[2][dw], wqv[3][dw]) : plo(wqv[2][dw], wqv[3][dw]);
      acc[e] = dot2(p23, x23, dot2(p01, x01, 0.f));
    }
    float v4[4], v2[2];
#pragma unroll
    for (int e = 0; e < 4; ++e) { auto r = __builtin_amdgcn_permlane32_swap(u_of(acc[e]), u_of(acc[e + 4]), false, false); v4[e] = f_of(r[0]) + f_of(r[1]); }
#pragma unroll
    for (int e = 0; e < 2; ++e) { auto r = __builtin_amdgcn_permlane16_swap(u_of(v4[e]), u_of(v4[e + 2]), false, false); v2[e] = f_of(r[0]) + f_of(r[1]); }
    const float t0 = v2[0] + dpp<ROR8>(v2[0]), t1 = v2[1] + dpp<ROR8>(v2[1]);
    const float res = (l & 8) ? t1 : t0;
    const int e = ((l >> 5) & 1) * 4 + ((l >> 4) & 1) * 2 + ((l >> 3) & 1);
    L.qred[w][8 * (l & 7) + e] = res;
  }
  bar();
  if (t < 32) {
    f32x2 a = *(f32x2*)&L.qred[0][2 * t], bb = *(f32x2*)&L.qred[1][2 * t], c = *(f32x2*)&L.qred[2][2 * t], d = *(f32x2*)&L.qred[3][2 * t];
    const float v0 = (a.x + bb.x) + (c.x + d.x), v1 = (a.y + bb.y) + (c.y + d.y);
    stsc(rws, WS_QP + ((g * 4 + (s & 3)) * 512 + (s >> 2) * 64 + 2 * t) * 8, u32x4{u_of(v0), tag, u_of(v1), tag});
  }
  STAMP(2);
  FENCE();
  KEEP(K[1]); FENCE(); LDK(6); LDK(7); FENCE();
  KEEP(K[3]); FENCE(); LDV(0); LDV(1); FENCE();
  KEEP(K[5]); FENCE(); LDV(2); LDV(3); FENCE();
  u32x4 qg[4];
  const u32 qoff = WS_QP + (g * 4 * 512 + w * 128 + 2 * l) * 8;
#pragma unroll
  for (int i = 0; i < 4; ++i) qg[i] = ldsc(rws, qoff + i * 4096);
  FENCE();
  KEEP(K[7]); FENCE(); LDV(4); LDV(5); FENCE();
  {
    bool ok = tags_ok(qg[0], tag) && tags_ok(qg[1], tag) && tags_ok(qg[2], tag) && tags_ok(qg[3], tag);
    for (int spin = 0; !__all(ok) && spin < SPIN_CAP; ++spin) {
      MEMCLOB();
#pragma unroll
      for (int i = 0; i < 4; ++i) qg[i] = ldsc(rws, qoff + i * 4096);
      ok = tags_ok(qg[0], tag) && tags_ok(qg[1], tag) && tags_ok(qg[2], tag) && tags_ok(qg[3], tag);
    }
  }
  FENCE(); LDV(6); LDV(7); FENCE();
  STAMP(3);
  {
    const float q0 = (f_of(qg[0].x) + f_of(qg[1].x)) + (f_of(qg[2].x) + f_of(qg[3].x));
    const float q1 = (f_of(qg[0].z) + f_of(qg[1].z)) + (f_of(qg[2].z) + f_of(qg[3].z));
    L.qs[w * 64 + l] = pk(q0, q1);
  }
  bar();
  float sc[8];
  {
    const u32x4 qf0 = *(const u32x4*)&L.qs[(c16 >> 2) * 64 + (c16 & 1) * 16 + j4 * 4];
    const u32x4 qf1 = *(const u32x4*)&L.qs[(c16 >> 2) * 64 + 32 + (c16 & 1) * 16 + j4 * 4];
#pragma unroll
    for (int bb = 0; bb < 4; ++bb) {
      f32x4 d = mfma32(K[2 * bb], qf0, f32x4{0.f, 0.f, 0.f, 0.f});
      d = mfma32(K[2 * bb + 1], qf1, d);
      const float u0 = (l & 1) ? d[1] : d[0], u1 = (l & 1) ? d[3] : d[2];
      sc[2 * bb] = rbf(rbf(u0 + dpp<XOR1>(u0)) * 0.08838834764831845f);
      sc[2 * bb + 1] = rbf(rbf(u1 + dpp<XOR1>(u1)) * 0.08838834764831845f);
    }
  }
  float mw = fmaxf(fmaxf(fmaxf(sc[0], sc[1]), fmaxf(sc[2], sc[3])), fmaxf(fmaxf(sc[4], sc[5]), fmaxf(sc[6], sc[7])));
  mw = x32_max(x16_max(mw));
  if (l < 16 && (l & 3) == 0) L.mx[l >> 2][w] = mw;
  bar();
  const f32x4 m4 = *(const f32x4*)&L.mx[c16 >> 2][0];
  const float M = fmaxf(fmaxf(m4[0], m4[1]), fmaxf(m4[2], m4[3]));
  float lsum = 0;
  u32x4 P;
#pragma unroll
  for (int bb = 0; bb < 4; ++bb) {
    const float p0 = __builtin_amdgcn_exp2f((sc[2 * bb] - M) * 1.4426950408889634f), p1 = __builtin_amdgcn_exp2f((sc[2 * bb + 1] - M) * 1.4426950408889634f);
    lsum += p0 + p1;
    P[bb] = pk(p0, p1);
  }
  lsum = x32_sum(x16_sum(lsum));
  if (l < 16 && (l & 3) == 0) L.ls[l >> 2][w] = lsum;
  STAMP(4);
  u32x4 wm[4], wov[4];
  FENCE();
  KEEP(V[3]);
  FENCE();
  if (isM1) {
    const rsrc_t r1 = mk(w1, 512 * FF * 2);
    const u32 rowb = (jm / 7) * 32 + 16 * (w >> 1), colb = (jm % 7) * 256 + 128 * (w & 1);
    const u32 voff = j4 * 4 * (FF * 2) + c16 * 16;
#pragma unroll
    for (int i = 0; i < 4; ++i) wm[i] = ldnt(r1, voff, (rowb + i) * (FF * 2) + colb * 2);
  }
  FENCE();
  KEEP(V[7]);
  FENCE();
  {
    const rsrc_t ro = mk(wo, 4096 * 512 * 2);
    const u32 voff = j4 * 4096 + c16 * 16;
#pragma unroll
    for (int i = 0; i < 4; ++i) wov[i] = ldnt(ro, voff, (g * 512 + s * 16 + i) * 1024 + w * 256);
  }
  if (isM2) {
    const rsrc_t r2 = mk(w2, 512 * FF * 2);
    const u32 voff = j4 * 16 * 1024 + c16 * 16;
#pragma unroll
    for (int i = 0; i < 4; ++i) wm[i] = ldnt(r2, voff, (fbase + 8 * (i >> 1) + (i & 1)) * 1024 + w * 256);
  }
  FENCE();
  STAMP(5);
  {
    f32x4 o[8];
#pragma unroll
    for (int e = 0; e < 8; ++e) {
      const int dw = e >> 1;
      u32x4 bv;
#pragma unroll
      for (int bb = 0; bb < 4; ++bb) bv[bb] = (e & 1) ? phi(V[2 * bb][dw], V[2 * bb + 1][dw]) : plo(V[2 * bb][dw], V[2 * bb + 1][dw]);
      o[e] = mfma32(P, bv, f32x4{0.f, 0.f, 0.f, 0.f});
    }
    *(f32x4*)&L.om[w][j4][c16 * 4] = f32x4{o[0][0], o[1][0], o[2][0], o[3][0]};
    *(f32x4*)&L.om[w][j4][64 + c16 * 4] = f32x4{o[4][0], o[5][0], o[6][0], o[7][0]};
  }
  bar();
  {
    const int d0 = 2 * l, idx = ((d0 >> 2) & 1) * 64 + (d0 >> 3) * 4 + (d0 & 3);
    const f32x2 a = *(f32x2*)&L.om[0][w][idx], bb = *(f32x2*)&L.om[1][w][idx], c = *(f32x2*)&L.om[2][w][idx], d = *(f32x2*)&L.om[3][w][idx];
    const float v0 = (a.x + bb.x) + (c.x + d.x), v1 = (a.y + bb.y) + (c.y + d.y);
    stsc(rws, WS_OP + (((g * 32 + w * 8 + (d0 >> 4)) * 32 + s) * 16 + (d0 & 15)) * 8, u32x4{u_of(v0), tag, u_of(v1), tag});
    if (l == 0) {
      const f32x4 l4 = *(const f32x4*)&L.ls[w][0], mm = *(const f32x4*)&L.mx[w][0];
      const float Mh = fmaxf(fmaxf(mm[0], mm[1]), fmaxf(mm[2], mm[3]));
      stsc(rws, WS_ST + ((g * 4 + w) * 32 + s) * 16, u32x4{u_of(Mh), tag, u_of((l4[0] + l4[1]) + (l4[2] + l4[3])), tag});
    }
  }
  STAMP(6);
  if (isM1) {
    const u32x2 a = *(const u32x2*)&L.yb[w][((jm / 7) * 32 + 16 * (w >> 1) + 4 * j4) >> 1];
    const f32x2 r = slab_gemv(wm, a, j4);
    const int cons = (((jm % 7) * 2 + (w & 1)) * 4 + j4) * 2 + (c16 >> 3), part = (jm / 7) * 2 + (w >> 1);
    stsc(rws, WS_HP + ((cons * 32 + part) * 16 + (c16 & 7) * 2) * 8, u32x4{u_of(r.x), tag, u_of(r.y), tag});
  }
  STAMP(7);
  {
    const u32 ooff = WS_OP + (((g * 32 + s) * 32 + (l >> 1)) * 16 + 4 * w + 2 * (l & 1)) * 8;
    const u32 soff = WS_ST + ((g * 4 + (s >> 3)) * 32 + (l >> 1)) * 16;
    u32x4 ov = ldsc(rws, ooff), sv = ldsc(rws, soff);
    bool ok = tags_ok(ov, tag) && tags_ok(sv, tag);
    for (int spin = 0; !__all(ok) && spin < SPIN_CAP; ++spin) {
      MEMCLOB();
      const u32x4 o2 = ldsc(rws, ok ? OOB : ooff), s2 = ldsc(rws, ok ? OOB : soff);
      if (!ok) { ov = o2; sv = s2; }
      ok = tags_ok(ov, tag) && tags_ok(sv, tag);
    }
    STAMP(8);
    const float mp = f_of(sv.x), lp = f_of(sv.z);
    float Mx = mp;
    Mx = fmaxf(Mx, dpp<XOR2>(Mx)); Mx = fmaxf(Mx, dpp<ROR4>(Mx)); Mx = fmaxf(Mx, dpp<ROR8>(Mx)); Mx = x32_max(x16_max(Mx));
    const float wp = __builtin_amdgcn_exp2f((mp - Mx) * 1.4426950408889634f);
    float Ls = wp * lp, o0 = wp * f_of(ov.x), o1 = wp * f_of(ov.z);
    Ls += dpp<XOR2>(Ls); o0 += dpp<XOR2>(o0); o1 += dpp<XOR2>(o1);
    Ls += dpp<ROR4>(Ls); o0 += dpp<ROR4>(o0); o1 += dpp<ROR4>(o1);
    Ls += dpp<ROR8>(Ls); o0 += dpp<ROR8>(o0); o1 += dpp<ROR8>(o1);
    Ls = x32_sum(x16_sum(Ls)); o0 = x32_sum(x16_sum(o0)); o1 = x32_sum(x16_sum(o1));
    const float inv = 1.f / Ls;
    if (l < 2) L.av[2 * w + l] = pk(o0 * inv, o1 * inv);
  }
  bar();
  {
    const u32x2 a = *(const u32x2*)&L.av[2 * j4];
    const f32x2 r = slab_gemv(wov, a, j4);
    stsc(rws, WS_SP + (((w * 16 + (l >> 2)) * 256 + b) * 8 + (l & 3) * 2) * 8, u32x4{u_of(r.x), tag, u_of(r.y), tag});
  }
  STAMP(9);
  if (isM2) {
    const u32 hoff = WS_HP + ((jm * 32 + (l >> 1)) * 16 + 4 * w + 2 * (l & 1)) * 8;
    u32x4 hv = ldsc(rws, hoff);
    bool ok = tags_ok(hv, tag);
    for (int spin = 0; !__all(ok) && spin < SPIN_CAP; ++spin) {
      MEMCLOB();
      const u32x4 h2 = ldsc(rws, ok ? OOB : hoff);
      if (!ok) hv = h2;
      ok = tags_ok(hv, tag);
    }
    float h0 = f_of(hv.x), h1 = f_of(hv.z);
    h0 += dpp<XOR2>(h0); h1 += dpp<XOR2>(h1);
    h0 += dpp<ROR4>(h0); h1 += dpp<ROR4>(h1);
    h0 += dpp<ROR8>(h0); h1 += dpp<ROR8>(h1);
    h0 = x32_sum(x16_sum(h0)); h1 = x32_sum(x16_sum(h1));
    float hp[2] = {rbf(rbf(h0) + lo16(b1v)), rbf(rbf(h1) + hi16(b1v))};
#pragma unroll
    for (int i = 0; i < 2; ++i) {
      const float p = hp[i], u = 0.7978845608028654f * (p + 0.044715f * p * p * p);
      const float th = 1.f - 2.f / (__builtin_amdgcn_exp2f(u * 2.8853900817779268f) + 1.f);
      hp[i] = 0.5f * p * (1.f + th);
    }
    if (l < 2) L.hv[2 * w + l] = pk(hp[0], hp[1]);
    bar();
    const u32x2 a = *(const u32x2*)&L.hv[2 * j4];
    const f32x2 r = slab_gemv(wm, a, j4);
    stsc(rws, WS_BP + (((w * 16 + (l >> 2)) * NM + jm) * 8 + (l & 3) * 2) * 8, u32x4{u_of(r.x), tag, u_of(r.y), tag});
  }
  STAMP(10);
  if (isFin) {
    u32x4 sg[4], bg[2];
    u32 so[4], bo[2];
#pragma unroll
    for (int i = 0; i < 4; ++i) so[i] = WS_SP + ((b * 256 + 64 * w + 16 * i + (l >> 2)) * 8 + (l & 3) * 2) * 8;
#pragma unroll
    for (int i = 0; i < 2; ++i) { const int p = 32 * w + 16 * i + (l >> 2); bo[i] = p < NM ? WS_BP + ((b * NM + p) * 8 + (l & 3) * 2) * 8 : OOB; }
    bool ok[6];
#pragma unroll
    for (int i = 0; i < 4; ++i) sg[i] = ldsc(rws, so[i]);
#pragma unroll
    for (int i = 0; i < 2; ++i) bg[i] = ldsc(rws, bo[i]);
#pragma unroll
    for (int i = 0; i < 4; ++i) ok[i] = tags_ok(sg[i], tag);
#pragma unroll
    for (int i = 0; i < 2; ++i) ok[4 + i] = bo[i] == OOB || tags_ok(bg[i], tag);
    for (int spin = 0; !__all(ok[0] && ok[1] && ok[2] && ok[3] && ok[4] && ok[5]) && spin < SPIN_CAP; ++spin) {
      MEMCLOB();
      u32x4 n[6];
#pragma unroll
      for (int i = 0; i < 4; ++i) n[i] = ldsc(rws, ok[i] ? OOB : so[i]);
#pragma unroll
      for (int i = 0; i < 2; ++i) n[4 + i] = ldsc(rws, ok[4 + i] ? OOB : bo[i]);
#pragma unroll
      for (int i = 0; i < 4; ++i) if (!ok[i]) { sg[i] = n[i]; ok[i] = tags_ok(n[i], tag); }
#pragma unroll
      for (int i = 0; i < 2; ++i) if (!ok[4 + i]) { bg[i] = n[4 + i]; ok[4 + i] = tags_ok(n[4 + i], tag); }
    }
    STAMP(11);
    float r[4];
    r[0] = (f_of(sg[0].x) + f_of(sg[1].x)) + (f_of(sg[2].x) + f_of(sg[3].x));
    r[1] = (f_of(sg[0].z) + f_of(sg[1].z)) + (f_of(sg[2].z) + f_of(sg[3].z));
    r[2] = f_of(bg[0].x) + f_of(bg[1].x);
    r[3] = f_of(bg[0].z) + f_of(bg[1].z);
#pragma unroll
    for (int i = 0; i < 4; ++i) { float v = r[i]; v += dpp<ROR4>(v); v += dpp<ROR8>(v); r[i] = x32_sum(x16_sum(v)); }
    if (l < 4) { *(f32x2*)&L.fin[w][0][2 * l] = f32x2{r[0], r[1]}; *(f32x2*)&L.fin[w][1][2 * l] = f32x2{r[2], r[3]}; }
    bar();
    if (t < 4) {
      float o2[2];
#pragma unroll
      for (int i = 0; i < 2; ++i) {
        const float sv = rbf((L.fin[0][0][2 * t + i] + L.fin[1][0][2 * t + i]) + (L.fin[2][0][2 * t + i] + L.fin[3][0][2 * t + i]));
        const float mv = rbf((L.fin[0][1][2 * t + i] + L.fin[1][1][2 * t + i]) + (L.fin[2][1][2 * t + i] + L.fin[3][1][2 * t + i]));
        const float B = rbf(mv + (i ? hi16(b2v) : lo16(b2v)));
        const float gt = rbf(B / (1.f + __builtin_amdgcn_exp2f(-B * 1.4426950408889634f)));
        o2[i] = (i ? hi16(xfv) : lo16(xfv)) + rbf(sv * gt);
      }
      *(u32*)(out + fcol) = pk(o2[0], o2[1]);
      if (b == 0 && t == 0) __builtin_amdgcn_raw_buffer_store_b32(epoch + 1u, rws, WS_EPOCH, 0, AUX_SC1);
    }
  }
  STAMP(12);
}

extern "C" void kernel_launch(void* const* d_in, const int* in_sizes, int n_in, void* d_out, int out_size, void* d_ws, size_t ws_size, hipStream_t stream) {
  fused_decode<<<NWG, NT, 0, stream>>>((const u16*)d_in[0], (const u16*)d_in[1], (const u16*)d_in[2], (const u16*)d_in[3], (const u16*)d_in[4],
                                       (const u16*)d_in[6], (const u16*)d_in[7], (const u16*)d_in[8], (const u16*)d_in[9], (u16*)d_out, (unsigned char*)d_ws);
}
```

```cpp
#include <hip/hip_runtime.h>
#include <stdint.h>

typedef unsigned short u16;
typedef unsigned u32;
typedef unsigned long long u64;
typedef u32 u32x4 __attribute__((ext_vector_type(4)));
typedef u32 u32x2 __attribute__((ext_vector_type(2)));
typedef float f32x4 __attribute__((ext_vector_type(4)));
typedef float f32x2 __attribute__((ext_vector_type(2)));
typedef short s16x4 __attribute__((ext_vector_type(4)));
typedef __bf16 bf16x8 __attribute__((ext_vector_type(8)));
typedef __bf16 bf16x2 __attribute__((ext_vector_type(2)));
typedef __amdgpu_buffer_rsrc_t rsrc_t;
#define LAS __attribute__((address_space(3)))

constexpr int SK = 65536, WIN = 4096, FF = 1792;
constexpr int NWG = 256, NT = 640;
constexpr int NC = 64;
constexpr u32 WS_EPOCH = 0;
constexpr u32 WS_QP = 4096;
constexpr u32 WS_ST = WS_QP + 8 * 4 * 512 * 8;
constexpr u32 WS_OP = WS_ST + 256 * 32 * 64;
constexpr u32 WS_HH = WS_OP + 256 * 32 * 128;
constexpr u32 WS_HP = WS_HH + 224 * 32 * 64;
constexpr u32 WS_FH = WS_HP + 224 * 32 * 64;
constexpr u32 WS_SB = WS_FH + NC * 32 * 64;
constexpr u32 WS_END = WS_SB + NC * 256 * 128;
constexpr u32 OOB = 0x80000000u;
constexpr u32 O_WQ = 0, O_K = 16384, O_V = 49152, O_W1 = 81920, O_W2 = 90112, O_WO = 98304, O_S = 114688;
constexpr u32 O_XN = O_S, O_Y = O_XN + 1024, O_QS = O_Y + 1024, O_P = O_QS + 1024, O_MW = O_P + 1024, O_LW = O_MW + 64, O_AV = O_LW + 64,
              O_HV = O_AV + 32, O_FIN = O_HV + 64, O_FL = O_FIN + 256, LDS_BYTES = O_FL + 128;
enum { F_LANDED = 0, F_OOK = 2, F_HOK = 3, F_FOK = 4, F_Q = 12, F_P = 16, F_O = 20, F_F = 24 };
constexpr int N_WQ = 16, N_K = N_WQ + 32, N_V = N_K + 32, N_W1 = N_V + 8, N_W2 = N_W1 + 8, N_WO = N_W2 + 16;
#define WINDOW 24
#define WINDOW2 16
#define Q_AT (N_WQ + 24)
#define ST_AT (N_V - 4)
#define HH_AT N_V
#define CODE_BYTES 13312
constexpr int AUX_NT = 2, AUX_SC1 = 16;
constexpr int SPIN_CAP = 1 << 14;

#define KEEP(x) asm volatile("" :: "v"(x))
#define MEMCLOB() asm volatile("" ::: "memory")
#define STAMP(k) do { } while (0)

__device__ __forceinline__ rsrc_t mk(const void* p, u32 bytes) { return __builtin_amdgcn_make_buffer_rsrc((void*)p, 0, bytes, 0x00020000); }
__device__ __forceinline__ u32x4 ldsc(rsrc_t r, u32 voff) { return __builtin_amdgcn_raw_buffer_load_b128(r, voff, 0, AUX_SC1); }
__device__ __forceinline__ void stsc(rsrc_t r, u32 voff, u32x4 v) { __builtin_amdgcn_raw_buffer_store_b128(v, r, voff, 0, AUX_SC1); }
__device__ __forceinline__ void stsc2(rsrc_t r, u32 voff, u32x2 v) { __builtin_amdgcn_raw_buffer_store_b64(v, r, voff, 0, AUX_SC1); }
__device__ __forceinline__ float f_of(u32 u) { return __builtin_bit_cast(float, u); }
__device__ __forceinline__ u32 u_of(float f) { return __builtin_bit_cast(u32, f); }
__device__ __forceinline__ float lo16(u32 w) { return f_of(w << 16); }
__device__ __forceinline__ float hi16(u32 w) { return f_of(w & 0xffff0000u); }
__device__ __forceinline__ u32 pk(float lo, float hi) { bf16x2 r = {(__bf16)lo, (__bf16)hi}; return __builtin_bit_cast(u32, r); }
__device__ __forceinline__ float rbf(float f) { return lo16(pk(f, f)); }
template <int CTRL> __device__ __forceinline__ float dpp(float x) { return f_of(__builtin_amdgcn_mov_dpp(u_of(x), CTRL, 0xf, 0xf, true)); }
constexpr int XOR1 = 0xB1, XOR2 = 0x4E, HMIR = 0x141, MIR = 0x140, ROR4 = 0x124, ROR8 = 0x128;
__device__ __forceinline__ float x16_sum(float x) { auto s = __builtin_amdgcn_permlane16_swap(u_of(x), u_of(x), false, false); return f_of(s[0]) + f_of(s[1]); }
__device__ __forceinline__ float x32_sum(float x) { auto s = __builtin_amdgcn_permlane32_swap(u_of(x), u_of(x), false, false); return f_of(s[0]) + f_of(s[1]); }
__device__ __forceinline__ float x16_max(float x) { auto s = __builtin_amdgcn_permlane16_swap(u_of(x), u_of(x), false, false); return fmaxf(f_of(s[0]), f_of(s[1])); }
__device__ __forceinline__ float x32_max(float x) { auto s = __builtin_amdgcn_permlane32_swap(u_of(x), u_of(x), false, false); return fmaxf(f_of(s[0]), f_of(s[1])); }
__device__ __forceinline__ float row_sum(float x) { x += dpp<XOR1>(x); x += dpp<XOR2>(x); x += dpp<HMIR>(x); x += dpp<MIR>(x); return x; }
__device__ __forceinline__ float wave_sum(float x) { return x32_sum(x16_sum(row_sum(x))); }
__device__ __forceinline__ float par_sum(float x) { x += dpp<XOR2>(x); x += dpp<ROR4>(x); x += dpp<ROR8>(x); return x32_sum(x16_sum(x)); }
__device__ __forceinline__ float par_max(float x) { x = fmaxf(x, dpp<XOR2>(x)); x = fmaxf(x, dpp<ROR4>(x)); x = fmaxf(x, dpp<ROR8>(x)); return x32_max(x16_max(x)); }
__device__ __forceinline__ float ex2(float x) { return __builtin_amdgcn_exp2f(x); }
__device__ __forceinline__ float rcp(float x) { return __builtin_amdgcn_rcpf(x); }
__device__ __forceinline__ f32x4 mfma32(u32x4 a, u32x4 b, f32x4 c) {
  return __builtin_amdgcn_mfma_f32_16x16x32_bf16(__builtin_bit_cast(bf16x8, a), __builtin_bit_cast(bf16x8, b), c, 0, 0, 0);
}
__device__ __forceinline__ f32x4 mfma16(u32x2 a, u32x2 b, f32x4 c) {
  return __builtin_amdgcn_mfma_f32_16x16x16bf16_1k(__builtin_bit_cast(s16x4, a), __builtin_bit_cast(s16x4, b), c, 0, 0, 0);
}
typedef LAS unsigned char* lds_t;
template <class T> __device__ __forceinline__ T lds_ld(lds_t s, u32 off) { return *(const LAS T*)(s + off); }
template <class T> __device__ __forceinline__ void lds_st(lds_t s, u32 off, T v) { *(LAS T*)(s + off) = v; }
__device__ __forceinline__ u32x2 lds_tr(lds_t s, u32 off) { return __builtin_bit_cast(u32x2, __builtin_amdgcn_ds_read_tr16_b64_v4i16((LAS s16x4*)(s + off))); }
__device__ __forceinline__ void set_flag(lds_t s, int f, u32 v) { MEMCLOB(); *(volatile LAS u32*)(s + O_FL + 4 * f) = v; MEMCLOB(); }
__device__ __forceinline__ void wait_ge(lds_t s, int f, int need) {
  for (int spin = 0; spin < (1 << 22); ++spin) {
    const int v = __builtin_amdgcn_readfirstlane(*(volatile LAS int*)(s + O_FL + 4 * f));
    if (v >= need) break;
  }
  MEMCLOB();
}
__device__ __forceinline__ void wait4(lds_t s, int f) {
  for (int spin = 0; spin < (1 << 22); ++spin) {
    const u32x4 v = *(volatile LAS u32x4*)(s + O_FL + 4 * f);
    if (__builtin_amdgcn_readfirstlane(v.x & v.y & v.z & v.w)) break;
  }
  MEMCLOB();
}
__device__ __forceinline__ u32x4 ln8(u32x4 xr) {
  float v[8];
#pragma unroll
  for (int i = 0; i < 4; ++i) { v[2 * i] = lo16(xr[i]); v[2 * i + 1] = hi16(xr[i]); }
  const float s = ((v[0] + v[1]) + (v[2] + v[3])) + ((v[4] + v[5]) + (v[6] + v[7]));
  const float m = wave_sum(s) * (1.f / 512);
  float q0 = 0, q1 = 0;
#pragma unroll
  for (int i = 0; i < 4; ++i) { const float d0 = v[2 * i] - m, d1 = v[2 * i + 1] - m; q0 = fmaf(d0, d0, q0); q1 = fmaf(d1, d1, q1); }
  const float var = wave_sum(q0 + q1) * (1.f / 512);
  const float mb = rbf(m);
  const float inv = rcp(rbf(__builtin_amdgcn_sqrtf(rbf(rbf(var) + 1e-5f))));
  u32x4 r;
#pragma unroll
  for (int i = 0; i < 4; ++i) { const u32 d = pk(v[2 * i] - mb, v[2 * i + 1] - mb); r[i] = pk(lo16(d) * inv, hi16(d) * inv); }
  return r;
}
__device__ __forceinline__ u64 poll64(const unsigned char* p) {
  u64 r; const u64 z = 0;
  asm volatile("global_atomic_or_x2 %0, %1, %2, off sc0 sc1\n\ts_waitcnt vmcnt(0)" : "=&v"(r) : "v"(p), "v"(z) : "memory");
  return r;
}
__device__ __forceinline__ void dma4(rsrc_t r, lds_t s, u32 ldsoff, u32 v0, u32 v1, u32 v2, u32 v3, u32 soff) {
  __builtin_amdgcn_raw_ptr_buffer_load_lds(r, (LAS void*)(s + ldsoff), 16, v0, soff, 0, AUX_NT);
  __builtin_amdgcn_raw_ptr_buffer_load_lds(r, (LAS void*)(s + ldsoff), 16, v1, soff, 1024, AUX_NT);
  __builtin_amdgcn_raw_ptr_buffer_load_lds(r, (LAS void*)(s + ldsoff), 16, v2, soff, 2048, AUX_NT);
  __builtin_amdgcn_raw_ptr_buffer_load_lds(r, (LAS void*)(s + ldsoff), 16, v3, soff, 3072, AUX_NT);
}
template <int N> __device__ __forceinline__ void landed(u32 flag_addr, int v) {
  asm volatile("s_waitcnt vmcnt(%0)\n\tds_write_b32 %1, %2" :: "i"(N), "v"(flag_addr), "v"(v) : "memory");
}
__device__ __forceinline__ u32 msk(bool c) { return c ? 0u : OOB; }
__device__ __forceinline__ bool tok(u32x4 v, u32 tag) { return (v.y == tag) & (v.w == tag); }
__device__ __forceinline__ u32 mktag(u32 epoch) { return ((epoch + 1u) * 0x9E3779B1u) ^ 0x5bd1e995u; }

__global__ void __launch_bounds__(NT) fused_decode(const u16* __restrict__ x, const u16* __restrict__ kc, const u16* __restrict__ vc,
                                                   const u16* __restrict__ wq, const u16* __restrict__ wo, const u16* __restrict__ w1,
                                                   const u16* __restrict__ b1, const u16* __restrict__ w2, const u16* __restrict__ b2,
                                                   u16* __restrict__ out, unsigned char* __restrict__ ws) {
  __shared__ __attribute__((aligned(16))) unsigned char smem[LDS_BYTES];
  lds_t S = (lds_t)smem;
  STAMP(0);
  const int t = threadIdx.x, l = t & 63, wv = __builtin_amdgcn_readfirstlane(t >> 6);
  if (wv == 0) {
    if (l < 32) lds_st<u32>(S, O_FL + 4 * l, 0u);
    asm volatile("s_waitcnt lgkmcnt(0)" ::: "memory");
  }
  asm volatile("s_load_dword s94, %0, 0x0\n\ts_load_dword s95, %0, 0x40" :: "s"(__builtin_amdgcn_kernarg_segment_ptr()) : "s94", "s95", "memory");
  const rsrc_t rcode = mk((const void*)(__builtin_amdgcn_s_getpc() & ~255ull), CODE_BYTES);
  asm volatile("buffer_load_dwordx4 v[120:123], %0, %1, 0 offen\n\tbuffer_load_dwordx4 v[124:127], %2, %1, 0 offen"
               :: "v"(l * 16 + (wv + 1) * 1024), "s"(rcode), "v"(l * 16 + (wv + 11) * 1024)
               : "v120", "v121", "v122", "v123", "v124", "v125", "v126", "v127", "memory");
  asm volatile("s_barrier" ::: "memory");
  const int b = blockIdx.x, g = b & 7, s = b >> 3;
  const int n16 = l & 15, j4 = l >> 4, q4 = (l >> 2) & 3, p4 = l & 3;
  const bool hasM = g < 7;
  const int me = g * 32 + s;

  if (__builtin_expect(wv == 8, 1)) {
    __builtin_amdgcn_s_setprio(3);
    const u32 fl = (u32)(size_t)(S + O_FL + 4 * F_LANDED);
    asm volatile("" :: "s"(wq), "s"(kc), "s"(vc), "s"(w1), "s"(w2), "s"(wo));
    STAMP(3);
    {
      const rsrc_t rq = mk(wq, 512 * 4096 * 2);
      const u32 base = ((s & 3) * 128) * 8192 + (g * 512 + (s >> 2) * 64) * 2;
      const u32 v0 = (l >> 3) * 8192 + (((l & 7) ^ (((l >> 4) & 1) << 1)) * 16), v1 = (v0 ^ 64) + 64512;
#pragma unroll 1
      for (int G = 0; G < 4; ++G) dma4(rq, S, O_WQ + G * 4096, v0, v1, v0 + 2 * 64512, v1 + 2 * 64512, base + G * 4 * 65536);
    }
    const size_t kvoff = ((size_t)g * SK + (SK - WIN) + s * 128) * 256;
    {
      const rsrc_t rk = mk((const char*)kc + kvoff, 32768);
      const u32 v0 = j4 * 256 + ((n16 ^ j4) * 16);
#pragma unroll 1
      for (int G = 0; G < 8; ++G) { landed<WINDOW - 4>(fl, N_WQ + 4 * G - (WINDOW - 4)); dma4(rk, S, O_K + G * 4096, v0, v0 ^ 64, v0 ^ 128, v0 ^ 192, G * 4096); }
    }
    STAMP(4);
    {
      const rsrc_t rv = mk((const char*)vc + kvoff, 32768);
      const u32 v0 = j4 * 256 + ((n16 ^ (j4 << 1)) * 16);
#pragma unroll 1
      for (int G = 0; G < 8; ++G) { landed<WINDOW - 4>(fl, N_K + 4 * G - (WINDOW - 4)); dma4(rv, S, O_V + G * 4096, v0, v0, v0 ^ 128, v0 ^ 128, G * 4096); }
    }
    STAMP(5);
    {
      const rsrc_t r1 = mk(w1, hasM ? 512 * FF * 2 : 0);
      const u32 hb = l >> 5, bl = hb * (FF * 2) + (((l & 31) ^ (hb << 1)) * 16);
      const u32 base = (s * 16) * (FF * 2) + g * 512;
#pragma unroll 1
      for (int G = 0; G < 2; ++G) {
        landed<WINDOW2 - 4>(fl, N_V + 4 * G - (WINDOW2 - 4));
        dma4(r1, S, O_W1 + G * 4096, bl, (bl ^ 64) + 6144, (bl ^ 128) + 2 * 6144, (bl ^ 192) + 3 * 6144, base + G * 8 * (FF * 2));
      }
    }
    {
      const rsrc_t r2 = mk((const char*)w2 - 1024, hasM ? 512 * FF * 2 + 1024 : 0);
      const u32 f0 = g * 256 + (s >> 2) * 32 + (s & 3) * 4;
#pragma unroll 1
      for (int G = 0; G < 2; ++G) {
        landed<WINDOW2 - 4>(fl, N_W1 + 4 * G - (WINDOW2 - 4));
        const u32 v = (l * 16) ^ (G << 7);
        dma4(r2, S, O_W2 + G * 4096, v + 1024, (v ^ 32) + 16384, v ^ 64, (v ^ 96) + 15360, (f0 + 2 * G) * 1024);
      }
    }
    {
      const rsrc_t ro = mk((const char*)wo - 1024, 4096 * 512 * 2 + 1024);
      const u32 r0 = g * 512 + (s >> 3) * 128 + ((s >> 1) & 3) * 32 + (s & 1) * 8;
#pragma unroll 1
      for (int G = 0; G < 4; ++G) {
        landed<WINDOW2 - 4>(fl, N_W2 + 4 * G - (WINDOW2 - 4));
        const u32 v = (l * 16) ^ ((G & 1) << 7);
        dma4(ro, S, O_WO + G * 4096, v + 1024, (v ^ 32) + 16384, v ^ 64, (v ^ 96) + 15360, (r0 + 2 * G) * 1024);
      }
    }
    STAMP(1);
    landed<8>(fl, N_WO - 8); landed<4>(fl, N_WO - 4); landed<0>(fl, N_WO);

    STAMP(2);
    __builtin_amdgcn_endpgm();
  }

  asm volatile("" :: "s"(x), "s"(ws), "s"(b1), "s"(b2), "s"(out));
  const rsrc_t rws = mk(ws, WS_END);

  if (wv == 9) {
    const u32 tag = mktag(__builtin_amdgcn_raw_buffer_load_b32(rws, WS_EPOCH, 0, AUX_SC1));
    const u64 tag2 = ((u64)tag << 32) | tag;

    wait_ge(S, F_LANDED, N_V);
    {
      const unsigned char* ps = ws + WS_ST + (me * 32 + (l >> 1)) * 64 + (l & 1) * 8;
      const unsigned char* ph = ws + WS_HH + ((hasM ? me : 0) * 32 + (l & 31)) * 64;
      bool so = false, ho = !hasM;
      for (int spin = 0; spin < SPIN_CAP && !(so && ho); ++spin) {
        if (!so && __all((u32)(poll64(ps) >> 32) == tag)) { so = true; set_flag(S, F_OOK, 1); }
        if (!ho && __all(poll64(ph) == tag2)) { ho = true; set_flag(S, F_HOK, 1); }
      }
      set_flag(S, F_OOK, 1); set_flag(S, F_HOK, 1);
    }
    STAMP(1);
    if (b < NC) {
      const unsigned char* p = ws + WS_FH + (b * 32 + (l & 31)) * 64;
      for (int spin = 0; spin < SPIN_CAP; ++spin) if (__all(poll64(p) == tag2)) break;
      set_flag(S, F_FOK, 1);
    }
    STAMP(2);
    __builtin_amdgcn_endpgm();
  }

  const u32x4 xr = __builtin_amdgcn_raw_buffer_load_b128(mk(x, 1024), l * 16, 0, 0);
  const u32 epoch = __builtin_amdgcn_raw_buffer_load_b32(rws, WS_EPOCH, 0, AUX_SC1);

  if (wv >= 4) {
    if (!hasM) __builtin_amdgcn_endpgm();
    const int w = wv - 4;
    const u32 f0 = g * 256 + (s >> 2) * 32 + (s & 3) * 4;
    const u32 b1lo = __builtin_amdgcn_raw_buffer_load_b16(mk(b1, FF * 2), (f0 + p4) * 2, 0, 0), b1hi = __builtin_amdgcn_raw_buffer_load_b16(mk(b1, FF * 2), (f0 + p4 + 16) * 2, 0, 0);
    lds_st<u32x4>(S, O_Y + l * 16, ln8(ln8(xr)));
    const u32x2 ay = lds_ld<u32x2>(S, O_Y + (s * 16 + 4 * j4) * 2);
    const u32 tag = mktag(epoch);

    const u32 a_hp = (WS_HP + ((g * 32 + (w * 2 + (j4 & 1)) * 4 + (n16 >> 2)) * 32 + s) * 64 + (n16 & 3) * 16) | msk(l < 32);
    const u32 a_bp = WS_SB + ((w * 16 + (l >> 2)) * 256 + me) * 128 + 64 + (l & 3) * 16;
    wait_ge(S, F_LANDED, HH_AT);
    stsc(rws, (WS_HH + ((g * 32 + w * 8 + (l >> 2)) * 32 + s) * 64 + (l & 3) * 16) | msk(l < 32), u32x4{tag, tag, tag, tag});
    wait_ge(S, F_LANDED, N_W1);
    {
      const u32 r = 4 * j4 + q4;
      const u32 ad = O_W1 + r * 512 + (((8 * w + (p4 >> 1)) ^ ((r & 7) << 1)) * 16) + (p4 & 1) * 8;
      f32x4 d[4];
#pragma unroll
      for (int tt = 0; tt < 4; ++tt) d[tt] = mfma16(ay, lds_tr(S, ad ^ (tt << 5)), f32x4{0.f, 0.f, 0.f, 0.f});
      const bool up = j4 & 1;
      stsc(rws, a_hp, u32x4{u_of(up ? d[2][0] : d[0][0]), tag, u_of(up ? d[3][0] : d[1][0]), tag});
    }
    STAMP(3);
    wait_ge(S, F_HOK, 1);
    const u32 hoff = WS_HP + (me * 32 + (l >> 2)) * 64 + (l & 3) * 16;
    u32x4 h0 = ldsc(rws, hoff), h1 = ldsc(rws, hoff + 16 * 64);
    for (int spin = 0; spin < SPIN_CAP && !__all(tok(h0, tag) & tok(h1, tag)); ++spin) { MEMCLOB(); h0 = ldsc(rws, hoff); h1 = ldsc(rws, hoff + 16 * 64); }
    STAMP(4);
    {
      float ha = f_of(h0.x) + f_of(h1.x), hb = f_of(h0.z) + f_of(h1.z);
      ha += dpp<ROR4>(ha); hb += dpp<ROR4>(hb); ha += dpp<ROR8>(ha); hb += dpp<ROR8>(hb);
      ha = x32_sum(x16_sum(ha)); hb = x32_sum(x16_sum(hb));
      const bool second = l & 4;
      const float p = rbf(rbf(second ? hb : ha) + f_of((second ? b1hi : b1lo) << 16));
      const float u = 0.7978845608028654f * (p + 0.044715f * p * p * p);
      const float th = 1.f - 2.f * rcp(ex2(u * 2.8853900817779268f) + 1.f);
      const float h = 0.5f * p * (1.f + th);
      if (l < 8) lds_st<u16>(S, O_HV + w * 16 + (p4 * 2 + (l >> 2)) * 2, (u16)pk(h, h));
    }
    u32x2 ah = lds_ld<u32x2>(S, O_HV + w * 16 + (j4 & 1) * 8);
    if (j4 >= 2) ah = u32x2{0u, 0u};
    wait_ge(S, F_LANDED, N_W2);
    {
      const u32 r = (4 * j4 + q4) & 7;
      const u32 ad = O_W2 + r * 1024 + (((16 * w + (p4 >> 1)) ^ (r << 1)) * 16) + (p4 & 1) * 8;
      f32x4 d[8];
#pragma unroll
      for (int tt = 0; tt < 8; ++tt) d[tt] = mfma16(ah, lds_tr(S, ad ^ (tt << 5)), f32x4{0.f, 0.f, 0.f, 0.f});
      const bool b0 = j4 & 1, b1_ = j4 & 2;
      const float e0 = b1_ ? (b0 ? d[6][0] : d[4][0]) : (b0 ? d[2][0] : d[0][0]), e1 = b1_ ? (b0 ? d[7][0] : d[5][0]) : (b0 ? d[3][0] : d[1][0]);
      stsc(rws, a_bp, u32x4{u_of(e0), tag, u_of(e1), tag});
    }
    STAMP(5);
    __builtin_amdgcn_endpgm();
  }

  const int w = wv;
  const int fcol = (b >> 4) * 128 + ((b >> 2) & 3) * 32 + (b & 3) * 4 + p4 + ((l >> 2) & 1) * 16;
  const u32 b2h = __builtin_amdgcn_raw_buffer_load_b16(mk(b2, 1024), (fcol * 2) | msk(b < NC), 0, 0);
  const u32 xfh = __builtin_amdgcn_raw_buffer_load_b16(mk(x, 1024), (fcol * 2) | msk(b < NC), 0, 0);
  lds_st<u32x4>(S, O_XN + l * 16, ln8(xr));
  const u32 tag = mktag(epoch);

  STAMP(1);
  {
    u32x4 ax[4];
#pragma unroll
    for (int ks = 0; ks < 4; ++ks) ax[ks] = lds_ld<u32x4>(S, O_XN + ((s & 3) * 128 + 32 * ks + 8 * j4) * 2);
    const u32 gq = (q4 >> 1) | ((j4 & 1) << 1);
    const u32 ad = O_WQ + (8 * j4 + q4) * 128 + (((2 * w + (p4 >> 1)) ^ (gq << 1)) * 16) + (p4 & 1) * 8;
    wait_ge(S, F_LANDED, N_WQ);
    f32x4 d = {0.f, 0.f, 0.f, 0.f};
#pragma unroll
    for (int ks = 0; ks < 4; ++ks) {
      const u32x2 b0 = lds_tr(S, ad + ks * 4096), b1_ = lds_tr(S, ad + ks * 4096 + 512);
      d = mfma32(ax[ks], u32x4{b0.x, b0.y, b1_.x, b1_.y}, d);
    }
    stsc2(rws, (WS_QP + ((g * 4 + (s & 3)) * 512 + (s >> 2) * 64 + w * 16 + n16) * 8) | msk(l < 16), u32x2{u_of(d[0]), tag});
  }
  STAMP(2);
  wait_ge(S, F_LANDED, Q_AT);
  {
    const u32 qoff = WS_QP + (g * 4 * 512 + w * 128 + 2 * l) * 8;
    u32x4 qg[4];
#pragma unroll
    for (int i = 0; i < 4; ++i) qg[i] = ldsc(rws, qoff + i * 4096);
    for (int spin = 0; spin < SPIN_CAP && !__all(tok(qg[0], tag) & tok(qg[1], tag) & tok(qg[2], tag) & tok(qg[3], tag)); ++spin) {
      MEMCLOB();
#pragma unroll
      for (int i = 0; i < 4; ++i) qg[i] = ldsc(rws, qoff + i * 4096);
    }
    lds_st<u32>(S, O_QS + w * 256 + l * 4, pk((f_of(qg[0].x) + f_of(qg[1].x)) + (f_of(qg[2].x) + f_of(qg[3].x)), (f_of(qg[0].z) + f_of(qg[1].z)) + (f_of(qg[2].z) + f_of(qg[3].z))));
    if (l == 0) set_flag(S, F_Q + w, 1);
  }
  STAMP(3);
  wait4(S, F_Q);
  {
    u32x4 bq[4];
#pragma unroll
    for (int ks = 0; ks < 4; ++ks) bq[ks] = lds_ld<u32x4>(S, O_QS + (n16 & 3) * 256 + ks * 64 + j4 * 16);
    wait_ge(S, F_LANDED, N_WQ + 8 * (w + 1));
    float sc[8];
#pragma unroll
    for (int kb = 0; kb < 2; ++kb) {
      f32x4 d = {0.f, 0.f, 0.f, 0.f};
#pragma unroll
      for (int ks = 0; ks < 4; ++ks)
        d = mfma32(lds_ld<u32x4>(S, O_K + (32 * w + 16 * kb + n16) * 256 + (((4 * ks + j4) ^ n16) * 16)), bq[ks], d);
#pragma unroll
      for (int r = 0; r < 4; ++r) sc[4 * kb + r] = rbf(rbf(d[r]) * 0.08838834764831845f);
    }
    float mw = fmaxf(fmaxf(fmaxf(sc[0], sc[1]), fmaxf(sc[2], sc[3])), fmaxf(fmaxf(sc[4], sc[5]), fmaxf(sc[6], sc[7])));
    mw = x32_max(x16_max(mw));
    float ls = 0; float p[8];
#pragma unroll
    for (int i = 0; i < 8; ++i) { p[i] = ex2((sc[i] - mw) * 1.4426950408889634f); ls += p[i]; }
    ls = x32_sum(x16_sum(ls));
    if (n16 < 4) {
      lds_st<u32x2>(S, O_P + n16 * 256 + (32 * w + 4 * j4) * 2, u32x2{pk(p[0], p[1]), pk(p[2], p[3])});
      lds_st<u32x2>(S, O_P + n16 * 256 + (32 * w + 16 + 4 * j4) * 2, u32x2{pk(p[4], p[5]), pk(p[6], p[7])});
    }
    if (l < 4) { lds_st<float>(S, O_MW + l * 16 + w * 4, mw); lds_st<float>(S, O_LW + l * 16 + w * 4, ls); }
    if (l == 0) set_flag(S, F_P + w, 1);
  }
  STAMP(4);
  wait4(S, F_P);
  {
    u32x4 ap[4];
#pragma unroll
    for (int ks = 0; ks < 4; ++ks) ap[ks] = lds_ld<u32x4>(S, O_P + (n16 >> 2) * 256 + ks * 64 + j4 * 16);
    const f32x4 m4 = lds_ld<f32x4>(S, O_MW + j4 * 16), l4 = lds_ld<f32x4>(S, O_LW + j4 * 16);
    const float M = fmaxf(fmaxf(m4[0], m4[1]), fmaxf(m4[2], m4[3]));
    float al[4];
#pragma unroll
    for (int ks = 0; ks < 4; ++ks) al[ks] = ex2((m4[ks] - M) * 1.4426950408889634f);
    const float L = (al[0] * l4[0] + al[1] * l4[1]) + (al[2] * l4[2] + al[3] * l4[3]);
    const u32 gv = q4 | ((j4 & 1) << 2);
    const u32 ad0 = O_V + (8 * j4 + q4) * 256 + (((4 * w + (p4 >> 1)) ^ (gv << 1)) * 16) + (p4 & 1) * 8, ad1 = ad0 ^ 32;
    const u32 a_op = WS_OP + ((g * 32 + (j4 * 4 + w) * 2 + (n16 >> 3)) * 32 + s) * 128 + (n16 & 7) * 16;
    float o0 = 0, o1 = 0;
#pragma unroll
    for (int ks = 0; ks < 4; ++ks) {
      if (ks == 3) {
        wait_ge(S, F_LANDED, ST_AT);
        stsc(rws, (WS_ST + ((g * 32 + j4 * 8 + 2 * w + ((n16 >> 2) & 1)) * 32 + s) * 64 + p4 * 16) | msk(n16 < 8), u32x4{u_of(M), tag, u_of(L), tag});
      }
      wait_ge(S, F_LANDED, N_K + 8 * (ks + 1));
      const u32x2 a0 = lds_tr(S, ad0 + ks * 8192), a1 = lds_tr(S, ad0 + ks * 8192 + 1024);
      const u32x2 c0 = lds_tr(S, ad1 + ks * 8192), c1 = lds_tr(S, ad1 + ks * 8192 + 1024);
      const f32x4 d0 = mfma32(ap[ks], u32x4{a0.x, a0.y, a1.x, a1.y}, f32x4{0.f, 0.f, 0.f, 0.f});
      const f32x4 d1 = mfma32(ap[ks], u32x4{c0.x, c0.y, c1.x, c1.y}, f32x4{0.f, 0.f, 0.f, 0.f});
      o0 = fmaf(al[ks], d0[0], o0); o1 = fmaf(al[ks], d1[0], o1);
    }
    stsc(rws, a_op, u32x4{u_of(o0), tag, u_of(o1), tag});
  }
  STAMP(5);
  wait_ge(S, F_OOK, 1);
  {
    const u32 ooff = WS_OP + (me * 32 + (l >> 1)) * 128 + (2 * w + (l & 1)) * 16, soff = WS_ST + (me * 32 + (l >> 1)) * 64;
    u32x4 ov = ldsc(rws, ooff), sv = ldsc(rws, soff);
    for (int spin = 0; spin < SPIN_CAP && !__all(tok(ov, tag) & tok(sv, tag)); ++spin) { MEMCLOB(); ov = ldsc(rws, ooff); sv = ldsc(rws, soff); }
    STAMP(6);
    if (w == 0 && (s & 7) == 0)
#pragma unroll
      for (int i = 0; i < 4; ++i) stsc(rws, WS_FH + ((i * 16 + (l >> 2)) * 32 + g * 4 + (s >> 3)) * 64 + (l & 3) * 16, u32x4{tag, tag, tag, tag});
    const float mp = f_of(sv.x), lp = f_of(sv.z);
    const float Mx = par_max(mp);
    const float wp = ex2((mp - Mx) * 1.4426950408889634f);
    const float Ls = par_sum(wp * lp), o0 = par_sum(wp * f_of(ov.x)), o1 = par_sum(wp * f_of(ov.z));
    const float inv = rcp(Ls);
    if (l < 2) lds_st<u32>(S, O_AV + w * 8 + l * 4, pk(o0 * inv, o1 * inv));
    if (l == 0) set_flag(S, F_O + w, 1);
  }
  wait4(S, F_O);
  {
    const u32x2 a = lds_ld<u32x2>(S, O_AV + j4 * 8);
    wait_ge(S, F_LANDED, N_WO);
    const u32 r = 4 * j4 + q4;
    const u32 ad = O_WO + r * 1024 + (((16 * w + (p4 >> 1)) ^ ((r & 7) << 1)) * 16) + (p4 & 1) * 8;
    f32x4 d[8];
#pragma unroll
    for (int tt = 0; tt < 8; ++tt) d[tt] = mfma16(a, lds_tr(S, ad ^ (tt << 5)), f32x4{0.f, 0.f, 0.f, 0.f});
    const bool b0 = j4 & 1, b1_ = j4 & 2;
    const float e0 = b1_ ? (b0 ? d[6][0] : d[4][0]) : (b0 ? d[2][0] : d[0][0]), e1 = b1_ ? (b0 ? d[7][0] : d[5][0]) : (b0 ? d[3][0] : d[1][0]);
    stsc(rws, WS_SB + ((w * 16 + (l >> 2)) * 256 + me) * 128 + (l & 3) * 16, u32x4{u_of(e0), tag, u_of(e1), tag});
  }
  STAMP(7);
  if (b < NC) {
    wait_ge(S, F_FOK, 1);
    u32 off[8]; u32x4 v[8];
#pragma unroll
    for (int i = 0; i < 8; ++i) {
      const int p = 64 * w + 8 * i + (l >> 3);
      off[i] = (WS_SB + (b * 256 + p) * 128 + (l & 7) * 16) | msk(!((l & 4) && p >= 224));
      v[i] = ldsc(rws, off[i]);
    }
    for (int spin = 0; spin < SPIN_CAP; ++spin) {
      bool ok = true;
#pragma unroll
      for (int i = 0; i < 8; ++i) ok = ok & (tok(v[i], tag) | (off[i] >= OOB));
      if (__all(ok)) break;
      MEMCLOB();
#pragma unroll
      for (int i = 0; i < 8; ++i) v[i] = ldsc(rws, off[i]);
    }
    STAMP(8);
    float r0 = 0, r1 = 0;
#pragma unroll
    for (int i = 0; i < 8; ++i) { r0 += f_of(v[i].x); r1 += f_of(v[i].z); }
    r0 += dpp<ROR8>(r0); r1 += dpp<ROR8>(r1);
    r0 = x32_sum(x16_sum(r0)); r1 = x32_sum(x16_sum(r1));
    if (l < 8) lds_st<f32x2>(S, O_FIN + w * 64 + l * 8, f32x2{r0, r1});
    if (l == 0) set_flag(S, F_F + w, 1);
    if (w == 0) {
      wait4(S, F_F);
      if (l < 8) {
        const u32 u = l & 3, e = l >> 2;
        float sv = 0, mv = 0;
#pragma unroll
        for (int ww = 0; ww < 4; ++ww) { sv += lds_ld<float>(S, O_FIN + ww * 64 + u * 8 + e * 4); mv += lds_ld<float>(S, O_FIN + ww * 64 + (4 + u) * 8 + e * 4); }
        const float B = rbf(rbf(mv) + f_of(b2h << 16));
        const float gt = rbf(B * rcp(1.f + ex2(-B * 1.4426950408889634f)));
        const float o = f_of(xfh << 16) + rbf(rbf(sv) * gt);
        out[fcol] = (u16)pk(o, o);
        if (b == 0 && l == 0) __builtin_amdgcn_raw_buffer_store_b32(epoch + 1u, rws, WS_EPOCH, 0, AUX_SC1);
      }
    }
  }
  STAMP(9);
}

extern "C" void kernel_launch(void* const* d_in, const int* in_sizes, int n_in, void* d_out, int out_size, void* d_ws, size_t ws_size, hipStream_t stream) {
  fused_decode<<<NWG, NT, 0, stream>>>((const u16*)d_in[0], (const u16*)d_in[1], (const u16*)d_in[2], (const u16*)d_in[3], (const u16*)d_in[4],
                                       (const u16*)d_in[6], (const u16*)d_in[7], (const u16*)d_in[8], (const u16*)d_in[9], (u16*)d_out, (unsigned char*)d_ws);
}
```

```cpp
#include <hip/hip_runtime.h>
#include <stdint.h>

typedef unsigned short u16;
typedef unsigned u32;
typedef unsigned long long u64;
typedef u32 u32x4 __attribute__((ext_vector_type(4)));
typedef u32 u32x2 __attribute__((ext_vector_type(2)));
typedef float f32x4 __attribute__((ext_vector_type(4)));
typedef float f32x2 __attribute__((ext_vector_type(2)));
typedef short s16x4 __attribute__((ext_vector_type(4)));
typedef __bf16 bf16x8 __attribute__((ext_vector_type(8)));
typedef __bf16 bf16x2 __attribute__((ext_vector_type(2)));
typedef __amdgpu_buffer_rsrc_t rsrc_t;
#define LAS __attribute__((address_space(3)))

constexpr int SK = 65536, WIN = 4096, FF = 1792;
constexpr int NWG = 256, NT = 640;
constexpr int NC = 64;
constexpr u32 WS_EPOCH = 0;
constexpr u32 WS_QP = 4096;
constexpr u32 WS_HB = WS_QP + 8 * 4 * 512 * 8;
constexpr u32 HB_ST = 0, HB_HH = 512, HB_FH = 768;
constexpr u32 WS_OP = WS_HB + 256 * 1024;
constexpr u32 WS_HP = WS_OP + 256 * 32 * 128;
constexpr u32 WS_SB = WS_HP + 224 * 32 * 64;
constexpr u32 WS_END = WS_SB + NC * 256 * 128;
constexpr u32 OOB = 0x80000000u;
constexpr u32 O_WQ = 0, O_K = 16384, O_V = 49152, O_W1 = 81920, O_W2 = 90112, O_WO = 98304, O_S = 114688;
constexpr u32 O_X = O_S, O_MISC = O_X + 1024, O_S2 = O_MISC + 1024;
constexpr u32 O_XN = O_S2, O_Y = O_XN + 1024, O_QS = O_Y + 1024, O_P = O_QS + 1024, O_MW = O_P + 1024, O_LW = O_MW + 64, O_AV = O_LW + 64,
              O_WT = O_AV + 64, O_HV = O_WT + 128, O_FIN = O_HV + 64, O_FL = O_FIN + 256, LDS_BYTES = O_FL + 128;
constexpr u32 O_INIT = O_AV;
enum { F_LANDED = 0, F_OOK = 2, F_HOK = 3, F_FOK = 4, F_WOK = 5, F_OG = 6, F_Q = 12, F_P = 16, F_F = 24 };
constexpr int N_X = 2, N_WQ = N_X + 16, N_K = N_WQ + 32, N_V = N_K + 32, N_W1 = N_V + 8, N_W2 = N_W1 + 8, N_WO = N_W2 + 16;
#define WINDOW 20
#define WINDOW2 16
#define Q_AT (N_WQ + 24)
#define ST_AT (N_V - 4)
#define HH_AT N_V
#define POLL_AT N_V
#define CODE_BYTES 11520
constexpr int AUX_NT = 2, AUX_SC1 = 16;
constexpr int SPIN_CAP = 1 << 14;

#define KEEP(x) asm volatile("" :: "v"(x))
#define MEMCLOB() asm volatile("" ::: "memory")
#define STAMP(k) do { } while (0)

__device__ __forceinline__ rsrc_t mk(const void* p, u32 bytes) { return __builtin_amdgcn_make_buffer_rsrc((void*)p, 0, bytes, 0x00020000); }
__device__ __forceinline__ u32x4 ldsc(rsrc_t r, u32 voff) { return __builtin_amdgcn_raw_buffer_load_b128(r, voff, 0, AUX_SC1); }
#define FIRST_AUX 0
__device__ __forceinline__ u32x4 ld1(rsrc_t r, u32 voff) { return __builtin_amdgcn_raw_buffer_load_b128(r, voff, 0, FIRST_AUX); }
__device__ __forceinline__ void stsc(rsrc_t r, u32 voff, u32x4 v) { __builtin_amdgcn_raw_buffer_store_b128(v, r, voff, 0, AUX_SC1); }
__device__ __forceinline__ void stsc2(rsrc_t r, u32 voff, u32x2 v) { __builtin_amdgcn_raw_buffer_store_b64(v, r, voff, 0, AUX_SC1); }
__device__ __forceinline__ float f_of(u32 u) { return __builtin_bit_cast(float, u); }
__device__ __forceinline__ u32 u_of(float f) { return __builtin_bit_cast(u32, f); }
__device__ __forceinline__ float lo16(u32 w) { return f_of(w << 16); }
__device__ __forceinline__ float hi16(u32 w) { return f_of(w & 0xffff0000u); }
__device__ __forceinline__ u32 pk(float lo, float hi) { bf16x2 r = {(__bf16)lo, (__bf16)hi}; return __builtin_bit_cast(u32, r); }
__device__ __forceinline__ float rbf(float f) { return lo16(pk(f, f)); }
template <int CTRL> __device__ __forceinline__ float dpp(float x) { return f_of(__builtin_amdgcn_mov_dpp(u_of(x), CTRL, 0xf, 0xf, true)); }
constexpr int XOR1 = 0xB1, XOR2 = 0x4E, HMIR = 0x141, MIR = 0x140, ROR4 = 0x124, ROR8 = 0x128;
__device__ __forceinline__ float x16_sum(float x) { auto s = __builtin_amdgcn_permlane16_swap(u_of(x), u_of(x), false, false); return f_of(s[0]) + f_of(s[1]); }
__device__ __forceinline__ float x32_sum(float x) { auto s = __builtin_amdgcn_permlane32_swap(u_of(x), u_of(x), false, false); return f_of(s[0]) + f_of(s[1]); }
__device__ __forceinline__ float x16_max(float x) { auto s = __builtin_amdgcn_permlane16_swap(u_of(x), u_of(x), false, false); return fmaxf(f_of(s[0]), f_of(s[1])); }
__device__ __forceinline__ float x32_max(float x) { auto s = __builtin_amdgcn_permlane32_swap(u_of(x), u_of(x), false, false); return fmaxf(f_of(s[0]), f_of(s[1])); }
__device__ __forceinline__ float row_sum(float x) { x += dpp<XOR1>(x); x += dpp<XOR2>(x); x += dpp<HMIR>(x); x += dpp<MIR>(x); return x; }
__device__ __forceinline__ float wave_sum(float x) { return x32_sum(x16_sum(row_sum(x))); }
__device__ __forceinline__ float vmax(float a, float b) { float r; asm("v_max_f32 %0, %1, %2" : "=v"(r) : "v"(a), "v"(b)); return r; }
__device__ __forceinline__ float vmax3(float a, float b, float c) { float r; asm("v_max3_f32 %0, %1, %2, %3" : "=v"(r) : "v"(a), "v"(b), "v"(c)); return r; }
#define DPPMAX(x, ctrl) asm("s_nop 1\n\tv_max_f32_dpp %0, %0, %0 " ctrl " row_mask:0xf bank_mask:0xf" : "+v"(x))
__device__ __forceinline__ float xr_max(float x) {
  auto s = __builtin_amdgcn_permlane16_swap(u_of(x), u_of(x), false, false); x = vmax(f_of(s[0]), f_of(s[1]));
  auto t = __builtin_amdgcn_permlane32_swap(u_of(x), u_of(x), false, false); return vmax(f_of(t[0]), f_of(t[1]));
}
__device__ __forceinline__ float par_sum(float x) { x += dpp<XOR2>(x); x += dpp<ROR4>(x); x += dpp<ROR8>(x); return x32_sum(x16_sum(x)); }
__device__ __forceinline__ float par_max(float x) { DPPMAX(x, "quad_perm:[2,3,0,1]"); DPPMAX(x, "row_ror:4"); DPPMAX(x, "row_ror:8"); return xr_max(x); }
__device__ __forceinline__ float ex2(float x) { return __builtin_amdgcn_exp2f(x); }
__device__ __forceinline__ float rcp(float x) { return __builtin_amdgcn_rcpf(x); }
__device__ __forceinline__ f32x4 mfma32(u32x4 a, u32x4 b, f32x4 c) {
  return __builtin_amdgcn_mfma_f32_16x16x32_bf16(__builtin_bit_cast(bf16x8, a), __builtin_bit_cast(bf16x8, b), c, 0, 0, 0);
}
__device__ __forceinline__ f32x4 mfma16(u32x2 a, u32x2 b, f32x4 c) {
  return __builtin_amdgcn_mfma_f32_16x16x16bf16_1k(__builtin_bit_cast(s16x4, a), __builtin_bit_cast(s16x4, b), c, 0, 0, 0);
}
typedef LAS unsigned char* lds_t;
template <class T> __device__ __forceinline__ T lds_ld(lds_t s, u32 off) { return *(const LAS T*)(s + off); }
template <class T> __device__ __forceinline__ void lds_st(lds_t s, u32 off, T v) { *(LAS T*)(s + off) = v; }
__device__ __forceinline__ u32x2 lds_tr(lds_t s, u32 off) { return __builtin_bit_cast(u32x2, __builtin_amdgcn_ds_read_tr16_b64_v4i16((LAS s16x4*)(s + off))); }
__device__ __forceinline__ void set_flag(lds_t s, int f, u32 v) { MEMCLOB(); *(volatile LAS u32*)(s + O_FL + 4 * f) = v; MEMCLOB(); }
__device__ __forceinline__ void wait_ge(lds_t s, int f, int need) {
#pragma unroll 1
  for (int spin = 0; spin < (1 << 22); ++spin) {
    const int v = __builtin_amdgcn_readfirstlane(*(volatile LAS int*)(s + O_FL + 4 * f));
    if (v >= need) break;
  }
  MEMCLOB();
}
__device__ __forceinline__ void wait4(lds_t s, int f) {
#pragma unroll 1
  for (int spin = 0; spin < (1 << 22); ++spin) {
    const u32x4 v = *(volatile LAS u32x4*)(s + O_FL + 4 * f);
    if (__builtin_amdgcn_readfirstlane(v.x & v.y & v.z & v.w)) break;
  }
  MEMCLOB();
}
__device__ __forceinline__ u32x4 ln8(u32x4 xr) {
  float v[8];
#pragma unroll
  for (int i = 0; i < 4; ++i) { v[2 * i] = lo16(xr[i]); v[2 * i + 1] = hi16(xr[i]); }
  const float s = ((v[0] + v[1]) + (v[2] + v[3])) + ((v[4] + v[5]) + (v[6] + v[7]));
  const float m = wave_sum(s) * (1.f / 512);
  float q0 = 0, q1 = 0;
#pragma unroll
  for (int i = 0; i < 4; ++i) { const float d0 = v[2 * i] - m, d1 = v[2 * i + 1] - m; q0 = fmaf(d0, d0, q0); q1 = fmaf(d1, d1, q1); }
  const float var = wave_sum(q0 + q1) * (1.f / 512);
  const float mb = rbf(m);
  const float inv = rcp(rbf(__builtin_amdgcn_sqrtf(rbf(rbf(var) + 1e-5f))));
  u32x4 r;
#pragma unroll
  for (int i = 0; i < 4; ++i) { const u32 d = pk(v[2 * i] - mb, v[2 * i + 1] - mb); r[i] = pk(lo16(d) * inv, hi16(d) * inv); }
  return r;
}
__device__ __forceinline__ u64 poll64(const unsigned char* p) {
  u64 r; const u64 z = 0;
  asm volatile("global_atomic_or_x2 %0, %1, %2, off sc0 sc1\n\ts_waitcnt vmcnt(0)" : "=&v"(r) : "v"(p), "v"(z) : "memory");
  return r;
}
__device__ __forceinline__ void dma4(rsrc_t r, lds_t s, u32 ldsoff, u32 v0, u32 v1, u32 v2, u32 v3, u32 soff) {
  __builtin_amdgcn_raw_ptr_buffer_load_lds(r, (LAS void*)(s + ldsoff), 16, v0, soff, 0, AUX_NT);
  __builtin_amdgcn_raw_ptr_buffer_load_lds(r, (LAS void*)(s + ldsoff), 16, v1, soff, 1024, AUX_NT);
  __builtin_amdgcn_raw_ptr_buffer_load_lds(r, (LAS void*)(s + ldsoff), 16, v2, soff, 2048, AUX_NT);
  __builtin_amdgcn_raw_ptr_buffer_load_lds(r, (LAS void*)(s + ldsoff), 16, v3, soff, 3072, AUX_NT);
}
template <int N> __device__ __forceinline__ void landed(u32 flag_addr, int v) {
  asm volatile("s_waitcnt vmcnt(%0)\n\tds_write_b32 %1, %2" :: "i"(N), "v"(flag_addr), "v"(v) : "memory");
}
__device__ __forceinline__ u32 msk(bool c) { return c ? 0u : OOB; }
__device__ __forceinline__ bool tok(u32x4 v, u32 tag) { return (v.y == tag) & (v.w == tag); }
__device__ __forceinline__ u32 mktag(u32 epoch) { return ((epoch + 1u) * 0x9E3779B1u) ^ 0x5bd1e995u; }

__global__ void __launch_bounds__(NT) fused_decode(const u16* __restrict__ x, const u16* __restrict__ kc, const u16* __restrict__ vc,
                                                   const u16* __restrict__ wq, const u16* __restrict__ wo, const u16* __restrict__ w1,
                                                   const u16* __restrict__ b1, const u16* __restrict__ w2, const u16* __restrict__ b2,
                                                   u16* __restrict__ out, unsigned char* __restrict__ ws) {
  __shared__ __attribute__((aligned(16))) unsigned char smem[LDS_BYTES];
  lds_t S = (lds_t)smem;
  STAMP(0);
  const int t = threadIdx.x, l = t & 63, wv = __builtin_amdgcn_readfirstlane(t >> 6);
  if (wv == 0) {
    if (l < 32) lds_st<u32>(S, O_FL + 4 * l, 0u); else if (l < 48) lds_st<u32>(S, O_AV + 4 * (l - 32), 0u);
    asm volatile("s_waitcnt lgkmcnt(0)" ::: "memory");
  }
  asm volatile("s_load_dword s94, %0, 0x0\n\ts_load_dword s95, %0, 0x40" :: "s"(__builtin_amdgcn_kernarg_segment_ptr()) : "s94", "s95", "memory");
  const rsrc_t rcode = mk((const void*)(__builtin_amdgcn_s_getpc() & ~255ull), CODE_BYTES);
  asm volatile("buffer_load_dwordx4 v[120:123], %0, %1, 0 offen\n\tbuffer_load_dwordx4 v[120:123], %2, %1, 0 offen"
               :: "v"(l * 16 + (wv + 1) * 1024), "s"(rcode), "v"(l * 16 + (wv + 11) * 1024) : "v120", "v121", "v122", "v123", "memory");
  asm volatile("s_barrier" ::: "memory");
  const int b = blockIdx.x, g = b & 7, s = b >> 3;
  const int n16 = l & 15, j4 = l >> 4, q4 = (l >> 2) & 3, p4 = l & 3;
  const bool hasM = g < 7;
  const int me = g * 32 + s;

  if (wv != 8) {
  asm volatile("" :: "s"(ws), "s"(out));
  const rsrc_t rws = mk(ws, WS_END);

  if (wv == 9) {
    const u32x4 warm = ldsc(rws, WS_HB + me * 1024 + l * 16);
    const u32x4 warm2 = ldsc(rws, (WS_HB + b * 1024 + HB_FH + l * 16) | msk(b < NC && l < 16));
    const unsigned char* ps = ws + WS_HB + me * 1024 + HB_ST + l * 8;
    KEEP(warm); KEEP(warm2);
    wait_ge(S, F_LANDED, N_X);
    const u32 tag = mktag(lds_ld<u32>(S, O_MISC));
    wait_ge(S, F_LANDED, POLL_AT);
_Pragma("unroll 1")
    for (int spin = 0; spin < SPIN_CAP; ++spin) {
      const u64 rs = poll64(ps);
      if (__all((u32)(rs >> 32) == tag)) {
        set_flag(S, F_OOK, 1);
        const float v = f_of((u32)rs), o = dpp<XOR1>(v);
        const float mp = (l & 1) ? o : v, lp = (l & 1) ? v : o;
        const float wp = ex2((mp - par_max(mp)) * 1.4426950408889634f);
        const float wn = wp * rcp(par_sum(wp * lp));
        if ((l & 1) == 0) lds_st<float>(S, O_WT + (l >> 1) * 4, wn);
        break;
      }
    }
    set_flag(S, F_OOK, 1); set_flag(S, F_WOK, 1);
    STAMP(1);
    if ((s & 7) == 0) {
      wait_ge(S, F_OG, 1);
      stsc2(rws, WS_HB + l * 1024 + HB_FH + (g * 4 + (s >> 3)) * 8, u32x2{tag, tag});
    }
    if (b < NC) {
      const unsigned char* p = ws + WS_HB + b * 1024 + HB_FH + (l & 31) * 8;
_Pragma("unroll 1")
      for (int spin = 0; spin < SPIN_CAP; ++spin) if (__all((u32)(poll64(p) >> 32) == tag)) break;
      set_flag(S, F_FOK, 1);
    }
    STAMP(2);
    __builtin_amdgcn_endpgm();
  }

  if (wv >= 4 && !hasM) __builtin_amdgcn_endpgm();
  wait_ge(S, F_LANDED, N_X);
  const u32x4 xr = lds_ld<u32x4>(S, O_X + l * 16);
  const u32 epoch = lds_ld<u32>(S, O_MISC);

  if (wv >= 4) {
    const int w = wv - 4;
    const u32 f0 = g * 256 + (s >> 2) * 32 + (s & 3) * 4;
    const u32 b1v = lds_ld<u16>(S, O_MISC + 16 + ((l & 4) ? 16 : 0) + ((f0 * 2) & 15) + p4 * 2);
    lds_st<u32x4>(S, O_Y + l * 16, ln8(ln8(xr)));
    const u32x2 ay = lds_ld<u32x2>(S, O_Y + (s * 16 + 4 * j4) * 2);
    const u32 tag = mktag(epoch);
    u32 a_hp = (WS_HP + ((g * 32 + (w * 2 + (j4 & 1)) * 4 + (n16 >> 2)) * 32 + s) * 64 + (n16 & 3) * 16) | msk(l < 32);
    u32 a_bp = WS_SB + ((w * 16 + (l >> 2)) * 256 + me) * 128 + 64 + (l & 3) * 16;
    u32 a_hh = (WS_HB + (g * 32 + w * 8 + (l & 7)) * 1024 + HB_HH + s * 8) | msk(l < 8);
    u32 hoff = WS_HP + (me * 32 + (l >> 2)) * 64 + (l & 3) * 16;
    u32 ad1 = O_W1 + (4 * j4 + q4) * 512 + (((8 * w + (p4 >> 1)) ^ (((4 * j4 + q4) & 7) << 1)) * 16) + (p4 & 1) * 8;
    u32 ad2 = O_W2 + ((4 * j4 + q4) & 7) * 1024 + (((16 * w + (p4 >> 1)) ^ (((4 * j4 + q4) & 7) << 1)) * 16) + (p4 & 1) * 8;
    float b1f = f_of(b1v << 16);
    asm volatile("" : "+v"(a_hp), "+v"(a_bp), "+v"(a_hh), "+v"(hoff), "+v"(ad1), "+v"(ad2), "+v"(b1f));
    wait_ge(S, F_LANDED, HH_AT);
    stsc2(rws, a_hh, u32x2{tag, tag});
    wait_ge(S, F_LANDED, N_W1);
    {
      f32x4 d[4];
#pragma unroll
      for (int tt = 0; tt < 4; ++tt) d[tt] = mfma16(ay, lds_tr(S, ad1 ^ (tt << 5)), f32x4{0.f, 0.f, 0.f, 0.f});
      const bool up = j4 & 1;
      stsc(rws, a_hp, u32x4{u_of(up ? d[2][0] : d[0][0]), tag, u_of(up ? d[3][0] : d[1][0]), tag});
    }
    STAMP(3);
    wait_ge(S, F_LANDED, N_W2);
    u32x2 bw[8];
#pragma unroll
    for (int tt = 0; tt < 8; ++tt) bw[tt] = lds_tr(S, ad2 ^ (tt << 5));
    if (w == 0) {
      const unsigned char* ph = ws + WS_HB + me * 1024 + HB_HH + (l & 31) * 8;
_Pragma("unroll 1")
      for (int spin = 0; spin < SPIN_CAP; ++spin) if (__all((u32)(poll64(ph) >> 32) == tag)) break;
      set_flag(S, F_HOK, 1);
    } else wait_ge(S, F_HOK, 1);
    u32x4 h0 = ld1(rws, hoff), h1 = ld1(rws, hoff + 16 * 64);
_Pragma("unroll 1")
    for (int spin = 0; spin < SPIN_CAP && !__all(tok(h0, tag) & tok(h1, tag)); ++spin) { MEMCLOB(); h0 = ldsc(rws, hoff); h1 = ldsc(rws, hoff + 16 * 64); }
    STAMP(4);
    {
      float ha = f_of(h0.x) + f_of(h1.x), hb = f_of(h0.z) + f_of(h1.z);
      ha += dpp<ROR4>(ha); hb += dpp<ROR4>(hb); ha += dpp<ROR8>(ha); hb += dpp<ROR8>(hb);
      ha = x32_sum(x16_sum(ha)); hb = x32_sum(x16_sum(hb));
      const float p = rbf(rbf((l & 4) ? hb : ha) + b1f);
      const float u = p * fmaf(0.044715f * 0.7978845608028654f * 2.8853900817779268f, p * p, 0.7978845608028654f * 2.8853900817779268f);
      const float h = p - p * rcp(ex2(u) + 1.f);
      if (l < 8) lds_st<u16>(S, O_HV + w * 16 + (p4 * 2 + (l >> 2)) * 2, (u16)pk(h, h));
    }
    u32x2 ah = lds_ld<u32x2>(S, O_HV + w * 16 + (j4 & 1) * 8);
    if (j4 >= 2) ah = u32x2{0u, 0u};
    {
      f32x4 d[8];
#pragma unroll
      for (int tt = 0; tt < 8; ++tt) d[tt] = mfma16(ah, bw[tt], f32x4{0.f, 0.f, 0.f, 0.f});
      const bool b0 = j4 & 1, b1_ = j4 & 2;
      const float e0 = b1_ ? (b0 ? d[6][0] : d[4][0]) : (b0 ? d[2][0] : d[0][0]), e1 = b1_ ? (b0 ? d[7][0] : d[5][0]) : (b0 ? d[3][0] : d[1][0]);
      stsc(rws, a_bp, u32x4{u_of(e0), tag, u_of(e1), tag});
    }
    STAMP(5);
    __builtin_amdgcn_endpgm();
  }

  const int w = wv;
  const int fcol = (b >> 4) * 128 + ((b >> 2) & 3) * 32 + (b & 3) * 4 + p4 + ((l >> 2) & 1) * 16;
  const u32 b2h = lds_ld<u16>(S, O_MISC + 48 + ((l & 4) ? 16 : 0) + (((b & 3) * 8) & 15) + p4 * 2);
  const u32 xfh = lds_ld<u16>(S, O_X + (fcol & 511) * 2);
  lds_st<u32x4>(S, O_XN + l * 16, ln8(xr));
  const u32 tag = mktag(epoch);
  STAMP(1);
  {
    u32x4 ax[4];
#pragma unroll
    for (int ks = 0; ks < 4; ++ks) ax[ks] = lds_ld<u32x4>(S, O_XN + ((s & 3) * 128 + 32 * ks + 8 * j4) * 2);
    const u32 gq = (q4 >> 1) | ((j4 & 1) << 1);
    u32 ad = O_WQ + (8 * j4 + q4) * 128 + (((2 * w + (p4 >> 1)) ^ (gq << 1)) * 16) + (p4 & 1) * 8;
    u32 a_qp = (WS_QP + ((g * 4 + (s & 3)) * 512 + (s >> 2) * 64 + w * 16 + n16) * 8) | msk(l < 16);
    asm volatile("" : "+v"(ad), "+v"(a_qp), "+v"(ax[0]), "+v"(ax[1]), "+v"(ax[2]), "+v"(ax[3]));
    wait_ge(S, F_LANDED, N_WQ);
    u32x2 bb[8];
#pragma unroll
    for (int i = 0; i < 8; ++i) bb[i] = lds_tr(S, ad + (i >> 1) * 4096 + (i & 1) * 512);
    f32x4 d = {0.f, 0.f, 0.f, 0.f};
#pragma unroll
    for (int ks = 0; ks < 4; ++ks) d = mfma32(ax[ks], u32x4{bb[2 * ks].x, bb[2 * ks].y, bb[2 * ks + 1].x, bb[2 * ks + 1].y}, d);
    stsc2(rws, a_qp, u32x2{u_of(d[0]), tag});
  }
  STAMP(2);
  u32x4 kf[8];
  {
    const u32 qoff = WS_QP + (g * 4 * 512 + w * 128 + 2 * l) * 8;
    wait_ge(S, F_LANDED, Q_AT > N_WQ + 8 * (3 + 1) ? Q_AT : Q_AT);
    u32x4 qg[4];
#pragma unroll
    for (int i = 0; i < 4; ++i) qg[i] = ld1(rws, qoff + i * 4096);
    wait_ge(S, F_LANDED, N_WQ + 8 * (w + 1));
#pragma unroll
    for (int i = 0; i < 8; ++i) kf[i] = lds_ld<u32x4>(S, O_K + (32 * w + 16 * (i >> 2) + n16) * 256 + (((4 * (i & 3) + j4) ^ n16) * 16));
_Pragma("unroll 1")
    for (int spin = 0; spin < SPIN_CAP && !__all(tok(qg[0], tag) & tok(qg[1], tag) & tok(qg[2], tag) & tok(qg[3], tag)); ++spin) {
      MEMCLOB();
#pragma unroll
      for (int i = 0; i < 4; ++i) qg[i] = ldsc(rws, qoff + i * 4096);
    }
    lds_st<u32>(S, O_QS + w * 256 + l * 4, pk((f_of(qg[0].x) + f_of(qg[1].x)) + (f_of(qg[2].x) + f_of(qg[3].x)), (f_of(qg[0].z) + f_of(qg[1].z)) + (f_of(qg[2].z) + f_of(qg[3].z))));
    if (l == 0) set_flag(S, F_Q + w, 1);
  }
  STAMP(3);
  wait4(S, F_Q);
  {
    u32x4 bq[4];
#pragma unroll
    for (int ks = 0; ks < 4; ++ks) bq[ks] = lds_ld<u32x4>(S, O_QS + (n16 & 3) * 256 + ks * 64 + j4 * 16);
    f32x4 d0 = {0.f, 0.f, 0.f, 0.f}, d1 = {0.f, 0.f, 0.f, 0.f};
#pragma unroll
    for (int ks = 0; ks < 4; ++ks) { d0 = mfma32(kf[ks], bq[ks], d0); d1 = mfma32(kf[4 + ks], bq[ks], d1); }
    constexpr float C2 = 0.08838834764831845f * 1.4426950408889634f;
    const float mw = xr_max(vmax(vmax3(vmax3(d0[0], d0[1], d0[2]), d0[3], d1[0]), vmax3(d1[1], d1[2], d1[3]))) * C2;
    float p[8];
#pragma unroll
    for (int i = 0; i < 4; ++i) { p[i] = ex2(fmaf(d0[i], C2, -mw)); p[4 + i] = ex2(fmaf(d1[i], C2, -mw)); }
    if (n16 < 4) {
      lds_st<u32x2>(S, O_P + n16 * 256 + (32 * w + 4 * j4) * 2, u32x2{pk(p[0], p[1]), pk(p[2], p[3])});
      lds_st<u32x2>(S, O_P + n16 * 256 + (32 * w + 16 + 4 * j4) * 2, u32x2{pk(p[4], p[5]), pk(p[6], p[7])});
    }
    const float ls = x32_sum(x16_sum(((p[0] + p[1]) + (p[2] + p[3])) + ((p[4] + p[5]) + (p[6] + p[7]))));
    if (l < 4) { lds_st<float>(S, O_MW + l * 16 + w * 4, mw); lds_st<float>(S, O_LW + l * 16 + w * 4, ls); }
    if (l == 0) set_flag(S, F_P + w, 1);
  }
  STAMP(4);
  wait4(S, F_P);
  u32 ooff, adw, a_sp;
  {
    u32x4 ap[4];
#pragma unroll
    for (int ks = 0; ks < 4; ++ks) ap[ks] = lds_ld<u32x4>(S, O_P + (n16 >> 2) * 256 + ks * 64 + j4 * 16);
    const f32x4 m4 = lds_ld<f32x4>(S, O_MW + j4 * 16), l4 = lds_ld<f32x4>(S, O_LW + j4 * 16);
    const float M = vmax(vmax3(m4[0], m4[1], m4[2]), m4[3]);
    float al[4];
#pragma unroll
    for (int ks = 0; ks < 4; ++ks) al[ks] = ex2(m4[ks] - M);
    const float L = (al[0] * l4[0] + al[1] * l4[1]) + (al[2] * l4[2] + al[3] * l4[3]);
    const u32 gv = q4 | ((j4 & 1) << 2);
    u32 ad0 = O_V + (8 * j4 + q4) * 256 + (((4 * w + (p4 >> 1)) ^ (gv << 1)) * 16) + (p4 & 1) * 8, ad1 = ad0 ^ 32;
    u32 a_op = WS_OP + ((g * 32 + (j4 * 4 + w) * 2 + (n16 >> 3)) * 32 + s) * 128 + (n16 & 7) * 16;
    u32 a_st = (WS_HB + (g * 32 + j4 * 8 + 2 * w + (n16 & 1)) * 1024 + HB_ST + s * 16) | msk(n16 < 2);
    ooff = WS_OP + (me * 32 + (l >> 1)) * 128 + (2 * w + (l & 1)) * 16;
    adw = O_WO + (4 * j4 + q4) * 1024 + (((16 * w + (p4 >> 1)) ^ (((4 * j4 + q4) & 7) << 1)) * 16) + (p4 & 1) * 8;
    a_sp = WS_SB + ((w * 16 + (l >> 2)) * 256 + me) * 128 + (l & 3) * 16;
    asm volatile("" : "+v"(ad0), "+v"(ad1), "+v"(a_op), "+v"(a_st), "+v"(ooff), "+v"(adw), "+v"(a_sp));
    float o0 = 0, o1 = 0;
#pragma unroll
    for (int ks = 0; ks < 4; ++ks) {
      if (ks == 3) {
        wait_ge(S, F_LANDED, ST_AT);
        stsc(rws, a_st, u32x4{u_of(M * 0.6931471805599453f), tag, u_of(L), tag});
      }
      wait_ge(S, F_LANDED, N_K + 8 * (ks + 1));
      const u32x2 a0 = lds_tr(S, ad0 + ks * 8192), a1 = lds_tr(S, ad0 + ks * 8192 + 1024);
      const u32x2 c0 = lds_tr(S, ad1 + ks * 8192), c1 = lds_tr(S, ad1 + ks * 8192 + 1024);
      const f32x4 d0 = mfma32(ap[ks], u32x4{a0.x, a0.y, a1.x, a1.y}, f32x4{0.f, 0.f, 0.f, 0.f});
      const f32x4 d1 = mfma32(ap[ks], u32x4{c0.x, c0.y, c1.x, c1.y}, f32x4{0.f, 0.f, 0.f, 0.f});
      o0 = fmaf(al[ks], d0[0], o0); o1 = fmaf(al[ks], d1[0], o1);
    }
    stsc(rws, a_op, u32x4{u_of(o0), tag, u_of(o1), tag});
  }
  STAMP(5);
  {
    wait_ge(S, F_OOK, 1);
    u32x4 ov = ld1(rws, ooff);
    wait_ge(S, F_LANDED, N_WO);
    u32x2 bw[8];
#pragma unroll
    for (int tt = 0; tt < 8; ++tt) bw[tt] = lds_tr(S, adw ^ (tt << 5));
    wait_ge(S, F_WOK, 1);
    const float wn = lds_ld<float>(S, O_WT + (l >> 1) * 4);
_Pragma("unroll 1")
    for (int spin = 0; spin < SPIN_CAP && !__all(tok(ov, tag)); ++spin) { MEMCLOB(); ov = ldsc(rws, ooff); }
    if (l == 0) set_flag(S, F_OG, 1);
    STAMP(6);
    float o0 = wn * f_of(ov.x), o1 = wn * f_of(ov.z);
    o0 += dpp<XOR2>(o0); o1 += dpp<XOR2>(o1); o0 += dpp<ROR4>(o0); o1 += dpp<ROR4>(o1); o0 += dpp<ROR8>(o0); o1 += dpp<ROR8>(o1);
    o0 = x32_sum(x16_sum(o0)); o1 = x32_sum(x16_sum(o1));
    const u32 pr = pk(o0, o1), po = __builtin_amdgcn_mov_dpp(pr, XOR1, 0xf, 0xf, true);
    if (l == 0) { MEMCLOB(); *(volatile LAS u32x4*)(S + O_AV + w * 16) = u32x4{pr, po, 1u, 1u}; MEMCLOB(); }
    u32x4 av;
#pragma unroll 1
    for (int spin = 0; spin < (1 << 22); ++spin) { av = *(volatile LAS u32x4*)(S + O_AV + j4 * 16); if (__all(av.z != 0)) break; }
    const u32x2 a = {av.x, av.y};
    f32x4 d[8];
#pragma unroll
    for (int tt = 0; tt < 8; ++tt) d[tt] = mfma16(a, bw[tt], f32x4{0.f, 0.f, 0.f, 0.f});
    const bool b0 = j4 & 1, b1_ = j4 & 2;
    const float e0 = b1_ ? (b0 ? d[6][0] : d[4][0]) : (b0 ? d[2][0] : d[0][0]), e1 = b1_ ? (b0 ? d[7][0] : d[5][0]) : (b0 ? d[3][0] : d[1][0]);
    stsc(rws, a_sp, u32x4{u_of(e0), tag, u_of(e1), tag});
  }
  STAMP(7);
  if (b < NC) {
    u32 off0 = WS_SB + (b * 256 + 64 * w + (l >> 3)) * 128 + (l & 7) * 16;
    u32 offm = off0 | msk(!((l & 4) && w == 3));
    asm volatile("" : "+v"(off0), "+v"(offm));
    wait_ge(S, F_FOK, 1);
    u32x4 v[8];
#pragma unroll
    for (int i = 0; i < 8; ++i) v[i] = ld1(rws, (i < 4 ? off0 : offm) + i * 1024);
_Pragma("unroll 1")
    for (int spin = 0; spin < SPIN_CAP; ++spin) {
      bool ok = true;
#pragma unroll
      for (int i = 0; i < 8; ++i) ok = ok & (tok(v[i], tag) | (i >= 4 && offm >= OOB));
      if (__all(ok)) break;
      MEMCLOB();
#pragma unroll
      for (int i = 0; i < 8; ++i) v[i] = ldsc(rws, (i < 4 ? off0 : offm) + i * 1024);
    }
    STAMP(8);
    float r0 = ((f_of(v[0].x) + f_of(v[1].x)) + (f_of(v[2].x) + f_of(v[3].x))) + ((f_of(v[4].x) + f_of(v[5].x)) + (f_of(v[6].x) + f_of(v[7].x)));
    float r1 = ((f_of(v[0].z) + f_of(v[1].z)) + (f_of(v[2].z) + f_of(v[3].z))) + ((f_of(v[4].z) + f_of(v[5].z)) + (f_of(v[6].z) + f_of(v[7].z)));
    r0 += dpp<ROR8>(r0); r1 += dpp<ROR8>(r1);
    r0 = x32_sum(x16_sum(r0)); r1 = x32_sum(x16_sum(r1));
    if (l < 8) lds_st<f32x2>(S, O_FIN + w * 64 + l * 8, f32x2{r0, r1});
    if (l == 0) set_flag(S, F_F + w, 1);
    if (w == 0) {
      wait4(S, F_F);
      if (l < 8) {
        const u32 u = l & 3, e = l >> 2;
        float sv = 0, mv = 0;
#pragma unroll
        for (int ww = 0; ww < 4; ++ww) { sv += lds_ld<float>(S, O_FIN + ww * 64 + u * 8 + e * 4); mv += lds_ld<float>(S, O_FIN + ww * 64 + (4 + u) * 8 + e * 4); }
        const float B = rbf(rbf(mv) + f_of(b2h << 16));
        const float gt = rbf(B * rcp(1.f + ex2(-B * 1.4426950408889634f)));
        const float o = f_of(xfh << 16) + rbf(rbf(sv) * gt);
        out[fcol] = (u16)pk(o, o);
        if (b == 0 && l == 0) __builtin_amdgcn_raw_buffer_store_b32(epoch + 1u, rws, WS_EPOCH, 0, AUX_SC1);
      }
    }
  }
  STAMP(9);
  __builtin_amdgcn_endpgm();
  }

  __builtin_amdgcn_s_setprio(3);
  const u32 fl = (u32)(size_t)(S + O_FL + 4 * F_LANDED);
  asm volatile("" :: "s"(wq), "s"(kc), "s"(vc), "s"(w1), "s"(w2), "s"(wo), "s"(x), "s"(ws), "s"(b1), "s"(b2));
  STAMP(3);
  {
    __builtin_amdgcn_raw_ptr_buffer_load_lds(mk(x, 1024), (LAS void*)(S + O_X), 16, l * 16, 0, 0, 0);
    const u32 f0 = g * 256 + (s >> 2) * 32 + (s & 3) * 4, c0 = ((b & 63) >> 4) * 128 + ((b >> 2) & 3) * 32 + (b & 3) * 4;
    const char* p = (const char*)ws;
    if (l == 1 || l == 2) p = (const char*)b1 + (((hasM ? f0 : 0) + (l - 1) * 16) * 2 & ~15u);
    if (l == 3 || l == 4) p = (const char*)b2 + ((c0 + (l - 3) * 16) * 2 & ~15u);
    __builtin_amdgcn_global_load_lds((const __attribute__((address_space(1))) void*)p, (LAS void*)(S + O_MISC), 16, 0, 0);
  }
  {
    const rsrc_t rq = mk(wq, 512 * 4096 * 2);
    const u32 base = ((s & 3) * 128) * 8192 + (g * 512 + (s >> 2) * 64) * 2;
    const u32 v0 = (l >> 3) * 8192 + (((l & 7) ^ (((l >> 4) & 1) << 1)) * 16), v1 = (v0 ^ 64) + 64512;
#pragma unroll 1
    for (int G = 0; G < 4; ++G) dma4(rq, S, O_WQ + G * 4096, v0, v1, v0 + 2 * 64512, v1 + 2 * 64512, base + G * 4 * 65536);
  }
  const size_t kvoff = ((size_t)g * SK + (SK - WIN) + s * 128) * 256;
  {
    const rsrc_t rk = mk((const char*)kc + kvoff, 32768);
    const u32 v0 = j4 * 256 + ((n16 ^ j4) * 16);
#pragma unroll 1
    for (int G = 0; G < 8; ++G) {
      if (G == 1) landed<N_WQ + 4 - N_X>(fl, N_X);
      landed<WINDOW - 4>(fl, G >= 1 ? max(N_X, N_WQ + 4 * G - (WINDOW - 4)) : -1);
      dma4(rk, S, O_K + G * 4096, v0, v0 ^ 64, v0 ^ 128, v0 ^ 192, G * 4096);
    }
  }
  STAMP(4);
  {
    const rsrc_t rv = mk((const char*)vc + kvoff, 32768);
    const u32 v0 = j4 * 256 + ((n16 ^ (j4 << 1)) * 16);
#pragma unroll 1
    for (int G = 0; G < 8; ++G) { landed<WINDOW - 4>(fl, N_K + 4 * G - (WINDOW - 4)); dma4(rv, S, O_V + G * 4096, v0, v0, v0 ^ 128, v0 ^ 128, G * 4096); }
  }
  STAMP(5);
  {
    const rsrc_t r1 = mk(w1, hasM ? 512 * FF * 2 : 0);
    const u32 hb = l >> 5, bl = hb * (FF * 2) + (((l & 31) ^ (hb << 1)) * 16);
    const u32 base = (s * 16) * (FF * 2) + g * 512;
#pragma unroll 1
    for (int G = 0; G < 2; ++G) {
      landed<WINDOW2 - 4>(fl, N_V + 4 * G - (WINDOW2 - 4));
      dma4(r1, S, O_W1 + G * 4096, bl, (bl ^ 64) + 6144, (bl ^ 128) + 2 * 6144, (bl ^ 192) + 3 * 6144, base + G * 8 * (FF * 2));
    }
  }
  {
    const rsrc_t r2 = mk((const char*)w2 - 1024, hasM ? 512 * FF * 2 + 1024 : 0);
    const u32 f0 = g * 256 + (s >> 2) * 32 + (s & 3) * 4;
#pragma unroll 1
    for (int G = 0; G < 2; ++G) {
      landed<WINDOW2 - 4>(fl, N_W1 + 4 * G - (WINDOW2 - 4));
      const u32 v = (l * 16) ^ (G << 7);
      dma4(r2, S, O_W2 + G * 4096, v + 1024, (v ^ 32) + 16384, v ^ 64, (v ^ 96) + 15360, (f0 + 2 * G) * 1024);
    }
  }
  {
    const rsrc_t ro = mk((const char*)wo - 1024, 4096 * 512 * 2 + 1024);
    const u32 r0 = g * 512 + (s >> 3) * 128 + ((s >> 1) & 3) * 32 + (s & 1) * 8;
#pragma unroll 1
    for (int G = 0; G < 4; ++G) {
      landed<WINDOW2 - 4>(fl, N_W2 + 4 * G - (WINDOW2 - 4));
      const u32 v = (l * 16) ^ ((G & 1) << 7);
      dma4(ro, S, O_WO + G * 4096, v + 1024, (v ^ 32) + 16384, v ^ 64, (v ^ 96) + 15360, (r0 + 2 * G) * 1024);
    }
  }
  STAMP(1);
  landed<8>(fl, N_WO - 8); landed<4>(fl, N_WO - 4); landed<0>(fl, N_WO);
  STAMP(2);
}

extern "C" void kernel_launch(void* const* d_in, const int* in_sizes, int n_in, void* d_out, int out_size, void* d_ws, size_t ws_size, hipStream_t stream) {
  fused_decode<<<NWG, NT, 0, stream>>>((const u16*)d_in[0], (const u16*)d_in[1], (const u16*)d_in[2], (const u16*)d_in[3], (const u16*)d_in[4],
                                       (const u16*)d_in[6], (const u16*)d_in[7], (const u16*)d_in[8], (const u16*)d_in[9], (u16*)d_out, (unsigned char*)d_ws);
}
```

```cpp
#include <hip/hip_runtime.h>
#include <stdint.h>

typedef unsigned short u16;
typedef unsigned u32;
typedef unsigned long long u64;
typedef u32 u32x4 __attribute__((ext_vector_type(4)));
typedef u32 u32x2 __attribute__((ext_vector_type(2)));
typedef float f32x4 __attribute__((ext_vector_type(4)));
typedef float f32x2 __attribute__((ext_vector_type(2)));
typedef short s16x4 __attribute__((ext_vector_type(4)));
typedef __bf16 bf16x8 __attribute__((ext_vector_type(8)));
typedef __bf16 bf16x2 __attribute__((ext_vector_type(2)));
typedef __amdgpu_buffer_rsrc_t rsrc_t;
#define LAS __attribute__((address_space(3)))

constexpr int SK = 65536, WIN = 4096, FF = 1792;
constexpr int NWG = 256, NT = 640;
constexpr int NC = 64;
constexpr u32 WS_EPOCH = 0;
constexpr u32 WS_QP = 4096;
constexpr u32 WS_HB = WS_QP + 8 * 4 * 512 * 8;
constexpr u32 HB_ST = 0, HB_HH = 512, HB_FH = 768;
constexpr u32 WS_OP = WS_HB + 256 * 1024;
constexpr u32 WS_HP = WS_OP + 256 * 32 * 128;
constexpr u32 WS_SB = WS_HP + 224 * 32 * 64;
constexpr u32 WS_END = WS_SB + NC * 256 * 128;
constexpr u32 OOB = 0x80000000u;
constexpr u32 O_WQ = 0, O_K = 16384, O_V = 49152, O_W1 = 81920, O_W2 = 90112, O_WO = 98304, O_S = 114688;
constexpr u32 O_X = O_S, O_MISC = O_X + 1024, O_S2 = O_MISC + 1024;
constexpr u32 O_XN = O_S2, O_Y = O_XN + 1024, O_QS = O_Y + 1024, O_P = O_QS + 1024, O_MW = O_P + 1024, O_LW = O_MW + 64, O_AV = O_LW + 64,
              O_WT = O_AV + 64, O_HV = O_WT + 128, O_FIN = O_HV + 64, O_FL = O_FIN + 256, LDS_BYTES = O_FL + 128;
constexpr u32 O_INIT = O_AV;
enum { F_LANDED = 0, F_OOK = 2, F_HOK = 3, F_FOK = 4, F_WOK = 5, F_OG = 6, F_Q = 12, F_P = 16, F_F = 24 };
constexpr int N_X = 2, N_WQ = N_X + 16, N_K = N_WQ + 32, N_V = N_K + 32, N_W1 = N_V + 8, N_W2 = N_W1 + 8, N_WO = N_W2 + 16;
#define WINDOW 20
#define WINDOW2 16
#define Q_AT (N_WQ + 24)
#define ST_AT (N_V - 4)
#define HH_AT N_V
#define POLL_AT N_V
#define CODE_BYTES 11520
constexpr int AUX_NT = 2, AUX_SC1 = 16;
constexpr int SPIN_CAP = 1 << 14;

#define KEEP(x) asm volatile("" :: "v"(x))
#define MEMCLOB() asm volatile("" ::: "memory")
#define STAMP(k) do { } while (0)

__device__ __forceinline__ rsrc_t mk(const void* p, u32 bytes) { return __builtin_amdgcn_make_buffer_rsrc((void*)p, 0, bytes, 0x00020000); }
__device__ __forceinline__ u32x4 ldsc(rsrc_t r, u32 voff) { return __builtin_amdgcn_raw_buffer_load_b128(r, voff, 0, AUX_SC1); }
#define FIRST_AUX 0
__device__ __forceinline__ u32x4 ld1(rsrc_t r, u32 voff) { return __builtin_amdgcn_raw_buffer_load_b128(r, voff, 0, FIRST_AUX); }
__device__ __forceinline__ void stsc(rsrc_t r, u32 voff, u32x4 v) { __builtin_amdgcn_raw_buffer_store_b128(v, r, voff, 0, AUX_SC1); }
__device__ __forceinline__ void stsc2(rsrc_t r, u32 voff, u32x2 v) { __builtin_amdgcn_raw_buffer_store_b64(v, r, voff, 0, AUX_SC1); }
__device__ __forceinline__ float f_of(u32 u) { return __builtin_bit_cast(float, u); }
__device__ __forceinline__ u32 u_of(float f) { return __builtin_bit_cast(u32, f); }
__device__ __forceinline__ float lo16(u32 w) { return f_of(w << 16); }
__device__ __forceinline__ float hi16(u32 w) { return f_of(w & 0xffff0000u); }
__device__ __forceinline__ u32 pk(float lo, float hi) { bf16x2 r = {(__bf16)lo, (__bf16)hi}; return __builtin_bit_cast(u32, r); }
__device__ __forceinline__ float rbf(float f) { return lo16(pk(f, f)); }
template <int CTRL> __device__ __forceinline__ float dpp(float x) { return f_of(__builtin_amdgcn_mov_dpp(u_of(x), CTRL, 0xf, 0xf, true)); }
constexpr int XOR1 = 0xB1, XOR2 = 0x4E, HMIR = 0x141, MIR = 0x140, ROR4 = 0x124, ROR8 = 0x128;
__device__ __forceinline__ float x16_sum(float x) { auto s = __builtin_amdgcn_permlane16_swap(u_of(x), u_of(x), false, false); return f_of(s[0]) + f_of(s[1]); }
__device__ __forceinline__ float x32_sum(float x) { auto s = __builtin_amdgcn_permlane32_swap(u_of(x), u_of(x), false, false); return f_of(s[0]) + f_of(s[1]); }
__device__ __forceinline__ float x16_max(float x) { auto s = __builtin_amdgcn_permlane16_swap(u_of(x), u_of(x), false, false); return fmaxf(f_of(s[0]), f_of(s[1])); }
__device__ __forceinline__ float x32_max(float x) { auto s = __builtin_amdgcn_permlane32_swap(u_of(x), u_of(x), false, false); return fmaxf(f_of(s[0]), f_of(s[1])); }
__device__ __forceinline__ float row_sum(float x) { x += dpp<XOR1>(x); x += dpp<XOR2>(x); x += dpp<HMIR>(x); x += dpp<MIR>(x); return x; }
__device__ __forceinline__ float wave_sum(float x) { return x32_sum(x16_sum(row_sum(x))); }
__device__ __forceinline__ float vmax(float a, float b) { float r; asm("v_max_f32 %0, %1, %2" : "=v"(r) : "v"(a), "v"(b)); return r; }
__device__ __forceinline__ float vmax3(float a, float b, float c) { float r; asm("v_max3_f32 %0, %1, %2, %3" : "=v"(r) : "v"(a), "v"(b), "v"(c)); return r; }
#define DPPMAX(x, ctrl) asm("s_nop 1\n\tv_max_f32_dpp %0, %0, %0 " ctrl " row_mask:0xf bank_mask:0xf" : "+v"(x))
__device__ __forceinline__ float xr_max(float x) {
  auto s = __builtin_amdgcn_permlane16_swap(u_of(x), u_of(x), false, false); x = vmax(f_of(s[0]), f_of(s[1]));
  auto t = __builtin_amdgcn_permlane32_swap(u_of(x), u_of(x), false, false); return vmax(f_of(t[0]), f_of(t[1]));
}
__device__ __forceinline__ float par_sum(float x) { x += dpp<XOR2>(x); x += dpp<ROR4>(x); x += dpp<ROR8>(x); return x32_sum(x16_sum(x)); }
__device__ __forceinline__ float par_max(float x) { DPPMAX(x, "quad_perm:[2,3,0,1]"); DPPMAX(x, "row_ror:4"); DPPMAX(x, "row_ror:8"); return xr_max(x); }
__device__ __forceinline__ float ex2(float x) { return __builtin_amdgcn_exp2f(x); }
__device__ __forceinline__ float rcp(float x) { return __builtin_amdgcn_rcpf(x); }
__device__ __forceinline__ f32x4 mfma32(u32x4 a, u32x4 b, f32x4 c) {
  return __builtin_amdgcn_mfma_f32_16x16x32_bf16(__builtin_bit_cast(bf16x8, a), __builtin_bit_cast(bf16x8, b), c, 0, 0, 0);
}
__device__ __forceinline__ f32x4 mfma16(u32x2 a, u32x2 b, f32x4 c) {
  return __builtin_amdgcn_mfma_f32_16x16x16bf16_1k(__builtin_bit_cast(s16x4, a), __builtin_bit_cast(s16x4, b), c, 0, 0, 0);
}
typedef LAS unsigned char* lds_t;
template <class T> __device__ __forceinline__ T lds_ld(lds_t s, u32 off) { return *(const LAS T*)(s + off); }
template <class T> __device__ __forceinline__ void lds_st(lds_t s, u32 off, T v) { *(LAS T*)(s + off) = v; }
__device__ __forceinline__ u32x2 lds_tr(lds_t s, u32 off) { return __builtin_bit_cast(u32x2, __builtin_amdgcn_ds_read_tr16_b64_v4i16((LAS s16x4*)(s + off))); }
__device__ __forceinline__ void set_flag(lds_t s, int f, u32 v) { MEMCLOB(); *(volatile LAS u32*)(s + O_FL + 4 * f) = v; MEMCLOB(); }
__device__ __forceinline__ void wait_ge(lds_t s, int f, int need) {
#pragma unroll 1
  for (int spin = 0; spin < (1 << 22); ++spin) {
    const int v = __builtin_amdgcn_readfirstlane(*(volatile LAS int*)(s + O_FL + 4 * f));
    if (v >= need) break;
  }
  MEMCLOB();
}
__device__ __forceinline__ void wait4(lds_t s, int f) {
#pragma unroll 1
  for (int spin = 0; spin < (1 << 22); ++spin) {
    const u32x4 v = *(volatile LAS u32x4*)(s + O_FL + 4 * f);
    if (__builtin_amdgcn_readfirstlane(v.x & v.y & v.z & v.w)) break;
  }
  MEMCLOB();
}
__device__ __forceinline__ u32x4 ln8(u32x4 xr) {
  float v[8];
#pragma unroll
  for (int i = 0; i < 4; ++i) { v[2 * i] = lo16(xr[i]); v[2 * i + 1] = hi16(xr[i]); }
  const float s = ((v[0] + v[1]) + (v[2] + v[3])) + ((v[4] + v[5]) + (v[6] + v[7]));
  const float m = wave_sum(s) * (1.f / 512);
  float q0 = 0, q1 = 0;
#pragma unroll
  for (int i = 0; i < 4; ++i) { const float d0 = v[2 * i] - m, d1 = v[2 * i + 1] - m; q0 = fmaf(d0, d0, q0); q1 = fmaf(d1, d1, q1); }
  const float var = wave_sum(q0 + q1) * (1.f / 512);
  const float mb = rbf(m);
  const float inv = rcp(rbf(__builtin_amdgcn_sqrtf(rbf(rbf(var) + 1e-5f))));
  u32x4 r;
#pragma unroll
  for (int i = 0; i < 4; ++i) { const u32 d = pk(v[2 * i] - mb, v[2 * i + 1] - mb); r[i] = pk(lo16(d) * inv, hi16(d) * inv); }
  return r;
}
__device__ __forceinline__ u64 poll64(const unsigned char* p) {
  u64 r; const u64 z = 0;
  asm volatile("global_atomic_or_x2 %0, %1, %2, off sc0 sc1\n\ts_waitcnt vmcnt(0)" : "=&v"(r) : "v"(p), "v"(z) : "memory");
  return r;
}
__device__ __forceinline__ void dma4(rsrc_t r, lds_t s, u32 ldsoff, u32 v0, u32 v1, u32 v2, u32 v3, u32 soff) {
  __builtin_amdgcn_raw_ptr_buffer_load_lds(r, (LAS void*)(s + ldsoff), 16, v0, soff, 0, AUX_NT);
  __builtin_amdgcn_raw_ptr_buffer_load_lds(r, (LAS void*)(s + ldsoff), 16, v1, soff, 1024, AUX_NT);
  __builtin_amdgcn_raw_ptr_buffer_load_lds(r, (LAS void*)(s + ldsoff), 16, v2, soff, 2048, AUX_NT);
  __builtin_amdgcn_raw_ptr_buffer_load_lds(r, (LAS void*)(s + ldsoff), 16, v3, soff, 3072, AUX_NT);
}
template <int N> __device__ __forceinline__ void landed(u32 flag_addr, int v) {
  asm volatile("s_waitcnt vmcnt(%0)\n\tds_write_b32 %1, %2" :: "i"(N), "v"(flag_addr), "v"(v) : "memory");
}
__device__ __forceinline__ u32 msk(bool c) { return c ? 0u : OOB; }
__device__ __forceinline__ bool tok(u32x4 v, u32 tag) { return (v.y == tag) & (v.w == tag); }
__device__ __forceinline__ u32 mktag(u32 epoch) { return ((epoch + 1u) * 0x9E3779B1u) ^ 0x5bd1e995u; }

__global__ void __launch_bounds__(NT) fused_decode(const u16* __restrict__ x, const u16* __restrict__ kc, const u16* __restrict__ vc,
                                                   const u16* __restrict__ wq, const u16* __restrict__ wo, const u16* __restrict__ w1,
                                                   const u16* __restrict__ b1, const u16* __restrict__ w2, const u16* __restrict__ b2,
                                                   u16* __restrict__ out, unsigned char* __restrict__ ws) {
  __shared__ __attribute__((aligned(16))) unsigned char smem[LDS_BYTES];
  lds_t S = (lds_t)smem;
  STAMP(0);
  const int t = threadIdx.x, l = t & 63, wv = __builtin_amdgcn_readfirstlane(t >> 6);
  if (wv == 0) {
    if (l < 32) lds_st<u32>(S, O_FL + 4 * l, 0u); else if (l < 48) lds_st<u32>(S, O_AV + 4 * (l - 32), 0u);
    asm volatile("s_waitcnt lgkmcnt(0)" ::: "memory");
  }
  asm volatile("s_load_dword s94, %0, 0x0\n\ts_load_dword s95, %0, 0x40" :: "s"(__builtin_amdgcn_kernarg_segment_ptr()) : "s94", "s95", "memory");
  const rsrc_t rcode = mk((const void*)(__builtin_amdgcn_s_getpc() & ~255ull), CODE_BYTES);
  asm volatile("buffer_load_dwordx4 v[120:123], %0, %1, 0 offen\n\tbuffer_load_dwordx4 v[120:123], %2, %1, 0 offen"
               :: "v"(l * 16 + (wv + 1) * 1024), "s"(rcode), "v"(l * 16 + (wv + 11) * 1024) : "v120", "v121", "v122", "v123", "memory");
  asm volatile("s_barrier" ::: "memory");
  const int b = blockIdx.x, g = b & 7, s = b >> 3;
  const int n16 = l & 15, j4 = l >> 4, q4 = (l >> 2) & 3, p4 = l & 3;
  const bool hasM = g < 7;
  const int me = g * 32 + s;

  if (wv != 8) {
  asm volatile("" :: "s"(ws), "s"(out));
  const rsrc_t rws = mk(ws, WS_END);

  if (wv == 9) {
    const u32x4 warm = ldsc(rws, WS_HB + me * 1024 + l * 16);
    const u32x4 warm2 = ldsc(rws, (WS_HB + b * 1024 + HB_FH + l * 16) | msk(b < NC && l < 16));
    const unsigned char* ps = ws + WS_HB + me * 1024 + HB_ST + l * 8;
    KEEP(warm); KEEP(warm2);
    wait_ge(S, F_LANDED, N_X);
    const u32 tag = mktag(lds_ld<u32>(S, O_MISC));
    wait_ge(S, F_LANDED, POLL_AT);
_Pragma("unroll 1")
    for (int spin = 0; spin < SPIN_CAP; ++spin) {
      const u64 rs = poll64(ps);
      if (__all((u32)(rs >> 32) == tag)) {
        set_flag(S, F_OOK, 1);
        const float v = f_of((u32)rs), o = dpp<XOR1>(v);
        const float mp = (l & 1) ? o : v, lp = (l & 1) ? v : o;
        const float wp = ex2((mp - par_max(mp)) * 1.4426950408889634f);
        const float wn = wp * rcp(par_sum(wp * lp));
        if ((l & 1) == 0) lds_st<float>(S, O_WT + (l >> 1) * 4, wn);
        break;
      }
    }
    set_flag(S, F_OOK, 1); set_flag(S, F_WOK, 1);
    STAMP(1);
    if ((s & 7) == 0) {
      wait_ge(S, F_OG, 1);
      stsc2(rws, WS_HB + l * 1024 + HB_FH + (g * 4 + (s >> 3)) * 8, u32x2{tag, tag});
    }
    if (b < NC) {
      const unsigned char* p = ws + WS_HB + b * 1024 + HB_FH + (l & 31) * 8;
_Pragma("unroll 1")
      for (int spin = 0; spin < SPIN_CAP; ++spin) if (__all((u32)(poll64(p) >> 32) == tag)) break;
      set_flag(S, F_FOK, 1);
    }
    STAMP(2);
    __builtin_amdgcn_endpgm();
  }

  if (wv >= 4 && !hasM) __builtin_amdgcn_endpgm();
  wait_ge(S, F_LANDED, N_X);
  const u32x4 xr = lds_ld<u32x4>(S, O_X + l * 16);
  const u32 epoch = lds_ld<u32>(S, O_MISC);

  if (wv >= 4) {
    const int w = wv - 4;
    const u32 f0 = g * 256 + (s >> 2) * 32 + (s & 3) * 4;
    const u32 b1v = lds_ld<u16>(S, O_MISC + 16 + ((l & 4) ? 16 : 0) + ((f0 * 2) & 15) + p4 * 2);
    lds_st<u32x4>(S, O_Y + l * 16, ln8(ln8(xr)));
    const u32x2 ay = lds_ld<u32x2>(S, O_Y + (s * 16 + 4 * j4) * 2);
    const u32 tag = mktag(epoch);
    u32 a_hp = (WS_HP + ((g * 32 + (w * 2 + (j4 & 1)) * 4 + (n16 >> 2)) * 32 + s) * 64 + (n16 & 3) * 16) | msk(l < 32);
    u32 a_bp = WS_SB + ((w * 16 + (l >> 2)) * 256 + me) * 128 + 64 + (l & 3) * 16;
    u32 a_hh = (WS_HB + (g * 32 + w * 8 + (l & 7)) * 1024 + HB_HH + s * 8) | msk(l < 8);
    u32 hoff = WS_HP + (me * 32 + (l >> 2)) * 64 + (l & 3) * 16;
    u32 ad1 = O_W1 + (4 * j4 + q4) * 512 + (((8 * w + (p4 >> 1)) ^ (((4 * j4 + q4) & 7) << 1)) * 16) + (p4 & 1) * 8;
    u32 ad2 = O_W2 + ((4 * j4 + q4) & 7) * 1024 + (((16 * w + (p4 >> 1)) ^ (((4 * j4 + q4) & 7) << 1)) * 16) + (p4 & 1) * 8;
    float b1f = f_of(b1v << 16);
    asm volatile("" : "+v"(a_hp), "+v"(a_bp), "+v"(a_hh), "+v"(hoff), "+v"(ad1), "+v"(ad2), "+v"(b1f));
    wait_ge(S, F_LANDED, HH_AT);
    stsc2(rws, a_hh, u32x2{tag, tag});
    wait_ge(S, F_LANDED, N_W1);
    {
      f32x4 d[4];
#pragma unroll
      for (int tt = 0; tt < 4; ++tt) d[tt] = mfma16(ay, lds_tr(S, ad1 ^ (tt << 5)), f32x4{0.f, 0.f, 0.f, 0.f});
      const bool up = j4 & 1;
      stsc(rws, a_hp, u32x4{u_of(up ? d[2][0] : d[0][0]), tag, u_of(up ? d[3][0] : d[1][0]), tag});
    }
    STAMP(3);
    wait_ge(S, F_LANDED, N_W2);
    u32x2 bw[8];
#pragma unroll
    for (int tt = 0; tt < 8; ++tt) bw[tt] = lds_tr(S, ad2 ^ (tt << 5));
    if (w == 0) {
      const unsigned char* ph = ws + WS_HB + me * 1024 + HB_HH + (l & 31) * 8;
_Pragma("unroll 1")
      for (int spin = 0; spin < SPIN_CAP; ++spin) if (__all((u32)(poll64(ph) >> 32) == tag)) break;
      set_flag(S, F_HOK, 1);
    } else wait_ge(S, F_HOK, 1);
    u32x4 h0 = ld1(rws, hoff), h1 = ld1(rws, hoff + 16 * 64);
_Pragma("unroll 1")
    for (int spin = 0; spin < SPIN_CAP && !__all(tok(h0, tag) & tok(h1, tag)); ++spin) { MEMCLOB(); h0 = ldsc(rws, hoff); h1 = ldsc(rws, hoff + 16 * 64); }
    STAMP(4);
    {
      float ha = f_of(h0.x) + f_of(h1.x), hb = f_of(h0.z) + f_of(h1.z);
      ha += dpp<ROR4>(ha); hb += dpp<ROR4>(hb); ha += dpp<ROR8>(ha); hb += dpp<ROR8>(hb);
      ha = x32_sum(x16_sum(ha)); hb = x32_sum(x16_sum(hb));
      const float p = rbf(rbf((l & 4) ? hb : ha) + b1f);
      const float u = p * fmaf(0.044715f * 0.7978845608028654f * 2.8853900817779268f, p * p, 0.7978845608028654f * 2.8853900817779268f);
      const float h = p - p * rcp(ex2(u) + 1.f);
      if (l < 8) lds_st<u16>(S, O_HV + w * 16 + (p4 * 2 + (l >> 2)) * 2, (u16)pk(h, h));
    }
    u32x2 ah = lds_ld<u32x2>(S, O_HV + w * 16 + (j4 & 1) * 8);
    if (j4 >= 2) ah = u32x2{0u, 0u};
    {
      f32x4 d[8];
#pragma unroll
      for (int tt = 0; tt < 8; ++tt) d[tt] = mfma16(ah, bw[tt], f32x4{0.f, 0.f, 0.f, 0.f});
      const bool b0 = j4 & 1, b1_ = j4 & 2;
      const float e0 = b1_ ? (b0 ? d[6][0] : d[4][0]) : (b0 ? d[2][0] : d[0][0]), e1 = b1_ ? (b0 ? d[7][0] : d[5][0]) : (b0 ? d[3][0] : d[1][0]);
      stsc(rws, a_bp, u32x4{u_of(e0), tag, u_of(e1), tag});
    }
    STAMP(5);
    __builtin_amdgcn_endpgm();
  }

  const int w = wv;
  const int fcol = (b >> 4) * 128 + ((b >> 2) & 3) * 32 + (b & 3) * 4 + p4 + ((l >> 2) & 1) * 16;
  const u32 b2h = lds_ld<u16>(S, O_MISC + 48 + ((l & 4) ? 16 : 0) + (((b & 3) * 8) & 15) + p4 * 2);
  const u32 xfh = lds_ld<u16>(S, O_X + (fcol & 511) * 2);
  lds_st<u32x4>(S, O_XN + l * 16, ln8(xr));
  const u32 tag = mktag(epoch);
  STAMP(1);
  {
    u32x4 ax[4];
#pragma unroll
    for (int ks = 0; ks < 4; ++ks) ax[ks] = lds_ld<u32x4>(S, O_XN + ((s & 3) * 128 + 32 * ks + 8 * j4) * 2);
    const u32 gq = (q4 >> 1) | ((j4 & 1) << 1);
    u32 ad = O_WQ + (8 * j4 + q4) * 128 + (((2 * w + (p4 >> 1)) ^ (gq << 1)) * 16) + (p4 & 1) * 8;
    u32 a_qp = (WS_QP + ((g * 4 + (s & 3)) * 512 + (s >> 2) * 64 + w * 16 + n16) * 8) | msk(l < 16);
    asm volatile("" : "+v"(ad), "+v"(a_qp), "+v"(ax[0]), "+v"(ax[1]), "+v"(ax[2]), "+v"(ax[3]));
    wait_ge(S, F_LANDED, N_WQ);
    u32x2 bb[8];
#pragma unroll
    for (int i = 0; i < 8; ++i) bb[i] = lds_tr(S, ad + (i >> 1) * 4096 + (i & 1) * 512);
    f32x4 d = {0.f, 0.f, 0.f, 0.f};
#pragma unroll
    for (int ks = 0; ks < 4; ++ks) d = mfma32(ax[ks], u32x4{bb[2 * ks].x, bb[2 * ks].y, bb[2 * ks + 1].x, bb[2 * ks + 1].y}, d);
    stsc2(rws, a_qp, u32x2{u_of(d[0]), tag});
  }
  STAMP(2);
  u32x4 kf[8];
  {
    const u32 qoff = WS_QP + (g * 4 * 512 + w * 128 + 2 * l) * 8;
    wait_ge(S, F_LANDED, Q_AT > N_WQ + 8 * (3 + 1) ? Q_AT : Q_AT);
    u32x4 qg[4];
#pragma unroll
    for (int i = 0; i < 4; ++i) qg[i] = ld1(rws, qoff + i * 4096);
    wait_ge(S, F_LANDED, N_WQ + 8 * (w + 1));
#pragma unroll
    for (int i = 0; i < 8; ++i) kf[i] = lds_ld<u32x4>(S, O_K + (32 * w + 16 * (i >> 2) + n16) * 256 + (((4 * (i & 3) + j4) ^ n16) * 16));
_Pragma("unroll 1")
    for (int spin = 0; spin < SPIN_CAP && !__all(tok(qg[0], tag) & tok(qg[1], tag) & tok(qg[2], tag) & tok(qg[3], tag)); ++spin) {
      MEMCLOB();
#pragma unroll
      for (int i = 0; i < 4; ++i) qg[i] = ldsc(rws, qoff + i * 4096);
    }
    lds_st<u32>(S, O_QS + w * 256 + l * 4, pk((f_of(qg[0].x) + f_of(qg[1].x)) + (f_of(qg[2].x) + f_of(qg[3].x)), (f_of(qg[0].z) + f_of(qg[1].z)) + (f_of(qg[2].z) + f_of(qg[3].z))));
    if (l == 0) set_flag(S, F_Q + w, 1);
  }
  STAMP(3);
  wait4(S, F_Q);
  {
    u32x4 bq[4];
#pragma unroll
    for (int ks = 0; ks < 4; ++ks) bq[ks] = lds_ld<u32x4>(S, O_QS + (n16 & 3) * 256 + ks * 64 + j4 * 16);
    f32x4 d0 = {0.f, 0.f, 0.f, 0.f}, d1 = {0.f, 0.f, 0.f, 0.f};
#pragma unroll
    for (int ks = 0; ks < 4; ++ks) { d0 = mfma32(kf[ks], bq[ks], d0); d1 = mfma32(kf[4 + ks], bq[ks], d1); }
    constexpr float C2 = 0.08838834764831845f * 1.4426950408889634f;
    float sc[8];
#pragma unroll
    for (int i = 0; i < 4; ++i) { sc[i] = d0[i] * C2; sc[4 + i] = d1[i] * C2; }
    const float mw = xr_max(fmaxf(fmaxf(fmaxf(sc[0], sc[1]), fmaxf(sc[2], sc[3])), fmaxf(fmaxf(sc[4], sc[5]), fmaxf(sc[6], sc[7]))));
    float p[8];
#pragma unroll
    for (int i = 0; i < 8; ++i) p[i] = ex2(sc[i] - mw);
    if (n16 < 4) {
      lds_st<u32x2>(S, O_P + n16 * 256 + (32 * w + 4 * j4) * 2, u32x2{pk(p[0], p[1]), pk(p[2], p[3])});
      lds_st<u32x2>(S, O_P + n16 * 256 + (32 * w + 16 + 4 * j4) * 2, u32x2{pk(p[4], p[5]), pk(p[6], p[7])});
    }
    const float ls = x32_sum(x16_sum(((p[0] + p[1]) + (p[2] + p[3])) + ((p[4] + p[5]) + (p[6] + p[7]))));
    if (l < 4) { lds_st<float>(S, O_MW + l * 16 + w * 4, mw); lds_st<float>(S, O_LW + l * 16 + w * 4, ls); }
    if (l == 0) set_flag(S, F_P + w, 1);
  }
  STAMP(4);
  wait4(S, F_P);
  u32 ooff, adw, a_sp;
  {
    u32x4 ap[4];
#pragma unroll
    for (int ks = 0; ks < 4; ++ks) ap[ks] = lds_ld<u32x4>(S, O_P + (n16 >> 2) * 256 + ks * 64 + j4 * 16);
    const f32x4 m4 = lds_ld<f32x4>(S, O_MW + j4 * 16), l4 = lds_ld<f32x4>(S, O_LW + j4 * 16);
    const float M = vmax(vmax3(m4[0], m4[1], m4[2]), m4[3]);
    float al[4];
#pragma unroll
    for (int ks = 0; ks < 4; ++ks) al[ks] = ex2(m4[ks] - M);
    const float L = (al[0] * l4[0] + al[1] * l4[1]) + (al[2] * l4[2] + al[3] * l4[3]);
    const u32 gv = q4 | ((j4 & 1) << 2);
    u32 ad0 = O_V + (8 * j4 + q4) * 256 + (((4 * w + (p4 >> 1)) ^ (gv << 1)) * 16) + (p4 & 1) * 8, ad1 = ad0 ^ 32;
    u32 a_op = WS_OP + ((g * 32 + (j4 * 4 + w) * 2 + (n16 >> 3)) * 32 + s) * 128 + (n16 & 7) * 16;
    u32 a_st = (WS_HB + (g * 32 + j4 * 8 + 2 * w + (n16 & 1)) * 1024 + HB_ST + s * 16) | msk(n16 < 2);
    ooff = WS_OP + (me * 32 + (l >> 1)) * 128 + (2 * w + (l & 1)) * 16;
    adw = O_WO + (4 * j4 + q4) * 1024 + (((16 * w + (p4 >> 1)) ^ (((4 * j4 + q4) & 7) << 1)) * 16) + (p4 & 1) * 8;
    a_sp = WS_SB + ((w * 16 + (l >> 2)) * 256 + me) * 128 + (l & 3) * 16;
    asm volatile("" : "+v"(ad0), "+v"(ad1), "+v"(a_op), "+v"(a_st), "+v"(ooff), "+v"(adw), "+v"(a_sp));
    float o0 = 0, o1 = 0;
#pragma unroll
    for (int ks = 0; ks < 4; ++ks) {
      if (ks == 3) {
        wait_ge(S, F_LANDED, ST_AT);
        stsc(rws, a_st, u32x4{u_of(M * 0.6931471805599453f), tag, u_of(L), tag});
      }
      wait_ge(S, F_LANDED, N_K + 8 * (ks + 1));
      const u32x2 a0 = lds_tr(S, ad0 + ks * 8192), a1 = lds_tr(S, ad0 + ks * 8192 + 1024);
      const u32x2 c0 = lds_tr(S, ad1 + ks * 8192), c1 = lds_tr(S, ad1 + ks * 8192 + 1024);
      const f32x4 d0 = mfma32(ap[ks], u32x4{a0.x, a0.y, a1.x, a1.y}, f32x4{0.f, 0.f, 0.f, 0.f});
      const f32x4 d1 = mfma32(ap[ks], u32x4{c0.x, c0.y, c1.x, c1.y}, f32x4{0.f, 0.f, 0.f, 0.f});
      o0 = fmaf(al[ks], d0[0], o0); o1 = fmaf(al[ks], d1[0], o1);
    }
    stsc(rws, a_op, u32x4{u_of(o0), tag, u_of(o1), tag});
  }
  STAMP(5);
  {
    wait_ge(S, F_OOK, 1);
    u32x4 ov = ld1(rws, ooff);
    wait_ge(S, F_LANDED, N_WO);
    u32x2 bw[8];
#pragma unroll
    for (int tt = 0; tt < 8; ++tt) bw[tt] = lds_tr(S, adw ^ (tt << 5));
    wait_ge(S, F_WOK, 1);
    const float wn = lds_ld<float>(S, O_WT + (l >> 1) * 4);
_Pragma("unroll 1")
    for (int spin = 0; spin < SPIN_CAP && !__all(tok(ov, tag)); ++spin) { MEMCLOB(); ov = ldsc(rws, ooff); }
    if (l == 0) set_flag(S, F_OG, 1);
    STAMP(6);
    float o0 = wn * f_of(ov.x), o1 = wn * f_of(ov.z);
    o0 += dpp<XOR2>(o0); o1 += dpp<XOR2>(o1); o0 += dpp<ROR4>(o0); o1 += dpp<ROR4>(o1); o0 += dpp<ROR8>(o0); o1 += dpp<ROR8>(o1);
    o0 = x32_sum(x16_sum(o0)); o1 = x32_sum(x16_sum(o1));
    const u32 pr = pk(o0, o1), po = __builtin_amdgcn_mov_dpp(pr, XOR1, 0xf, 0xf, true);
    if (l == 0) { MEMCLOB(); *(volatile LAS u32x4*)(S + O_AV + w * 16) = u32x4{pr, po, 1u, 1u}; MEMCLOB(); }
    u32x4 av;
#pragma unroll 1
    for (int spin = 0; spin < (1 << 22); ++spin) { av = *(volatile LAS u32x4*)(S + O_AV + j4 * 16); if (__all(av.z != 0)) break; }
    const u32x2 a = {av.x, av.y};
    f32x4 d[8];
#pragma unroll
    for (int tt = 0; tt < 8; ++tt) d[tt] = mfma16(a, bw[tt], f32x4{0.f, 0.f, 0.f, 0.f});
    const bool b0 = j4 & 1, b1_ = j4 & 2;
    const float e0 = b1_ ? (b0 ? d[6][0] : d[4][0]) : (b0 ? d[2][0] : d[0][0]), e1 = b1_ ? (b0 ? d[7][0] : d[5][0]) : (b0 ? d[3][0] : d[1][0]);
    stsc(rws, a_sp, u32x4{u_of(e0), tag, u_of(e1), tag});
  }
  STAMP(7);
  if (b < NC) {
    u32 off0 = WS_SB + (b * 256 + 64 * w + (l >> 3)) * 128 + (l & 7) * 16;
    u32 offm = off0 | msk(!((l & 4) && w == 3));
    asm volatile("" : "+v"(off0), "+v"(offm));
    wait_ge(S, F_FOK, 1);
    u32x4 v[8];
#pragma unroll
    for (int i = 0; i < 8; ++i) v[i] = ld1(rws, (i < 4 ? off0 : offm) + i * 1024);
_Pragma("unroll 1")
    for (int spin = 0; spin < SPIN_CAP; ++spin) {
      bool ok = true;
#pragma unroll
      for (int i = 0; i < 8; ++i) ok = ok & (tok(v[i], tag) | (i >= 4 && offm >= OOB));
      if (__all(ok)) break;
      MEMCLOB();
#pragma unroll
      for (int i = 0; i < 8; ++i) v[i] = ldsc(rws, (i < 4 ? off0 : offm) + i * 1024);
    }
    STAMP(8);
    float r0 = ((f_of(v[0].x) + f_of(v[1].x)) + (f_of(v[2].x) + f_of(v[3].x))) + ((f_of(v[4].x) + f_of(v[5].x)) + (f_of(v[6].x) + f_of(v[7].x)));
    float r1 = ((f_of(v[0].z) + f_of(v[1].z)) + (f_of(v[2].z) + f_of(v[3].z))) + ((f_of(v[4].z) + f_of(v[5].z)) + (f_of(v[6].z) + f_of(v[7].z)));
    r0 += dpp<ROR8>(r0); r1 += dpp<ROR8>(r1);
    r0 = x32_sum(x16_sum(r0)); r1 = x32_sum(x16_sum(r1));
    if (l < 8) lds_st<f32x2>(S, O_FIN + w * 64 + l * 8, f32x2{r0, r1});
    if (l == 0) set_flag(S, F_F + w, 1);
    if (w == 0) {
      wait4(S, F_F);
      if (l < 8) {
        const u32 u = l & 3, e = l >> 2;
        float sv = 0, mv = 0;
#pragma unroll
        for (int ww = 0; ww < 4; ++ww) { sv += lds_ld<float>(S, O_FIN + ww * 64 + u * 8 + e * 4); mv += lds_ld<float>(S, O_FIN + ww * 64 + (4 + u) * 8 + e * 4); }
        const float B = rbf(rbf(mv) + f_of(b2h << 16));
        const float gt = rbf(B * rcp(1.f + ex2(-B * 1.4426950408889634f)));
        const float o = f_of(xfh << 16) + rbf(rbf(sv) * gt);
        out[fcol] = (u16)pk(o, o);
        if (b == 0 && l == 0) __builtin_amdgcn_raw_buffer_store_b32(epoch + 1u, rws, WS_EPOCH, 0, AUX_SC1);
      }
    }
  }
  STAMP(9);
  __builtin_amdgcn_endpgm();
  }

  __builtin_amdgcn_s_setprio(3);
  const u32 fl = (u32)(size_t)(S + O_FL + 4 * F_LANDED);
  asm volatile("" :: "s"(wq), "s"(kc), "s"(vc), "s"(w1), "s"(w2), "s"(wo), "s"(x), "s"(ws), "s"(b1), "s"(b2));
  STAMP(3);
  {
    __builtin_amdgcn_raw_ptr_buffer_load_lds(mk(x, 1024), (LAS void*)(S + O_X), 16, l * 16, 0, 0, 0);
    const u32 f0 = g * 256 + (s >> 2) * 32 + (s & 3) * 4, c0 = ((b & 63) >> 4) * 128 + ((b >> 2) & 3) * 32 + (b & 3) * 4;
    const char* p = (const char*)ws;
    if (l == 1 || l == 2) p = (const char*)b1 + (((hasM ? f0 : 0) + (l - 1) * 16) * 2 & ~15u);
    if (l == 3 || l == 4) p = (const char*)b2 + ((c0 + (l - 3) * 16) * 2 & ~15u);
    __builtin_amdgcn_global_load_lds((const __attribute__((address_space(1))) void*)p, (LAS void*)(S + O_MISC), 16, 0, 0);
  }
  {
    const rsrc_t rq = mk(wq, 512 * 4096 * 2);
    const u32 base = ((s & 3) * 128) * 8192 + (g * 512 + (s >> 2) * 64) * 2;
    const u32 v0 = (l >> 3) * 8192 + (((l & 7) ^ (((l >> 4) & 1) << 1)) * 16), v1 = (v0 ^ 64) + 64512;
#pragma unroll 1
    for (int G = 0; G < 4; ++G) dma4(rq, S, O_WQ + G * 4096, v0, v1, v0 + 2 * 64512, v1 + 2 * 64512, base + G * 4 * 65536);
  }
  const size_t kvoff = ((size_t)g * SK + (SK - WIN) + s * 128) * 256;
  {
    const rsrc_t rk = mk((const char*)kc + kvoff, 32768);
    const u32 v0 = j4 * 256 + ((n16 ^ j4) * 16);
#pragma unroll 1
    for (int G = 0; G < 8; ++G) {
      if (G == 1) landed<N_WQ + 4 - N_X>(fl, N_X);
      landed<WINDOW - 4>(fl, G >= 1 ? max(N_X, N_WQ + 4 * G - (WINDOW - 4)) : -1);
      dma4(rk, S, O_K + G * 4096, v0, v0 ^ 64, v0 ^ 128, v0 ^ 192, G * 4096);
    }
  }
  STAMP(4);
  {
    const rsrc_t rv = mk((const char*)vc + kvoff, 32768);
    const u32 v0 = j4 * 256 + ((n16 ^ (j4 << 1)) * 16);
#pragma unroll 1
    for (int G = 0; G < 8; ++G) { landed<WINDOW - 4>(fl, N_K + 4 * G - (WINDOW - 4)); dma4(rv, S, O_V + G * 4096, v0, v0, v0 ^ 128, v0 ^ 128, G * 4096); }
  }
  STAMP(5);
  {
    const rsrc_t r1 = mk(w1, hasM ? 512 * FF * 2 : 0);
    const u32 hb = l >> 5, bl = hb * (FF * 2) + (((l & 31) ^ (hb << 1)) * 16);
    const u32 base = (s * 16) * (FF * 2) + g * 512;
#pragma unroll 1
    for (int G = 0; G < 2; ++G) {
      landed<WINDOW2 - 4>(fl, N_V + 4 * G - (WINDOW2 - 4));
      dma4(r1, S, O_W1 + G * 4096, bl, (bl ^ 64) + 6144, (bl ^ 128) + 2 * 6144, (bl ^ 192) + 3 * 6144, base + G * 8 * (FF * 2));
    }
  }
  {
    const rsrc_t r2 = mk((const char*)w2 - 1024, hasM ? 512 * FF * 2 + 1024 : 0);
    const u32 f0 = g * 256 + (s >> 2) * 32 + (s & 3) * 4;
#pragma unroll 1
    for (int G = 0; G < 2; ++G) {
      landed<WINDOW2 - 4>(fl, N_W1 + 4 * G - (WINDOW2 - 4));
      const u32 v = (l * 16) ^ (G << 7);
      dma4(r2, S, O_W2 + G * 4096, v + 1024, (v ^ 32) + 16384, v ^ 64, (v ^ 96) + 15360, (f0 + 2 * G) * 1024);
    }
  }
  {
    const rsrc_t ro = mk((const char*)wo - 1024, 4096 * 512 * 2 + 1024);
    const u32 r0 = g * 512 + (s >> 3) * 128 + ((s >> 1) & 3) * 32 + (s & 1) * 8;
#pragma unroll 1
    for (int G = 0; G < 4; ++G) {
      landed<WINDOW2 - 4>(fl, N_W2 + 4 * G - (WINDOW2 - 4));
      const u32 v = (l * 16) ^ ((G & 1) << 7);
      dma4(ro, S, O_WO + G * 4096, v + 1024, (v ^ 32) + 16384, v ^ 64, (v ^ 96) + 15360, (r0 + 2 * G) * 1024);
    }
  }
  STAMP(1);
  landed<8>(fl, N_WO - 8); landed<4>(fl, N_WO - 4); landed<0>(fl, N_WO);
  STAMP(2);
}

extern "C" void kernel_launch(void* const* d_in, const int* in_sizes, int n_in, void* d_out, int out_size, void* d_ws, size_t ws_size, hipStream_t stream) {
  fused_decode<<<NWG, NT, 0, stream>>>((const u16*)d_in[0], (const u16*)d_in[1], (const u16*)d_in[2], (const u16*)d_in[3], (const u16*)d_in[4],
                                       (const u16*)d_in[6], (const u16*)d_in[7], (const u16*)d_in[8], (const u16*)d_in[9], (u16*)d_out, (unsigned char*)d_ws);
}
```

```cpp
#include <hip/hip_runtime.h>
#include <stdint.h>

typedef unsigned short u16;
typedef unsigned u32;
typedef unsigned long long u64;
typedef u32 u32x4 __attribute__((ext_vector_type(4)));
typedef u32 u32x2 __attribute__((ext_vector_type(2)));
typedef float f32x4 __attribute__((ext_vector_type(4)));
typedef float f32x2 __attribute__((ext_vector_type(2)));
typedef short s16x4 __attribute__((ext_vector_type(4)));
typedef __bf16 bf16x8 __attribute__((ext_vector_type(8)));
typedef __bf16 bf16x2 __attribute__((ext_vector_type(2)));
typedef __amdgpu_buffer_rsrc_t rsrc_t;
#define LAS __attribute__((address_space(3)))

constexpr int SK = 65536, WIN = 4096, FF = 1792;
constexpr int NWG = 256, NT = 640;
constexpr int NC = 64;
constexpr u32 WS_EPOCH = 0;
constexpr u32 WS_QP = 4096;
constexpr u32 WS_HB = WS_QP + 8 * 4 * 256 * 8;
constexpr u32 HB_ST = 0, HB_HH = 512, HB_FH = 768;
constexpr u32 WS_OP = WS_HB + 256 * 1024;
constexpr u32 WS_HP = WS_OP + 256 * 32 * 128;
constexpr u32 WS_SB = WS_HP + 224 * 32 * 64;
constexpr u32 WS_END = WS_SB + NC * 256 * 64;
constexpr u32 OOB = 0x80000000u;
constexpr u32 O_WQ = 0, O_K = 16384, O_V = 49152, O_W1 = 81920, O_W2 = 90112, O_WO = 98304, O_S = 114688;
constexpr u32 O_X = O_S, O_MISC = O_X + 1024, O_S2 = O_MISC + 1024;
constexpr u32 O_XN = O_S2, O_Y = O_XN + 1024, O_QS = O_Y + 1024, O_P = O_QS + 1024, O_MW = O_P + 1024, O_LW = O_MW + 64, O_AV = O_LW + 64,
              O_WT = O_AV + 64, O_HV = O_WT + 128, O_FIN = O_HV + 64, O_BP = O_FIN + 512, O_FL = O_BP + 1024, LDS_BYTES = O_FL + 256;
constexpr u32 O_INIT = O_AV;
enum { F_LANDED = 0, F_OOK = 2, F_HOK = 3, F_FOK = 4, F_WOK = 5, F_OG = 6, F_Q = 12, F_P = 16, F_WQ = 20, F_F = 24, F_B = 28 };
constexpr int N_X = 2, N_W1 = N_X + 8, N_K = N_W1 + 32, N_V = N_K + 32, N_W2 = N_V + 8, N_WO = N_W2 + 16;
#define WINDOW 24
#define WINDOW2 16
#define Q_AT (N_X + 20)
#define ST_AT (N_V - 4)
#define HH_AT N_V
#define K_T 100
#define Q_T 240
#define H_T 330
#define POLL_AT N_V
#define CODE_BYTES 11520
constexpr int AUX_NT = 2, AUX_SC1 = 16;
constexpr int SPIN_CAP = 1 << 14;

#define DIAG(x)
#define KEEP(x) asm volatile("" :: "v"(x))
#define MEMCLOB() asm volatile("" ::: "memory")
#define STAMP(k) do { } while (0)

__device__ __forceinline__ rsrc_t mk(const void* p, u32 bytes) { return __builtin_amdgcn_make_buffer_rsrc((void*)p, 0, bytes, 0x00020000); }
__device__ __forceinline__ u32x4 ldsc(rsrc_t r, u32 voff) { return __builtin_amdgcn_raw_buffer_load_b128(r, voff, 0, AUX_SC1); }
#define FIRST_AUX 16
__device__ __forceinline__ u32x4 ld1(rsrc_t r, u32 voff) { return __builtin_amdgcn_raw_buffer_load_b128(r, voff, 0, FIRST_AUX); }
__device__ __forceinline__ void stsc(rsrc_t r, u32 voff, u32x4 v) { __builtin_amdgcn_raw_buffer_store_b128(v, r, voff, 0, AUX_SC1); }
__device__ __forceinline__ void stsc2(rsrc_t r, u32 voff, u32x2 v) { __builtin_amdgcn_raw_buffer_store_b64(v, r, voff, 0, AUX_SC1); }
__device__ __forceinline__ float f_of(u32 u) { return __builtin_bit_cast(float, u); }
__device__ __forceinline__ u32 u_of(float f) { return __builtin_bit_cast(u32, f); }
__device__ __forceinline__ float lo16(u32 w) { return f_of(w << 16); }
__device__ __forceinline__ float hi16(u32 w) { return f_of(w & 0xffff0000u); }
__device__ __forceinline__ u32 pk(float lo, float hi) { bf16x2 r = {(__bf16)lo, (__bf16)hi}; return __builtin_bit_cast(u32, r); }
__device__ __forceinline__ float rbf(float f) { return lo16(pk(f, f)); }
template <int CTRL> __device__ __forceinline__ float dpp(float x) { return f_of(__builtin_amdgcn_mov_dpp(u_of(x), CTRL, 0xf, 0xf, true)); }
constexpr int XOR1 = 0xB1, XOR2 = 0x4E, HMIR = 0x141, MIR = 0x140, ROR4 = 0x124, ROR8 = 0x128;
__device__ __forceinline__ float x16_sum(float x) { auto s = __builtin_amdgcn_permlane16_swap(u_of(x), u_of(x), false, false); return f_of(s[0]) + f_of(s[1]); }
__device__ __forceinline__ float x32_sum(float x) { auto s = __builtin_amdgcn_permlane32_swap(u_of(x), u_of(x), false, false); return f_of(s[0]) + f_of(s[1]); }
__device__ __forceinline__ float x16_max(float x) { auto s = __builtin_amdgcn_permlane16_swap(u_of(x), u_of(x), false, false); return fmaxf(f_of(s[0]), f_of(s[1])); }
__device__ __forceinline__ float x32_max(float x) { auto s = __builtin_amdgcn_permlane32_swap(u_of(x), u_of(x), false, false); return fmaxf(f_of(s[0]), f_of(s[1])); }
__device__ __forceinline__ float row_sum(float x) { x += dpp<XOR1>(x); x += dpp<XOR2>(x); x += dpp<HMIR>(x); x += dpp<MIR>(x); return x; }
__device__ __forceinline__ float wave_sum(float x) { return x32_sum(x16_sum(row_sum(x))); }
__device__ __forceinline__ float vmax(float a, float b) { float r; asm("v_max_f32 %0, %1, %2" : "=v"(r) : "v"(a), "v"(b)); return r; }
__device__ __forceinline__ float vmax3(float a, float b, float c) { float r; asm("v_max3_f32 %0, %1, %2, %3" : "=v"(r) : "v"(a), "v"(b), "v"(c)); return r; }
#define DPPMAX(x, ctrl) asm("s_nop 1\n\tv_max_f32_dpp %0, %0, %0 " ctrl " row_mask:0xf bank_mask:0xf" : "+v"(x))
__device__ __forceinline__ float xr_max(float x) {
  auto s = __builtin_amdgcn_permlane16_swap(u_of(x), u_of(x), false, false); x = vmax(f_of(s[0]), f_of(s[1]));
  auto t = __builtin_amdgcn_permlane32_swap(u_of(x), u_of(x), false, false); return vmax(f_of(t[0]), f_of(t[1]));
}
__device__ __forceinline__ float par_sum(float x) { x += dpp<XOR2>(x); x += dpp<ROR4>(x); x += dpp<ROR8>(x); return x32_sum(x16_sum(x)); }
__device__ __forceinline__ float par_max(float x) { DPPMAX(x, "quad_perm:[2,3,0,1]"); DPPMAX(x, "row_ror:4"); DPPMAX(x, "row_ror:8"); return xr_max(x); }
__device__ __forceinline__ float ex2(float x) { return __builtin_amdgcn_exp2f(x); }
__device__ __forceinline__ float rcp(float x) { return __builtin_amdgcn_rcpf(x); }
__device__ __forceinline__ f32x4 mfma32(u32x4 a, u32x4 b, f32x4 c) {
  return __builtin_amdgcn_mfma_f32_16x16x32_bf16(__builtin_bit_cast(bf16x8, a), __builtin_bit_cast(bf16x8, b), c, 0, 0, 0);
}
__device__ __forceinline__ f32x4 mfma16(u32x2 a, u32x2 b, f32x4 c) {
  return __builtin_amdgcn_mfma_f32_16x16x16bf16_1k(__builtin_bit_cast(s16x4, a), __builtin_bit_cast(s16x4, b), c, 0, 0, 0);
}
typedef LAS unsigned char* lds_t;
template <class T> __device__ __forceinline__ T lds_ld(lds_t s, u32 off) { return *(const LAS T*)(s + off); }
template <class T> __device__ __forceinline__ void lds_st(lds_t s, u32 off, T v) { *(LAS T*)(s + off) = v; }
__device__ __forceinline__ u32x2 lds_tr(lds_t s, u32 off) { return __builtin_bit_cast(u32x2, __builtin_amdgcn_ds_read_tr16_b64_v4i16((LAS s16x4*)(s + off))); }
__device__ __forceinline__ void set_flag(lds_t s, int f, u32 v) { MEMCLOB(); *(volatile LAS u32*)(s + O_FL + 4 * f) = v; MEMCLOB(); }
__device__ __forceinline__ void wait_ge(lds_t s, int f, int need) {
#pragma unroll 1
  for (int spin = 0; spin < (1 << 22); ++spin) {
    const int v = __builtin_amdgcn_readfirstlane(*(volatile LAS int*)(s + O_FL + 4 * f));
    if (v >= need) break;
  }
  MEMCLOB();
}
__device__ __forceinline__ void wait_landed(lds_t s, int& seen, int need) {
  if (seen < need) {
#pragma unroll 1
    for (int spin = 0; spin < (1 << 22); ++spin) {
      seen = __builtin_amdgcn_readfirstlane(*(volatile LAS int*)(s + O_FL + 4 * F_LANDED));
      if (seen >= need) break;
    }
  }
  MEMCLOB();
}
__device__ __forceinline__ bool all4(u32x4 f) { return __builtin_amdgcn_readfirstlane(f.x & f.y & f.z & f.w) != 0; }
__device__ __forceinline__ void wait4(lds_t s, int f) {
#pragma unroll 1
  for (int spin = 0; spin < (1 << 22); ++spin) {
    const u32x4 v = *(volatile LAS u32x4*)(s + O_FL + 4 * f);
    if (__builtin_amdgcn_readfirstlane(v.x & v.y & v.z & v.w)) break;
  }
  MEMCLOB();
}
__device__ __forceinline__ u32x4 ln8(u32x4 xr) {
  float v[8];
#pragma unroll
  for (int i = 0; i < 4; ++i) { v[2 * i] = lo16(xr[i]); v[2 * i + 1] = hi16(xr[i]); }
  const float s = ((v[0] + v[1]) + (v[2] + v[3])) + ((v[4] + v[5]) + (v[6] + v[7]));
  const float m = wave_sum(s) * (1.f / 512);
  float q0 = 0, q1 = 0;
#pragma unroll
  for (int i = 0; i < 4; ++i) { const float d0 = v[2 * i] - m, d1 = v[2 * i + 1] - m; q0 = fmaf(d0, d0, q0); q1 = fmaf(d1, d1, q1); }
  const float var = wave_sum(q0 + q1) * (1.f / 512);
  const float mb = rbf(m);
  const float inv = rcp(rbf(__builtin_amdgcn_sqrtf(rbf(rbf(var) + 1e-5f))));
  u32x4 r;
#pragma unroll
  for (int i = 0; i < 4; ++i) { const u32 d = pk(v[2 * i] - mb, v[2 * i + 1] - mb); r[i] = pk(lo16(d) * inv, hi16(d) * inv); }
  return r;
}
__device__ __forceinline__ u64 poll64(const unsigned char* p) {
  u64 r; const u64 z = 0;
  asm volatile("global_atomic_or_x2 %0, %1, %2, off sc0 sc1\n\ts_waitcnt vmcnt(0)" : "=&v"(r) : "v"(p), "v"(z) : "memory");
  return r;
}
__device__ __forceinline__ u32 poll_until(const unsigned char* p, u32 tag, int& count) {
  u32 lo; int n;
  asm volatile(
    "v_mov_b32 v108, 0\n\tv_mov_b32 v109, 0\n\t"
    "global_atomic_or_x2 v[104:105], %2, v[108:109], off sc0 sc1\n\t"
    "s_sleep 5\n\t"
    "global_atomic_or_x2 v[106:107], %2, v[108:109], off sc0 sc1\n\t"
    "s_mov_b32 %1, 0\n"
    "1:\n\t"
    "s_waitcnt vmcnt(1)\n\t"
    "v_cmp_ne_u32 vcc, %3, v105\n\t"
    "s_cbranch_vccz 2f\n\t"
    "global_atomic_or_x2 v[104:105], %2, v[108:109], off sc0 sc1\n\t"
    "s_waitcnt vmcnt(1)\n\t"
    "v_cmp_ne_u32 vcc, %3, v107\n\t"
    "s_cbranch_vccz 3f\n\t"
    "global_atomic_or_x2 v[106:107], %2, v[108:109], off sc0 sc1\n\t"
    "s_add_u32 %1, %1, 1\n\t"
    "s_cmp_lt_u32 %1, %4\n\t"
    "s_cbranch_scc1 1b\n"
    "2:\n\t"
    "v_mov_b32 %0, v104\n\t"
    "s_branch 4f\n"
    "3:\n\t"
    "v_mov_b32 %0, v106\n"
    "4:"
    : "=&v"(lo), "=&s"(n) : "v"(p), "v"(tag), "s"(SPIN_CAP) : "vcc", "scc", "v104", "v105", "v106", "v107", "v108", "v109", "memory");
  DIAG(count += n);
  return lo;
}
__device__ __forceinline__ void wait_until(u32 t) { while ((int)((u32)__builtin_amdgcn_s_memrealtime() - t) < 0) { } }
__device__ __forceinline__ void dma4(rsrc_t r, lds_t s, u32 ldsoff, u32 v0, u32 v1, u32 v2, u32 v3, u32 soff) {
  __builtin_amdgcn_raw_ptr_buffer_load_lds(r, (LAS void*)(s + ldsoff), 16, v0, soff, 0, AUX_NT);
  __builtin_amdgcn_raw_ptr_buffer_load_lds(r, (LAS void*)(s + ldsoff), 16, v1, soff, 1024, AUX_NT);
  __builtin_amdgcn_raw_ptr_buffer_load_lds(r, (LAS void*)(s + ldsoff), 16, v2, soff, 2048, AUX_NT);
  __builtin_amdgcn_raw_ptr_buffer_load_lds(r, (LAS void*)(s + ldsoff), 16, v3, soff, 3072, AUX_NT);
}
template <int N> __device__ __forceinline__ void landed(u32 flag_addr, int v) {
  asm volatile("s_waitcnt vmcnt(%0)\n\tds_write_b32 %1, %2" :: "i"(N), "v"(flag_addr), "v"(v) : "memory");
}
template <int N> __device__ __forceinline__ void drain(u32 flag_addr) {
  landed<N>(flag_addr, N_WO - N);
  if constexpr (N >= 4) drain<N - 4>(flag_addr);
}
__device__ __forceinline__ u32 msk(bool c) { return c ? 0u : OOB; }
__device__ __forceinline__ bool tok(u32x4 v, u32 tag) { return (v.y == tag) & (v.w == tag); }
__device__ __forceinline__ u32 mktag(u32 epoch) { return ((epoch + 1u) * 0x9E3779B1u) ^ 0x5bd1e995u; }

__global__ void __launch_bounds__(NT) fused_decode(const u16* __restrict__ x, const u16* __restrict__ kc, const u16* __restrict__ vc,
                                                   const u16* __restrict__ wq, const u16* __restrict__ wo, const u16* __restrict__ w1,
                                                   const u16* __restrict__ b1, const u16* __restrict__ w2, const u16* __restrict__ b2,
                                                   u16* __restrict__ out, unsigned char* __restrict__ ws) {
  __shared__ __attribute__((aligned(16))) unsigned char smem[LDS_BYTES];
  lds_t S = (lds_t)smem;
  STAMP(0);
  const u32 tstart = (u32)__builtin_amdgcn_s_memrealtime();
  const int t = threadIdx.x, l = t & 63, wv = __builtin_amdgcn_readfirstlane(t >> 6);
  if (wv == 0) {
    lds_st<u32>(S, O_FL + 4 * l, 0u); if (l < 16) lds_st<u32>(S, O_AV + 4 * l, 0u);
    asm volatile("s_waitcnt lgkmcnt(0)" ::: "memory");
  }
  asm volatile("s_load_dword s94, %0, 0x0\n\ts_load_dword s95, %0, 0x40" :: "s"(__builtin_amdgcn_kernarg_segment_ptr()) : "s94", "s95", "memory");
  const rsrc_t rcode = mk((const void*)(__builtin_amdgcn_s_getpc() & ~255ull), CODE_BYTES);
  asm volatile("buffer_load_dwordx4 v[120:123], %0, %1, 0 offen\n\tbuffer_load_dwordx4 v[120:123], %2, %1, 0 offen"
               :: "v"(l * 16 + (wv + 1) * 1024), "s"(rcode), "v"(l * 16 + (wv + 11) * 1024) : "v120", "v121", "v122", "v123", "memory");
  asm volatile("s_barrier" ::: "memory");
  const int b = blockIdx.x, g = b & 7, s = b >> 3;
  const int n16 = l & 15, j4 = l >> 4, q4 = (l >> 2) & 3, p4 = l & 3;
  const bool hasM = g < 7;
  const int me = g * 32 + s;
  const rsrc_t rws = mk(ws, WS_END);
  if (wv < 8) {
    const rsrc_t rx = mk(x, 1024);
    asm volatile("buffer_load_dwordx4 v[110:113], %0, %1, 0 offen\n\tbuffer_load_dword v114, off, %2, 0"
                 :: "v"(l * 16), "s"(rx), "s"(rws) : "v110", "v111", "v112", "v113", "v114", "memory");
    if (wv < 4) {
      const rsrc_t rq = mk(wq, 512 * 4096 * 2);
      const u32 base = ((s & 3) * 128 + 32 * wv) * 8192 + (g * 512 + (s >> 2) * 64) * 2;
      const u32 v0 = (l >> 3) * 8192 + (((l & 7) ^ (((l >> 4) & 1) << 1)) * 16), v1 = (v0 ^ 64) + 64512;
      const u32 m = (u32)(size_t)(S + O_WQ) + wv * 4096;
      asm volatile("s_mov_b32 m0, %0\n\ts_nop 0\n\t"
                   "buffer_load_dwordx4 %1, %5, %6 offen nt lds\n\tbuffer_load_dwordx4 %2, %5, %6 offen offset:1024 nt lds\n\t"
                   "buffer_load_dwordx4 %3, %5, %6 offen offset:2048 nt lds\n\tbuffer_load_dwordx4 %4, %5, %6 offen offset:3072 nt lds"
                   :: "s"(m), "v"(v0), "v"(v1), "v"(v0 + 2 * 64512), "v"(v1 + 2 * 64512), "s"(rq), "s"(base) : "memory");
    }
  }

  if (wv != 8) {
  asm volatile("" :: "s"(out));

  if (wv == 9) {
    int c1 = 0, c2 = 0;
    const u32x4 warm = ldsc(rws, WS_HB + me * 1024 + l * 16);
    const u32x4 warm2 = ldsc(rws, (WS_HB + b * 1024 + HB_FH + l * 16) | msk(b < NC && l < 16));
    const unsigned char* ps = ws + WS_HB + me * 1024 + HB_ST + l * 8;
    KEEP(warm); KEEP(warm2);
    wait_ge(S, F_LANDED, N_X);
    const u32 tag = mktag(lds_ld<u32>(S, O_MISC));
    wait_ge(S, F_LANDED, POLL_AT);
    {
      const float v = f_of(poll_until(ps, tag, c1));
      set_flag(S, F_OOK, 1);
      const float o = dpp<XOR1>(v);
      const float mp = (l & 1) ? o : v, lp = (l & 1) ? v : o;
      const float wp = ex2(mp - par_max(mp));
      const float wn = wp * rcp(par_sum(wp * lp));
      if ((l & 1) == 0) lds_st<float>(S, O_WT + (l >> 1) * 4, wn);
    }
    set_flag(S, F_WOK, 1);
    STAMP(1);
    if ((s & 7) == 0) {
      wait_ge(S, F_OG, 1);
      stsc2(rws, WS_HB + l * 1024 + HB_FH + (g * 4 + (s >> 3)) * 8, u32x2{tag, tag});
    }
    if (b < NC) {
      const unsigned char* p = ws + WS_HB + b * 1024 + HB_FH + (l & 31) * 8;
      poll_until(p, tag, c2);
      set_flag(S, F_FOK, 1);
    }
    STAMP(2);
    __builtin_amdgcn_endpgm();
  }

  if (wv >= 4 && !hasM) __builtin_amdgcn_endpgm();
  u32x4 xr; u32 epoch;
  if (wv < 4) asm volatile("s_waitcnt vmcnt(4)\n\tv_mov_b32 %0, v114" : "=v"(epoch) :: "memory");
  else asm volatile("s_waitcnt vmcnt(0)\n\tv_mov_b32 %0, v114" : "=v"(epoch) :: "memory");
  asm volatile("v_mov_b32 %0, v110\n\tv_mov_b32 %1, v111\n\tv_mov_b32 %2, v112\n\tv_mov_b32 %3, v113" : "=v"(xr.x), "=v"(xr.y), "=v"(xr.z), "=v"(xr.w) :: "memory");

  if (wv >= 4) {
    const int w = wv - 4;
    const u32 f0 = g * 256 + (s >> 2) * 32 + (s & 3) * 4;
    lds_st<u32x4>(S, O_Y + l * 16, ln8(ln8(xr)));
    const u32x2 ay = lds_ld<u32x2>(S, O_Y + (s * 16 + 4 * j4) * 2);
    wait_ge(S, F_LANDED, N_X);
    const u32 b1v = lds_ld<u16>(S, O_MISC + 16 + ((l & 4) ? 16 : 0) + ((f0 * 2) & 15) + p4 * 2);
    const u32 tag = mktag(epoch);
    u32 a_hp = (WS_HP + ((g * 32 + (w * 2 + (j4 & 1)) * 4 + (n16 >> 2)) * 32 + s) * 64 + (n16 & 3) * 16) | msk(l < 32);
    u32 a_bp = O_BP + w * 256 + l * 4;
    u32 hoff = WS_HP + (me * 32 + (l >> 2)) * 64 + (l & 3) * 16;
    u32 ad1 = O_W1 + (4 * j4 + q4) * 512 + (((8 * w + (p4 >> 1)) ^ (((4 * j4 + q4) & 7) << 1)) * 16) + (p4 & 1) * 8;
    u32 ad2 = O_W2 + ((4 * j4 + q4) & 7) * 1024 + (((16 * w + (p4 >> 1)) ^ (((4 * j4 + q4) & 7) << 1)) * 16) + (p4 & 1) * 8;
    float b1f = f_of(b1v << 16);
    asm volatile("" : "+v"(a_hp), "+v"(a_bp), "+v"(hoff), "+v"(ad1), "+v"(ad2), "+v"(b1f));
    wait_ge(S, F_LANDED, N_W1);
    {
      f32x4 d[4];
#pragma unroll
      for (int tt = 0; tt < 4; ++tt) d[tt] = mfma16(ay, lds_tr(S, ad1 ^ (tt << 5)), f32x4{0.f, 0.f, 0.f, 0.f});
      const bool up = j4 & 1;
      stsc(rws, a_hp, u32x4{u_of(up ? d[2][0] : d[0][0]), tag, u_of(up ? d[3][0] : d[1][0]), tag});
    }
    STAMP(3);
    wait_until(tstart + H_T);
    u32x4 h0 = ld1(rws, hoff), h1 = ld1(rws, hoff + 16 * 64);
_Pragma("unroll 1")
    for (int spin = 0; spin < SPIN_CAP && !__all(tok(h0, tag) & tok(h1, tag)); ++spin) { MEMCLOB(); h0 = ldsc(rws, hoff); h1 = ldsc(rws, hoff + 16 * 64); }
    STAMP(4);
    {
      float ha = f_of(h0.x) + f_of(h1.x), hb = f_of(h0.z) + f_of(h1.z);
      ha += dpp<ROR4>(ha); hb += dpp<ROR4>(hb); ha += dpp<ROR8>(ha); hb += dpp<ROR8>(hb);
      ha = x32_sum(x16_sum(ha)); hb = x32_sum(x16_sum(hb));
      const float p = rbf(rbf((l & 4) ? hb : ha) + b1f);
      const float u = p * fmaf(0.044715f * 0.7978845608028654f * 2.8853900817779268f, p * p, 0.7978845608028654f * 2.8853900817779268f);
      const float h = p - p * rcp(ex2(u) + 1.f);
      if (l < 8) lds_st<u16>(S, O_HV + w * 16 + (p4 * 2 + (l >> 2)) * 2, (u16)pk(h, h));
    }
    u32x2 ah = lds_ld<u32x2>(S, O_HV + w * 16 + (j4 & 1) * 8);
    if (j4 >= 2) ah = u32x2{0u, 0u};
    wait_ge(S, F_LANDED, N_W2);
    {
      f32x4 d[8];
#pragma unroll
      for (int tt = 0; tt < 8; ++tt) d[tt] = mfma16(ah, lds_tr(S, ad2 ^ (tt << 5)), f32x4{0.f, 0.f, 0.f, 0.f});
      const bool b0 = j4 & 1, b1_ = j4 & 2;
      const float e0 = b1_ ? (b0 ? d[6][0] : d[4][0]) : (b0 ? d[2][0] : d[0][0]), e1 = b1_ ? (b0 ? d[7][0] : d[5][0]) : (b0 ? d[3][0] : d[1][0]);
      lds_st<u32>(S, a_bp, pk(e0, e1));
      if (l == 0) set_flag(S, F_B + w, 1);
    }
    STAMP(5);
    __builtin_amdgcn_endpgm();
  }

  const int w = wv;
  int c3 = 0, c4 = 0;
  int seen = -100;
  const int fcol = (b >> 4) * 128 + ((b >> 2) & 3) * 32 + (b & 3) * 4 + p4 + ((l >> 2) & 1) * 16;
  lds_st<u32x4>(S, O_XN + l * 16, ln8(xr));
  const u32 tag = mktag(epoch);
  STAMP(1);
  const u32 lb = (u32)(size_t)S;
  {
    u32x4 ax[4];
#pragma unroll
    for (int ks = 0; ks < 4; ++ks) ax[ks] = lds_ld<u32x4>(S, O_XN + ((s & 3) * 128 + 32 * ks + 8 * j4) * 2);
    const u32 gq = (q4 >> 1) | ((j4 & 1) << 1);
    u32 ad = O_WQ + (8 * j4 + q4) * 128 + (((2 * w + (p4 >> 1)) ^ (gq << 1)) * 16) + (p4 & 1) * 8;
    u32 a_qp = (WS_QP + ((g * 4 + (s & 3)) * 256 + (s >> 2) * 32 + w * 8 + (n16 >> 1)) * 8) | msk(l < 16 && (l & 1) == 0);
    asm volatile("" : "+v"(ad), "+v"(a_qp), "+v"(ax[0]), "+v"(ax[1]), "+v"(ax[2]), "+v"(ax[3]));
    asm volatile("s_waitcnt vmcnt(0)" ::: "memory");
    if (l == 0) set_flag(S, F_WQ + w, 1);
    wait4(S, F_WQ);
    u32x2 bb[8];
#pragma unroll
    for (int i = 0; i < 8; ++i) bb[i] = lds_tr(S, ad + (i >> 1) * 4096 + (i & 1) * 512);
    f32x4 d = {0.f, 0.f, 0.f, 0.f};
#pragma unroll
    for (int ks = 0; ks < 4; ++ks) d = mfma32(ax[ks], u32x4{bb[2 * ks].x, bb[2 * ks].y, bb[2 * ks + 1].x, bb[2 * ks + 1].y}, d);
    stsc2(rws, a_qp, u32x2{pk(d[0], dpp<XOR1>(d[0])), tag});
  }
  STAMP(2);
  wait_landed(S, seen, N_X);
  const u32 b2h = lds_ld<u16>(S, O_MISC + 48 + ((l & 4) ? 16 : 0) + (((b & 3) * 8) & 15) + p4 * 2);
  const u32 xfh = lds_ld<u16>(S, O_X + (fcol & 511) * 2);
  u32 qoff = WS_QP + ((g * 4 + 2 * (l >> 5)) * 256 + w * 64 + 2 * (l & 31)) * 8;
  u32 adk = O_K + (32 * w + n16) * 256;
  u32 a_qs = lb + O_QS + (n16 & 3) * 256 + j4 * 16, a_qw = O_QS + w * 256 + (l & 31) * 8;
  u32 a_pw = O_P + (n16 & 3) * 256 + (32 * w + 4 * j4) * 2, a_pr = lb + O_P + (n16 >> 2) * 256 + j4 * 16;
  u32 a_mw = O_MW + (l & 3) * 16 + w * 4, a_mr = lb + O_MW + j4 * 16;
  u32 ad0 = O_V + (8 * j4 + q4) * 256 + (((4 * w + (p4 >> 1)) ^ ((q4 | ((j4 & 1) << 2)) << 1)) * 16) + (p4 & 1) * 8, ad1 = ad0 ^ 32;
  u32 a_op = WS_OP + ((g * 32 + (j4 * 4 + w) * 2 + (n16 >> 3)) * 32 + s) * 128 + (n16 & 7) * 16;
  u32 a_st = (WS_HB + (g * 32 + j4 * 8 + 2 * w + (n16 & 1)) * 1024 + HB_ST + s * 16) | msk(n16 < 2);
  u32 ooff = WS_OP + (me * 32 + (l >> 1)) * 128 + (2 * w + (l & 1)) * 16;
  u32 adw = O_WO + (4 * j4 + q4) * 1024 + (((16 * w + (p4 >> 1)) ^ (((4 * j4 + q4) & 7) << 1)) * 16) + (p4 & 1) * 8;
  u32 a_sp = WS_SB + ((w * 16 + (l >> 2)) * 256 + me) * 64 + (l & 3) * 16;
  u32 off0 = WS_SB + ((b & 63) * 256 + 64 * w + (l >> 2)) * 64 + (l & 3) * 16;
  asm volatile("" : "+v"(qoff), "+v"(adk), "+v"(a_qs), "+v"(a_qw), "+v"(a_pw), "+v"(a_pr), "+v"(a_mw), "+v"(a_mr), "+v"(ad0), "+v"(ad1));
  asm volatile("" : "+v"(a_op), "+v"(a_st), "+v"(ooff), "+v"(adw), "+v"(a_sp), "+v"(off0));
  const u32 a_fq = lb + O_FL + 4 * F_Q, a_fp = lb + O_FL + 4 * F_P;
  u32x4 kf[8];
  {
    wait_until(tstart + Q_T);
    u32x4 qa = ld1(rws, qoff), qb = ld1(rws, qoff + 2048);
    wait_landed(S, seen, N_W1 + 8 * (w + 1));
#pragma unroll
    for (int i = 0; i < 8; ++i) kf[i] = lds_ld<u32x4>(S, adk + (i >> 2) * 4096 + (((4 * (i & 3) + j4) ^ n16) * 16));
_Pragma("unroll 1")
    for (int spin = 0; spin < SPIN_CAP && !__all(tok(qa, tag) & tok(qb, tag)); ++spin) { MEMCLOB(); qa = ldsc(rws, qoff); qb = ldsc(rws, qoff + 2048); }
    const float q0 = x32_sum(lo16(qa.x) + lo16(qb.x)), q1 = x32_sum(hi16(qa.x) + hi16(qb.x)), q2 = x32_sum(lo16(qa.z) + lo16(qb.z)), q3 = x32_sum(hi16(qa.z) + hi16(qb.z));
    if (l < 32) lds_st<u32x2>(S, a_qw, u32x2{pk(q0, q1), pk(q2, q3)});
    if (l == 0) set_flag(S, F_Q + w, 1);
  }
  STAMP(3);
  {
    u32x4 fl4, bq0, bq1, bq2, bq3;
#pragma unroll 1
    for (int spin = 0; spin < (1 << 22); ++spin) {
      asm volatile("ds_read_b128 %0, %5\n\tds_read_b128 %1, %6\n\tds_read_b128 %2, %6 offset:64\n\tds_read_b128 %3, %6 offset:128\n\tds_read_b128 %4, %6 offset:192\n\ts_waitcnt lgkmcnt(0)"
                   : "=&v"(fl4), "=&v"(bq0), "=&v"(bq1), "=&v"(bq2), "=&v"(bq3) : "v"(a_fq), "v"(a_qs) : "memory");
      if (all4(fl4)) break;
    }
    f32x4 d0 = mfma32(kf[0], bq0, f32x4{0.f, 0.f, 0.f, 0.f}), d1 = mfma32(kf[4], bq0, f32x4{0.f, 0.f, 0.f, 0.f});
    d0 = mfma32(kf[1], bq1, d0); d1 = mfma32(kf[5], bq1, d1);
    d0 = mfma32(kf[2], bq2, d0); d1 = mfma32(kf[6], bq2, d1);
    d0 = mfma32(kf[3], bq3, d0); d1 = mfma32(kf[7], bq3, d1);
    constexpr float C2 = 0.08838834764831845f * 1.4426950408889634f;
    float sc[8];
#pragma unroll
    for (int i = 0; i < 4; ++i) { sc[i] = d0[i] * C2; sc[4 + i] = d1[i] * C2; }
    const float mw = xr_max(fmaxf(fmaxf(fmaxf(sc[0], sc[1]), fmaxf(sc[2], sc[3])), fmaxf(fmaxf(sc[4], sc[5]), fmaxf(sc[6], sc[7]))));
    float p[8];
#pragma unroll
    for (int i = 0; i < 8; ++i) p[i] = ex2(sc[i] - mw);
    if (n16 < 4) {
      lds_st<u32x2>(S, a_pw, u32x2{pk(p[0], p[1]), pk(p[2], p[3])});
      lds_st<u32x2>(S, a_pw + 32, u32x2{pk(p[4], p[5]), pk(p[6], p[7])});
      if (l < 4) lds_st<float>(S, a_mw, mw);
    }
    if (l == 0) set_flag(S, F_P + w, 1);
  }
  STAMP(4);
  {
    u32x4 fl4, ap[4], m4u;
#pragma unroll 1
    for (int spin = 0; spin < (1 << 22); ++spin) {
      asm volatile("ds_read_b128 %0, %6\n\tds_read_b128 %1, %7\n\tds_read_b128 %2, %7 offset:64\n\tds_read_b128 %3, %7 offset:128\n\tds_read_b128 %4, %7 offset:192\n\tds_read_b128 %5, %8\n\ts_waitcnt lgkmcnt(0)"
                   : "=&v"(fl4), "=&v"(ap[0]), "=&v"(ap[1]), "=&v"(ap[2]), "=&v"(ap[3]), "=&v"(m4u) : "v"(a_fp), "v"(a_pr), "v"(a_mr) : "memory");
      if (all4(fl4)) break;
    }
    const float m0 = f_of(m4u.x), m1 = f_of(m4u.y), m2 = f_of(m4u.z), m3 = f_of(m4u.w);
    const float M = vmax(vmax3(m0, m1, m2), m3);
    const float al[4] = {ex2(m0 - M), ex2(m1 - M), ex2(m2 - M), ex2(m3 - M)};
    const u32x4 ones = {0x3f803f80u, 0x3f803f80u, 0x3f803f80u, 0x3f803f80u};
    float L = 0;
#pragma unroll
    for (int ks = 0; ks < 4; ++ks) L = fmaf(al[ks], mfma32(ap[ks], ones, f32x4{0.f, 0.f, 0.f, 0.f})[0], L);
    const u32x4 stv = {u_of(M), tag, u_of(L), tag};
    float o0 = 0, o1 = 0;
    wait_landed(S, seen, N_K + 24);
    {
      u32x2 vf[12];
#pragma unroll
      for (int i = 0; i < 12; ++i) vf[i] = lds_tr(S, ((i & 2) ? ad1 : ad0) + (i >> 2) * 8192 + (i & 1) * 1024);
#pragma unroll
      for (int ks = 0; ks < 3; ++ks) {
        o0 = fmaf(al[ks], mfma32(ap[ks], u32x4{vf[4 * ks].x, vf[4 * ks].y, vf[4 * ks + 1].x, vf[4 * ks + 1].y}, f32x4{0.f, 0.f, 0.f, 0.f})[0], o0);
        o1 = fmaf(al[ks], mfma32(ap[ks], u32x4{vf[4 * ks + 2].x, vf[4 * ks + 2].y, vf[4 * ks + 3].x, vf[4 * ks + 3].y}, f32x4{0.f, 0.f, 0.f, 0.f})[0], o1);
      }
    }
    wait_landed(S, seen, ST_AT);
    stsc(rws, a_st, stv);
    wait_landed(S, seen, N_V);
    {
      const u32x2 a0 = lds_tr(S, ad0 + 3 * 8192), a1 = lds_tr(S, ad0 + 3 * 8192 + 1024), c0 = lds_tr(S, ad1 + 3 * 8192), c1 = lds_tr(S, ad1 + 3 * 8192 + 1024);
      o0 = fmaf(al[3], mfma32(ap[3], u32x4{a0.x, a0.y, a1.x, a1.y}, f32x4{0.f, 0.f, 0.f, 0.f})[0], o0);
      o1 = fmaf(al[3], mfma32(ap[3], u32x4{c0.x, c0.y, c1.x, c1.y}, f32x4{0.f, 0.f, 0.f, 0.f})[0], o1);
    }
    stsc(rws, a_op, u32x4{u_of(o0), tag, u_of(o1), tag});
  }
  STAMP(5);
  {
    wait_ge(S, F_OOK, 1);
    u32x4 ov = ld1(rws, ooff);
    wait_landed(S, seen, N_WO);
    u32x2 bw[8];
#pragma unroll
    for (int tt = 0; tt < 8; ++tt) bw[tt] = lds_tr(S, adw ^ (tt << 5));
    u32 bp = 0;
    if (hasM) { wait_ge(S, F_B + w, 1); bp = lds_ld<u32>(S, O_BP + w * 256 + l * 4); }
    wait_ge(S, F_WOK, 1);
    const float wn = lds_ld<float>(S, O_WT + (l >> 1) * 4);
_Pragma("unroll 1")
    for (int spin = 0; spin < SPIN_CAP && !__all(tok(ov, tag)); ++spin) { DIAG(c3++); MEMCLOB(); ov = ldsc(rws, ooff); }
    if (t == 0) set_flag(S, F_OG, 1);
    STAMP(6);
    float o0 = wn * f_of(ov.x), o1 = wn * f_of(ov.z);
    o0 += dpp<XOR2>(o0); o1 += dpp<XOR2>(o1); o0 += dpp<ROR4>(o0); o1 += dpp<ROR4>(o1); o0 += dpp<ROR8>(o0); o1 += dpp<ROR8>(o1);
    o0 = x16_sum(o0); o1 = x16_sum(o1); o0 = x32_sum(o0); o1 = x32_sum(o1);
    const u32 pr = pk(o0, o1), po = __builtin_amdgcn_mov_dpp(pr, XOR1, 0xf, 0xf, true);
    if (l == 0) { MEMCLOB(); *(volatile LAS u32x4*)(S + O_AV + w * 16) = u32x4{pr, po, 1u, 1u}; MEMCLOB(); }
    u32x4 av;
#pragma unroll 1
    for (int spin = 0; spin < (1 << 22); ++spin) { av = *(volatile LAS u32x4*)(S + O_AV + j4 * 16); if (__all(av.z != 0)) break; }
    const u32x2 a = {av.x, av.y};
    f32x4 d[8];
#pragma unroll
    for (int tt = 0; tt < 8; ++tt) d[tt] = mfma16(a, bw[tt], f32x4{0.f, 0.f, 0.f, 0.f});
    const bool b0 = j4 & 1, b1_ = j4 & 2;
    const float e0 = b1_ ? (b0 ? d[6][0] : d[4][0]) : (b0 ? d[2][0] : d[0][0]), e1 = b1_ ? (b0 ? d[7][0] : d[5][0]) : (b0 ? d[3][0] : d[1][0]);
    stsc(rws, a_sp, u32x4{pk(e0, e1), tag, bp, tag});
  }
  STAMP(7);
  if (b < NC) {
    const float b2f = f_of(b2h << 16), xff = f_of(xfh << 16);
    u32 a_fw = O_FIN + (l & 3) * 64 + w * 4;
    u32 a_ff = lb + O_FL + 4 * F_F, a_fr = lb + O_FIN + (l & 3) * 64 + ((l >> 2) & 1) * 16;
    asm volatile("" : "+v"(a_fw), "+v"(a_ff), "+v"(a_fr));
    wait_ge(S, F_FOK, 1);
    u32x4 v[4];
#pragma unroll
    for (int i = 0; i < 4; ++i) v[i] = ld1(rws, off0 + i * 1024);
_Pragma("unroll 1")
    for (int spin = 0; spin < SPIN_CAP && !__all(tok(v[0], tag) & tok(v[1], tag) & tok(v[2], tag) & tok(v[3], tag)); ++spin) {
      DIAG(c4++);
      MEMCLOB();
#pragma unroll
      for (int i = 0; i < 4; ++i) v[i] = ldsc(rws, off0 + i * 1024);
    }
    STAMP(8);
    float r[4];
    r[0] = (lo16(v[0].x) + lo16(v[1].x)) + (lo16(v[2].x) + lo16(v[3].x)); r[1] = (hi16(v[0].x) + hi16(v[1].x)) + (hi16(v[2].x) + hi16(v[3].x));
    r[2] = (lo16(v[0].z) + lo16(v[1].z)) + (lo16(v[2].z) + lo16(v[3].z)); r[3] = (hi16(v[0].z) + hi16(v[1].z)) + (hi16(v[2].z) + hi16(v[3].z));
#pragma unroll
    for (int i = 0; i < 4; ++i) r[i] += dpp<ROR4>(r[i]);
#pragma unroll
    for (int i = 0; i < 4; ++i) r[i] += dpp<ROR8>(r[i]);
#pragma unroll
    for (int i = 0; i < 4; ++i) r[i] = x16_sum(r[i]);
#pragma unroll
    for (int i = 0; i < 4; ++i) r[i] = x32_sum(r[i]);
    if (l < 4) {
#pragma unroll
      for (int i = 0; i < 4; ++i) lds_st<float>(S, a_fw + i * 16, r[i]);
    }
    if (l == 0) set_flag(S, F_F + w, 1);
    if (w == 0) {
      u32x4 fl4, s4, m4;
#pragma unroll 1
      for (int spin = 0; spin < (1 << 22); ++spin) {
        asm volatile("ds_read_b128 %0, %3\n\tds_read_b128 %1, %4\n\tds_read_b128 %2, %4 offset:32\n\ts_waitcnt lgkmcnt(0)"
                     : "=&v"(fl4), "=&v"(s4), "=&v"(m4) : "v"(a_ff), "v"(a_fr) : "memory");
        if (all4(fl4)) break;
      }
      const float sv = (f_of(s4.x) + f_of(s4.y)) + (f_of(s4.z) + f_of(s4.w)), mv = (f_of(m4.x) + f_of(m4.y)) + (f_of(m4.z) + f_of(m4.w));
      const float B = rbf(rbf(mv) + b2f);
      const float gt = rbf(B * rcp(1.f + ex2(-B * 1.4426950408889634f)));
      const float o = xff + rbf(rbf(sv) * gt);
      if (l < 8) out[fcol] = (u16)pk(o, o);
      if (b == 0 && l == 0) __builtin_amdgcn_raw_buffer_store_b32(epoch + 1u, rws, WS_EPOCH, 0, AUX_SC1);
    }
  }
  STAMP(9);
  __builtin_amdgcn_endpgm();
  }

  __builtin_amdgcn_s_setprio(3);
  const u32 fl = (u32)(size_t)(S + O_FL + 4 * F_LANDED);
  asm volatile("" :: "s"(kc), "s"(vc), "s"(w1), "s"(w2), "s"(wo), "s"(x), "s"(ws), "s"(b1), "s"(b2));
  STAMP(3);
  {
    __builtin_amdgcn_raw_ptr_buffer_load_lds(mk(x, 1024), (LAS void*)(S + O_X), 16, l * 16, 0, 0, 0);
    const u32 f0 = g * 256 + (s >> 2) * 32 + (s & 3) * 4, c0 = ((b & 63) >> 4) * 128 + ((b >> 2) & 3) * 32 + (b & 3) * 4;
    const char* p = (const char*)ws;
    if (l == 1 || l == 2) p = (const char*)b1 + (((hasM ? f0 : 0) + (l - 1) * 16) * 2 & ~15u);
    if (l == 3 || l == 4) p = (const char*)b2 + ((c0 + (l - 3) * 16) * 2 & ~15u);
    __builtin_amdgcn_global_load_lds((const __attribute__((address_space(1))) void*)p, (LAS void*)(S + O_MISC), 16, 0, 0);
  }
  {
    const rsrc_t r1 = mk(w1, hasM ? 512 * FF * 2 : 0);
    const u32 hb = l >> 5, bl = hb * (FF * 2) + (((l & 31) ^ (hb << 1)) * 16);
    const u32 base = (s * 16) * (FF * 2) + g * 512;
#pragma unroll 1
    for (int G = 0; G < 2; ++G) dma4(r1, S, O_W1 + G * 4096, bl, (bl ^ 64) + 6144, (bl ^ 128) + 2 * 6144, (bl ^ 192) + 3 * 6144, base + G * 8 * (FF * 2));
  }
  const size_t kvoff = ((size_t)g * SK + (SK - WIN) + s * 128) * 256;
  {
    const rsrc_t rk = mk((const char*)kc + kvoff, 32768);
    const u32 v0 = j4 * 256 + ((n16 ^ j4) * 16);
    wait_until(tstart + K_T);
#pragma unroll 1
    for (int G = 0; G < 8; ++G) { landed<WINDOW - 4>(fl, N_W1 + 4 * G - (WINDOW - 4)); dma4(rk, S, O_K + G * 4096, v0, v0 ^ 64, v0 ^ 128, v0 ^ 192, G * 4096); }
  }
  STAMP(4);
  {
    const rsrc_t rv = mk((const char*)vc + kvoff, 32768);
    const u32 v0 = j4 * 256 + ((n16 ^ (j4 << 1)) * 16);
#pragma unroll 1
    for (int G = 0; G < 8; ++G) { landed<WINDOW - 4>(fl, N_K + 4 * G - (WINDOW - 4)); dma4(rv, S, O_V + G * 4096, v0, v0, v0 ^ 128, v0 ^ 128, G * 4096); }
  }
  STAMP(5);
  {
    const rsrc_t r2 = mk((const char*)w2 - 1024, hasM ? 512 * FF * 2 + 1024 : 0);
    const u32 f0 = g * 256 + (s >> 2) * 32 + (s & 3) * 4;
#pragma unroll 1
    for (int G = 0; G < 2; ++G) {
      landed<WINDOW2 - 4>(fl, N_V + 4 * G - (WINDOW2 - 4));
      const u32 v = (l * 16) ^ (G << 7);
      dma4(r2, S, O_W2 + G * 4096, v + 1024, (v ^ 32) + 16384, v ^ 64, (v ^ 96) + 15360, (f0 + 2 * G) * 1024);
    }
  }
  {
    const rsrc_t ro = mk((const char*)wo - 1024, 4096 * 512 * 2 + 1024);
    const u32 r0 = g * 512 + (s >> 3) * 128 + ((s >> 1) & 3) * 32 + (s & 1) * 8;
#pragma unroll 1
    for (int G = 0; G < 4; ++G) {
      landed<WINDOW2 - 4>(fl, N_W2 + 4 * G - (WINDOW2 - 4));
      const u32 v = (l * 16) ^ ((G & 1) << 7);
      dma4(ro, S, O_WO + G * 4096, v + 1024, (v ^ 32) + 16384, v ^ 64, (v ^ 96) + 15360, (r0 + 2 * G) * 1024);
    }
  }
  STAMP(1);
  drain<WINDOW2 - 4>(fl);
  STAMP(2);
}

extern "C" void kernel_launch(void* const* d_in, const int* in_sizes, int n_in, void* d_out, int out_size, void* d_ws, size_t ws_size, hipStream_t stream) {
  fused_decode<<<NWG, NT, 0, stream>>>((const u16*)d_in[0], (const u16*)d_in[1], (const u16*)d_in[2], (const u16*)d_in[3], (const u16*)d_in[4],
                                       (const u16*)d_in[6], (const u16*)d_in[7], (const u16*)d_in[8], (const u16*)d_in[9], (u16*)d_out, (unsigned char*)d_ws);
}
```

```cpp
#include <hip/hip_runtime.h>
#include <stdint.h>

typedef unsigned short u16;
typedef unsigned u32;
typedef unsigned long long u64;
typedef u32 u32x4 __attribute__((ext_vector_type(4)));
typedef u32 u32x2 __attribute__((ext_vector_type(2)));
typedef float f32x4 __attribute__((ext_vector_type(4)));
typedef float f32x2 __attribute__((ext_vector_type(2)));
typedef short s16x4 __attribute__((ext_vector_type(4)));
typedef __bf16 bf16x8 __attribute__((ext_vector_type(8)));
typedef __bf16 bf16x2 __attribute__((ext_vector_type(2)));
typedef __amdgpu_buffer_rsrc_t rsrc_t;
#define LAS __attribute__((address_space(3)))

constexpr int SK = 65536, WIN = 4096, FF = 1792;
constexpr int NWG = 256, NT = 640;
constexpr int NC = 64;
constexpr u32 WS_EPOCH = 0;
constexpr u32 WS_QP = 4096;
constexpr u32 WS_HB = WS_QP + 8 * 4 * 256 * 8;
constexpr u32 WS_ST = WS_HB + 256 * 128;
constexpr u32 WS_OP = WS_ST + 256 * 512;
constexpr u32 WS_HP = WS_OP + 256 * 32 * 128;
constexpr u32 WS_SB = WS_HP + 224 * 32 * 64;
constexpr u32 WS_END = WS_SB + NC * 256 * 64;
constexpr u32 OOB = 0x80000000u;
constexpr u32 O_WQ = 0, O_K = 16384, O_V = 49152, O_W1 = 81920, O_W2 = 90112, O_WO = 98304, O_S = 114688;
constexpr u32 O_X = O_S, O_MISC = O_X + 1024, O_S2 = O_MISC + 1024;
constexpr u32 O_XN = O_S2, O_Y = O_XN + 1024, O_QS = O_Y + 1024, O_P = O_QS + 1024, O_MW = O_P + 1024, O_LW = O_MW + 64, O_AV = O_LW + 64,
              O_EP = O_AV + 64, O_WT = O_EP + 16, O_HV = O_WT + 128, O_HX = O_HV + 64, O_FIN = O_HX + 128, O_BP = O_FIN + 512, O_FL = O_BP + 1024, LDS_BYTES = O_FL + 256;
constexpr u32 O_INIT = O_AV;
enum { F_LANDED = 0, F_OOK = 2, F_HOK = 3, F_FOK = 4, F_WOK = 5, F_OG = 6, F_XN = 7, F_Y = 8, F_INIT = 9, F_H = 32, F_Q = 12, F_P = 16, F_WQ = 20, F_F = 24, F_B = 28 };
constexpr int N_X = 2, N_W1 = N_X + 8, N_K = N_W1 + 32, N_V = N_K + 32, N_END = N_V + 24;
#define WO_FIRST 0
constexpr int N_W2 = WO_FIRST ? N_END : N_V + 8, N_WO = WO_FIRST ? N_V + 16 : N_END;
#define WINDOW 24
#define WINDOW2 24
#define Q_AT (N_X + 20)
#define ST_AT (N_V - 4)
#define HH_AT N_V
#define POSTMODE 0
#define HOLD 45
#define K_T 100
#define Q_T 265
#define H_T 330
#define POLL_AT N_V
#define CODE_BYTES 11520
#define SPINSLEEP 0
constexpr int AUX_NT = 2, AUX_SC1 = 16;
#define LSPIN (1 << 22)
#define GSPIN (1 << 14)
constexpr int SPIN_CAP = GSPIN;

#define DIAG(x)
#define KEEP(x) asm volatile("" :: "v"(x))
#define MEMCLOB() asm volatile("" ::: "memory")
#define STAMP(k) do { } while (0)

__device__ __forceinline__ rsrc_t mk(const void* p, u32 bytes) { return __builtin_amdgcn_make_buffer_rsrc((void*)p, 0, bytes, 0x00020000); }
__device__ __forceinline__ u32x4 ldsc(rsrc_t r, u32 voff) { return __builtin_amdgcn_raw_buffer_load_b128(r, voff, 0, AUX_SC1); }
#define FIRST_AUX 16
__device__ __forceinline__ u32x4 ld1(rsrc_t r, u32 voff) { return __builtin_amdgcn_raw_buffer_load_b128(r, voff, 0, FIRST_AUX); }
__device__ __forceinline__ void stsc(rsrc_t r, u32 voff, u32x4 v) { __builtin_amdgcn_raw_buffer_store_b128(v, r, voff, 0, AUX_SC1); }
__device__ __forceinline__ void stsc2(rsrc_t r, u32 voff, u32x2 v) { __builtin_amdgcn_raw_buffer_store_b64(v, r, voff, 0, AUX_SC1); }
__device__ __forceinline__ float f_of(u32 u) { return __builtin_bit_cast(float, u); }
__device__ __forceinline__ u32 u_of(float f) { return __builtin_bit_cast(u32, f); }
__device__ __forceinline__ float lo16(u32 w) { return f_of(w << 16); }
__device__ __forceinline__ float hi16(u32 w) { return f_of(w & 0xffff0000u); }
__device__ __forceinline__ u32 pk(float lo, float hi) { bf16x2 r = {(__bf16)lo, (__bf16)hi}; return __builtin_bit_cast(u32, r); }
__device__ __forceinline__ float rbf(float f) { return lo16(pk(f, f)); }
template <int CTRL> __device__ __forceinline__ float dpp(float x) { return f_of(__builtin_amdgcn_mov_dpp(u_of(x), CTRL, 0xf, 0xf, true)); }
constexpr int XOR1 = 0xB1, XOR2 = 0x4E, HMIR = 0x141, MIR = 0x140, ROR4 = 0x124, ROR8 = 0x128;
__device__ __forceinline__ float x16_sum(float x) { auto s = __builtin_amdgcn_permlane16_swap(u_of(x), u_of(x), false, false); return f_of(s[0]) + f_of(s[1]); }
__device__ __forceinline__ float x32_sum(float x) { auto s = __builtin_amdgcn_permlane32_swap(u_of(x), u_of(x), false, false); return f_of(s[0]) + f_of(s[1]); }
__device__ __forceinline__ float x16_max(float x) { auto s = __builtin_amdgcn_permlane16_swap(u_of(x), u_of(x), false, false); return fmaxf(f_of(s[0]), f_of(s[1])); }
__device__ __forceinline__ float x32_max(float x) { auto s = __builtin_amdgcn_permlane32_swap(u_of(x), u_of(x), false, false); return fmaxf(f_of(s[0]), f_of(s[1])); }
__device__ __forceinline__ float row_sum(float x) { x += dpp<XOR1>(x); x += dpp<XOR2>(x); x += dpp<HMIR>(x); x += dpp<MIR>(x); return x; }
__device__ __forceinline__ float wave_sum(float x) { return x32_sum(x16_sum(row_sum(x))); }
__device__ __forceinline__ float vmax(float a, float b) { float r; asm("v_max_f32 %0, %1, %2" : "=v"(r) : "v"(a), "v"(b)); return r; }
__device__ __forceinline__ float vmax3(float a, float b, float c) { float r; asm("v_max3_f32 %0, %1, %2, %3" : "=v"(r) : "v"(a), "v"(b), "v"(c)); return r; }
#define DPPMAX(x, ctrl) asm("s_nop 1\n\tv_max_f32_dpp %0, %0, %0 " ctrl " row_mask:0xf bank_mask:0xf" : "+v"(x))
__device__ __forceinline__ float xr_max(float x) {
  auto s = __builtin_amdgcn_permlane16_swap(u_of(x), u_of(x), false, false); x = vmax(f_of(s[0]), f_of(s[1]));
  auto t = __builtin_amdgcn_permlane32_swap(u_of(x), u_of(x), false, false); return vmax(f_of(t[0]), f_of(t[1]));
}
__device__ __forceinline__ float par_sum(float x) { x += dpp<XOR2>(x); x += dpp<ROR4>(x); x += dpp<ROR8>(x); return x32_sum(x16_sum(x)); }
__device__ __forceinline__ float par_max(float x) { DPPMAX(x, "quad_perm:[2,3,0,1]"); DPPMAX(x, "row_ror:4"); DPPMAX(x, "row_ror:8"); return xr_max(x); }
__device__ __forceinline__ float ex2(float x) { return __builtin_amdgcn_exp2f(x); }
__device__ __forceinline__ float rcp(float x) { return __builtin_amdgcn_rcpf(x); }
__device__ __forceinline__ f32x4 mfma32(u32x4 a, u32x4 b, f32x4 c) {
  return __builtin_amdgcn_mfma_f32_16x16x32_bf16(__builtin_bit_cast(bf16x8, a), __builtin_bit_cast(bf16x8, b), c, 0, 0, 0);
}
__device__ __forceinline__ f32x4 mfma16(u32x2 a, u32x2 b, f32x4 c) {
  return __builtin_amdgcn_mfma_f32_16x16x16bf16_1k(__builtin_bit_cast(s16x4, a), __builtin_bit_cast(s16x4, b), c, 0, 0, 0);
}
typedef LAS unsigned char* lds_t;
template <class T> __device__ __forceinline__ T lds_ld(lds_t s, u32 off) { return *(const LAS T*)(s + off); }
template <class T> __device__ __forceinline__ void lds_st(lds_t s, u32 off, T v) { *(LAS T*)(s + off) = v; }
__device__ __forceinline__ u32x2 lds_tr(lds_t s, u32 off) { return __builtin_bit_cast(u32x2, __builtin_amdgcn_ds_read_tr16_b64_v4i16((LAS s16x4*)(s + off))); }
__device__ __forceinline__ void set_flag(lds_t s, int f, u32 v) { MEMCLOB(); *(volatile LAS u32*)(s + O_FL + 4 * f) = v; MEMCLOB(); }
__device__ __forceinline__ void wait_ge(lds_t s, int f, int need) {
#pragma unroll 1
  for (int spin = 0; spin < LSPIN; ++spin) {
    const int v = __builtin_amdgcn_readfirstlane(*(volatile LAS int*)(s + O_FL + 4 * f));
    if (v >= need) break;
    if (SPINSLEEP) __builtin_amdgcn_s_sleep(SPINSLEEP);
  }
  MEMCLOB();
}
__device__ __forceinline__ void wait_landed(lds_t s, int& seen, int need) {
  if (seen < need) {
#pragma unroll 1
    for (int spin = 0; spin < LSPIN; ++spin) {
      seen = __builtin_amdgcn_readfirstlane(*(volatile LAS int*)(s + O_FL + 4 * F_LANDED));
      if (seen >= need) break;
      if (SPINSLEEP) __builtin_amdgcn_s_sleep(SPINSLEEP);
    }
  }
  MEMCLOB();
}
__device__ __forceinline__ bool all4(u32x4 f) { return __builtin_amdgcn_readfirstlane(f.x & f.y & f.z & f.w) != 0; }
__device__ __forceinline__ void wait4(lds_t s, int f) {
#pragma unroll 1
  for (int spin = 0; spin < LSPIN; ++spin) {
    const u32x4 v = *(volatile LAS u32x4*)(s + O_FL + 4 * f);
    if (__builtin_amdgcn_readfirstlane(v.x & v.y & v.z & v.w)) break;
  }
  MEMCLOB();
}
__device__ __forceinline__ u32x4 ln8(u32x4 xr) {
  float v[8];
#pragma unroll
  for (int i = 0; i < 4; ++i) { v[2 * i] = lo16(xr[i]); v[2 * i + 1] = hi16(xr[i]); }
  const float s = ((v[0] + v[1]) + (v[2] + v[3])) + ((v[4] + v[5]) + (v[6] + v[7]));
  const float m = wave_sum(s) * (1.f / 512);
  float q0 = 0, q1 = 0;
#pragma unroll
  for (int i = 0; i < 4; ++i) { const float d0 = v[2 * i] - m, d1 = v[2 * i + 1] - m; q0 = fmaf(d0, d0, q0); q1 = fmaf(d1, d1, q1); }
  const float var = wave_sum(q0 + q1) * (1.f / 512);
  const float mb = rbf(m);
  const float inv = rcp(rbf(__builtin_amdgcn_sqrtf(rbf(rbf(var) + 1e-5f))));
  u32x4 r;
#pragma unroll
  for (int i = 0; i < 4; ++i) { const u32 d = pk(v[2 * i] - mb, v[2 * i + 1] - mb); r[i] = pk(lo16(d) * inv, hi16(d) * inv); }
  return r;
}
__device__ __forceinline__ u64 poll64(const unsigned char* p) {
  u64 r; const u64 z = 0;
  asm volatile("global_atomic_or_x2 %0, %1, %2, off sc0 sc1\n\ts_waitcnt vmcnt(0)" : "=&v"(r) : "v"(p), "v"(z) : "memory");
  return r;
}
__device__ __forceinline__ void poll_word(const unsigned char* p, u32 tag, int& count) {
  int n;
  asm volatile(
    "v_mov_b32 v108, 0\n\tv_mov_b32 v109, 0\n\t"
    "s_mov_b64 exec, 1\n\t"
    "global_atomic_or_x2 v[104:105], %1, v[108:109], off sc0 sc1\n\t"
    "s_sleep 4\n\t"
    "global_atomic_or_x2 v[106:107], %1, v[108:109], off sc0 sc1\n\t"
    "s_mov_b32 %0, 0\n"
    "1:\n\t"
    "s_waitcnt vmcnt(1)\n\t"
    "v_not_b32 v104, v104\n\tv_xor_b32 v105, %2, v105\n\tv_or_b32 v104, v104, v105\n\t"
    "v_cmp_ne_u32 vcc, 0, v104\n\t"
    "s_cbranch_vccz 2f\n\t"
    "global_atomic_or_x2 v[104:105], %1, v[108:109], off sc0 sc1\n\t"
    "s_waitcnt vmcnt(1)\n\t"
    "v_not_b32 v106, v106\n\tv_xor_b32 v107, %2, v107\n\tv_or_b32 v106, v106, v107\n\t"
    "v_cmp_ne_u32 vcc, 0, v106\n\t"
    "s_cbranch_vccz 2f\n\t"
    "global_atomic_or_x2 v[106:107], %1, v[108:109], off sc0 sc1\n\t"
    "s_add_u32 %0, %0, 1\n\t"
    "s_cmp_lt_u32 %0, %3\n\t"
    "s_cbranch_scc1 1b\n"
    "2:\n\t"
    "s_mov_b64 exec, -1"
    : "=&s"(n) : "v"(p), "v"(tag), "s"(SPIN_CAP) : "vcc", "scc", "v104", "v105", "v106", "v107", "v108", "v109", "memory");
  DIAG(count += n);
}
__device__ __forceinline__ void arrive(unsigned char* p, u32 bit) {
  __hip_atomic_fetch_or((u64*)p, (u64)bit, __ATOMIC_RELAXED, __HIP_MEMORY_SCOPE_AGENT);
}
#define TSLEEP 0
__device__ __forceinline__ void wait_until(u32 t) { while ((int)((u32)__builtin_amdgcn_s_memrealtime() - t) < 0) { if (TSLEEP) __builtin_amdgcn_s_sleep(TSLEEP); } }
__device__ __forceinline__ void dma4(rsrc_t r, lds_t s, u32 ldsoff, u32 v0, u32 v1, u32 v2, u32 v3, u32 soff) {
  __builtin_amdgcn_raw_ptr_buffer_load_lds(r, (LAS void*)(s + ldsoff), 16, v0, soff, 0, AUX_NT);
  __builtin_amdgcn_raw_ptr_buffer_load_lds(r, (LAS void*)(s + ldsoff), 16, v1, soff, 1024, AUX_NT);
  __builtin_amdgcn_raw_ptr_buffer_load_lds(r, (LAS void*)(s + ldsoff), 16, v2, soff, 2048, AUX_NT);
  __builtin_amdgcn_raw_ptr_buffer_load_lds(r, (LAS void*)(s + ldsoff), 16, v3, soff, 3072, AUX_NT);
}
template <int N> __device__ __forceinline__ void landed(u32 flag_addr, int v) {
  asm volatile("s_waitcnt vmcnt(%0)\n\tds_write_b32 %1, %2" :: "i"(N), "v"(flag_addr), "v"(v) : "memory");
}
template <int N> __device__ __forceinline__ void drain(u32 flag_addr, int total) {
  landed<N>(flag_addr, total - N);
  if constexpr (N >= 4) drain<N - 4>(flag_addr, total);
}
__device__ __forceinline__ u32 msk(bool c) { return c ? 0u : OOB; }
__device__ __forceinline__ bool tok(u32x4 v, u32 tag) { return (v.y == tag) & (v.w == tag); }
__device__ __forceinline__ u32 mktag(u32 epoch) { return ((epoch + 1u) * 0x9E3779B1u) ^ 0x5bd1e995u; }

__global__ void __launch_bounds__(NT) fused_decode(const u16* __restrict__ x, const u16* __restrict__ kc, const u16* __restrict__ vc,
                                                   const u16* __restrict__ wq, const u16* __restrict__ wo, const u16* __restrict__ w1,
                                                   const u16* __restrict__ b1, const u16* __restrict__ w2, const u16* __restrict__ b2,
                                                   u16* __restrict__ out, unsigned char* __restrict__ ws) {
  __shared__ __attribute__((aligned(16))) unsigned char smem[LDS_BYTES];
  lds_t S = (lds_t)smem;
  STAMP(0);
  const u32 tstart = (u32)__builtin_amdgcn_s_memrealtime();
  const int t = threadIdx.x, l = t & 63, wv = __builtin_amdgcn_readfirstlane(t >> 6);
  if (wv == 0) {
    lds_st<u32>(S, O_FL + 4 * l, 0u); if (l < 16) lds_st<u32>(S, O_AV + 4 * l, 0u);
    asm volatile("s_waitcnt lgkmcnt(0)" ::: "memory");
  }
  asm volatile("s_load_dword s94, %0, 0x0\n\ts_load_dword s95, %0, 0x40" :: "s"(__builtin_amdgcn_kernarg_segment_ptr()) : "s94", "s95", "memory");
  const rsrc_t rcode = mk((const void*)(__builtin_amdgcn_s_getpc() & ~255ull), CODE_BYTES);
  {
    const u32 sel = (blockIdx.x >> 3) + wv;
    asm volatile("buffer_load_dwordx4 v[120:123], %0, %1, 0 offen\n\tbuffer_load_dwordx4 v[120:123], %2, %1, 0 offen"
                 :: "v"((l * 16 + (wv + 1) * 1024) | ((sel & 3) ? OOB : 0u)), "s"(rcode), "v"((l * 16 + (wv + 11) * 1024) | ((sel & 3) ? OOB : 0u))
                 : "v120", "v121", "v122", "v123", "memory");
  }
  asm volatile("s_barrier" ::: "memory");
  const int b = blockIdx.x, g = b & 7, s = b >> 3;
  const int n16 = l & 15, j4 = l >> 4, q4 = (l >> 2) & 3, p4 = l & 3;
  const bool hasM = g < 7;
  const int me = g * 32 + s;
  const rsrc_t rws = mk(ws, WS_END);
  if (wv < 4) {
    if (wv == 0) {
      const rsrc_t rx = mk(x, 1024);
      asm volatile("buffer_load_dwordx4 v[110:113], %0, %1, 0 offen\n\tbuffer_load_dword v114, off, %2, 0"
                   :: "v"(l * 16), "s"(rx), "s"(rws) : "v110", "v111", "v112", "v113", "v114", "memory");
    }
    {
      const rsrc_t rq = mk(wq, 512 * 4096 * 2);
      const u32 base = ((s & 3) * 128 + 32 * wv) * 8192 + (g * 512 + (s >> 2) * 64) * 2;
      const u32 v0 = (l >> 3) * 8192 + (((l & 7) ^ (((l >> 4) & 1) << 1)) * 16), v1 = (v0 ^ 64) + 64512;
      const u32 m = (u32)(size_t)(S + O_WQ) + wv * 4096;
      asm volatile("s_mov_b32 m0, %0\n\ts_nop 0\n\t"
                   "buffer_load_dwordx4 %1, %5, %6 offen nt lds\n\tbuffer_load_dwordx4 %2, %5, %6 offen offset:1024 nt lds\n\t"
                   "buffer_load_dwordx4 %3, %5, %6 offen offset:2048 nt lds\n\tbuffer_load_dwordx4 %4, %5, %6 offen offset:3072 nt lds"
                   :: "s"(m), "v"(v0), "v"(v1), "v"(v0 + 2 * 64512), "v"(v1 + 2 * 64512), "s"(rq), "s"(base) : "memory");
    }
  }

  if (wv != 8) {
  asm volatile("" :: "s"(out));

  if (wv == 9) {
    int c1 = 0, c2 = 0;
    u32 soff = (WS_ST + (me * 32 + (l & 31)) * 16) | msk(l < 32);
    const u32x4 warm = ldsc(rws, soff);
    wait_ge(S, F_XN, 1);
    const u32 tag = mktag(lds_ld<u32>(S, O_EP));
    KEEP(warm);
    stsc(rws, (WS_HB + ((l & 4) ? (b & 63) : me) * 128 + ((l & 4) ? 64 : 0) + (l & 3) * 16) | msk(l < 4 || (l < 8 && b < NC)), u32x4{0u, tag, 0u, tag});
    wait_ge(S, F_LANDED, POLL_AT);
    STAMP(3);
    poll_word(ws + WS_HB + me * 128, tag, c1);
    set_flag(S, F_OOK, 1);
    STAMP(4);
    {
      u32x4 sv = ldsc(rws, soff);
_Pragma("unroll 1")
      for (int spin = 0; spin < SPIN_CAP && !__all(tok(sv, tag) | (l >= 32)); ++spin) { MEMCLOB(); sv = ldsc(rws, soff); }
      float mp = f_of(sv.x);
      float Mx = mp;
      DPPMAX(Mx, "quad_perm:[1,0,3,2]"); DPPMAX(Mx, "quad_perm:[2,3,0,1]"); DPPMAX(Mx, "row_half_mirror"); DPPMAX(Mx, "row_mirror");
      { auto r = __builtin_amdgcn_permlane16_swap(u_of(Mx), u_of(Mx), false, false); Mx = vmax(f_of(r[0]), f_of(r[1])); }
      const float wp = ex2(mp - Mx);
      const float wn = wp * rcp(x16_sum(row_sum(wp * f_of(sv.z))));
      if (l < 32) lds_st<float>(S, O_WT + l * 4, wn);
    }
    set_flag(S, F_WOK, 1);
    STAMP(1);
    if ((s & 7) == 0) {
      wait_ge(S, F_OG, 1);
      arrive(ws + WS_HB + l * 128 + 64, 1u << (g * 4 + (s >> 3)));
    }
    if (b < NC) {
      poll_word(ws + WS_HB + b * 128 + 64, tag, c2);
      set_flag(S, F_FOK, 1);
    }
    STAMP(2);
    __builtin_amdgcn_endpgm();
  }

  if (wv >= 4 && !hasM) __builtin_amdgcn_endpgm();
  if (wv == 0) {
    u32x4 xr; u32 ep;
    asm volatile("s_waitcnt vmcnt(4)\n\tv_mov_b32 %0, v110\n\tv_mov_b32 %1, v111\n\tv_mov_b32 %2, v112\n\tv_mov_b32 %3, v113\n\tv_mov_b32 %4, v114"
                 : "=v"(xr.x), "=v"(xr.y), "=v"(xr.z), "=v"(xr.w), "=v"(ep) :: "memory");
    lds_st<u32x4>(S, O_XN + l * 16, ln8(xr));
    if (l == 0) { lds_st<u32>(S, O_EP, ep); set_flag(S, F_XN, 1); }
  }
  wait_ge(S, F_XN, 1);
  const u32 epoch = lds_ld<u32>(S, O_EP);

  if (wv >= 4) {
    const int w = wv - 4;
    const u32 f0 = g * 256 + (s >> 2) * 32 + (s & 3) * 4;
    if (w == 0) { lds_st<u32x4>(S, O_Y + l * 16, ln8(lds_ld<u32x4>(S, O_XN + l * 16))); if (l == 0) set_flag(S, F_Y, 1); }
    wait_ge(S, F_Y, 1);
    const u32x2 ay = lds_ld<u32x2>(S, O_Y + (s * 16 + 4 * j4) * 2);
    wait_ge(S, F_LANDED, N_X);
    const u32 b1v = lds_ld<u16>(S, O_MISC + 16 + ((l & 4) ? 16 : 0) + ((f0 * 2) & 15) + p4 * 2);
    const u32 tag = mktag(epoch);
    u32 a_hp = (WS_HP + ((g * 32 + (w * 2 + (j4 & 1)) * 4 + (n16 >> 2)) * 32 + s) * 64 + (n16 & 3) * 16) | msk(l < 32);
    u32 a_bp = O_BP + w * 256 + l * 4;
    u32 hoff = (WS_HP + (me * 32 + 8 * w + ((l >> 2) & 7)) * 64 + (l & 3) * 16) | msk(l < 32);
    u32 ad1 = O_W1 + (4 * j4 + q4) * 512 + (((8 * w + (p4 >> 1)) ^ (((4 * j4 + q4) & 7) << 1)) * 16) + (p4 & 1) * 8;
    u32 ad2 = O_W2 + ((4 * j4 + q4) & 7) * 1024 + (((16 * w + (p4 >> 1)) ^ (((4 * j4 + q4) & 7) << 1)) * 16) + (p4 & 1) * 8;
    float b1f = f_of(b1v << 16);
    asm volatile("" : "+v"(a_hp), "+v"(a_bp), "+v"(hoff), "+v"(ad1), "+v"(ad2), "+v"(b1f));
    wait_ge(S, F_LANDED, N_W1);
    {
      f32x4 d[4];
#pragma unroll
      for (int tt = 0; tt < 4; ++tt) d[tt] = mfma16(ay, lds_tr(S, ad1 ^ (tt << 5)), f32x4{0.f, 0.f, 0.f, 0.f});
      const bool up = j4 & 1;
      stsc(rws, a_hp, u32x4{u_of(up ? d[2][0] : d[0][0]), tag, u_of(up ? d[3][0] : d[1][0]), tag});
    }
    STAMP(3);
    wait_until(tstart + H_T);
    u32x4 h0 = ld1(rws, hoff);
_Pragma("unroll 1")
    for (int spin = 0; spin < SPIN_CAP && !__all(tok(h0, tag) | (l >= 32)); ++spin) { MEMCLOB(); h0 = ldsc(rws, hoff); }
    STAMP(4);
    {
      float ha = f_of(h0.x), hb = f_of(h0.z);
      ha += dpp<ROR4>(ha); hb += dpp<ROR4>(hb); ha += dpp<ROR8>(ha); hb += dpp<ROR8>(hb);
      ha = x16_sum(ha); hb = x16_sum(hb);
      if (l < 4) { lds_st<float>(S, O_HX + l * 16 + w * 4, ha); lds_st<float>(S, O_HX + 64 + l * 16 + w * 4, hb); }
      if (l == 0) set_flag(S, F_H + w, 1);
      wait4(S, F_H);
      const f32x4 h4 = lds_ld<f32x4>(S, O_HX + ((l & 4) ? 64 : 0) + p4 * 16);
      hb = ha = (h4[0] + h4[1]) + (h4[2] + h4[3]);
      const float p = rbf(rbf((l & 4) ? hb : ha) + b1f);
      const float u = p * fmaf(0.044715f * 0.7978845608028654f * 2.8853900817779268f, p * p, 0.7978845608028654f * 2.8853900817779268f);
      const float h = p - p * rcp(ex2(u) + 1.f);
      if (l < 8) lds_st<u16>(S, O_HV + w * 16 + (p4 * 2 + (l >> 2)) * 2, (u16)pk(h, h));
    }
    u32x2 ah = lds_ld<u32x2>(S, O_HV + w * 16 + (j4 & 1) * 8);
    if (j4 >= 2) ah = u32x2{0u, 0u};
    wait_ge(S, F_LANDED, N_W2);
    {
      f32x4 d[8];
#pragma unroll
      for (int tt = 0; tt < 8; ++tt) d[tt] = mfma16(ah, lds_tr(S, ad2 ^ (tt << 5)), f32x4{0.f, 0.f, 0.f, 0.f});
      const bool b0 = j4 & 1, b1_ = j4 & 2;
      const float e0 = b1_ ? (b0 ? d[6][0] : d[4][0]) : (b0 ? d[2][0] : d[0][0]), e1 = b1_ ? (b0 ? d[7][0] : d[5][0]) : (b0 ? d[3][0] : d[1][0]);
      lds_st<u32>(S, a_bp, pk(e0, e1));
      if (l == 0) set_flag(S, F_B + w, 1);
    }
    STAMP(5);
    __builtin_amdgcn_endpgm();
  }

  const int w = wv;
  int c3 = 0, c4 = 0;
  int seen = -100;
  const int fcol = (b >> 4) * 128 + ((b >> 2) & 3) * 32 + (b & 3) * 4 + p4 + ((l >> 2) & 1) * 16;
  const u32 tag = mktag(epoch);
  STAMP(1);
  const u32 lb = (u32)(size_t)S;
  {
    u32x4 ax[4];
#pragma unroll
    for (int ks = 0; ks < 4; ++ks) ax[ks] = lds_ld<u32x4>(S, O_XN + ((s & 3) * 128 + 32 * ks + 8 * j4) * 2);
    const u32 gq = (q4 >> 1) | ((j4 & 1) << 1);
    u32 ad = O_WQ + (8 * j4 + q4) * 128 + (((2 * w + (p4 >> 1)) ^ (gq << 1)) * 16) + (p4 & 1) * 8;
    u32 a_qp = (WS_QP + ((g * 4 + (s & 3)) * 256 + (s >> 2) * 32 + w * 8 + (n16 >> 1)) * 8) | msk(l < 16 && (l & 1) == 0);
    asm volatile("" : "+v"(ad), "+v"(a_qp), "+v"(ax[0]), "+v"(ax[1]), "+v"(ax[2]), "+v"(ax[3]));
    asm volatile("s_waitcnt vmcnt(0)" ::: "memory");
    if (l == 0) set_flag(S, F_WQ + w, 1);
    wait4(S, F_WQ);
    u32x2 bb[8];
#pragma unroll
    for (int i = 0; i < 8; ++i) bb[i] = lds_tr(S, ad + (i >> 1) * 4096 + (i & 1) * 512);
    f32x4 d = {0.f, 0.f, 0.f, 0.f};
#pragma unroll
    for (int ks = 0; ks < 4; ++ks) d = mfma32(ax[ks], u32x4{bb[2 * ks].x, bb[2 * ks].y, bb[2 * ks + 1].x, bb[2 * ks + 1].y}, d);
    stsc2(rws, a_qp, u32x2{pk(d[0], dpp<XOR1>(d[0])), tag});
  }
  STAMP(2);
  wait_landed(S, seen, N_X);
  const u32 b2h = lds_ld<u16>(S, O_MISC + 48 + ((l & 4) ? 16 : 0) + (((b & 3) * 8) & 15) + p4 * 2);
  const u32 xfh = lds_ld<u16>(S, O_X + (fcol & 511) * 2);
  u32 qoff = WS_QP + ((g * 4 + 2 * (l >> 5)) * 256 + w * 64 + 2 * (l & 31)) * 8;
  u32 adk = O_K + (32 * w + n16) * 256;
  u32 a_qs = lb + O_QS + (n16 & 3) * 256 + j4 * 16, a_qw = O_QS + w * 256 + (l & 31) * 8;
  u32 a_pw = O_P + (n16 & 3) * 256 + (32 * w + 4 * j4) * 2, a_pr = lb + O_P + (n16 >> 2) * 256 + j4 * 16;
  u32 a_mw = O_MW + (l & 3) * 16 + w * 4, a_mr = lb + O_MW + j4 * 16;
  u32 ad0 = O_V + (8 * j4 + q4) * 256 + (((4 * w + (p4 >> 1)) ^ ((q4 | ((j4 & 1) << 2)) << 1)) * 16) + (p4 & 1) * 8, ad1 = ad0 ^ 32;
  u32 a_op = WS_OP + ((g * 32 + (j4 * 4 + w) * 2 + (n16 >> 3)) * 32 + s) * 128 + (n16 & 7) * 16;
  u32 a_st = (WS_ST + ((g * 32 + j4 * 8 + 2 * w + (n16 & 1)) * 32 + s) * 16) | msk(n16 < 2);
  u32 ooff = WS_OP + (me * 32 + (l >> 1)) * 128 + (2 * w + (l & 1)) * 16;
  u32 adw = O_WO + (4 * j4 + q4) * 1024 + (((16 * w + (p4 >> 1)) ^ (((4 * j4 + q4) & 7) << 1)) * 16) + (p4 & 1) * 8;
  u32 a_sp = WS_SB + ((w * 16 + (l >> 2)) * 256 + me) * 64 + (l & 3) * 16;
  u32 off0 = WS_SB + ((b & 63) * 256 + 64 * w + (l >> 2)) * 64 + (l & 3) * 16;
  asm volatile("" : "+v"(qoff), "+v"(adk), "+v"(a_qs), "+v"(a_qw), "+v"(a_pw), "+v"(a_pr), "+v"(a_mw), "+v"(a_mr), "+v"(ad0), "+v"(ad1));
  asm volatile("" : "+v"(a_op), "+v"(a_st), "+v"(ooff), "+v"(adw), "+v"(a_sp), "+v"(off0));
  const u32 a_fq = lb + O_FL + 4 * F_Q, a_fp = lb + O_FL + 4 * F_P;
  u32x4 kf[8];
  {
    wait_until(tstart + Q_T);
    u32x4 qa = ld1(rws, qoff), qb = ld1(rws, qoff + 2048);
    wait_landed(S, seen, N_W1 + 8 * (w + 1));
#pragma unroll
    for (int i = 0; i < 8; ++i) kf[i] = lds_ld<u32x4>(S, adk + (i >> 2) * 4096 + (((4 * (i & 3) + j4) ^ n16) * 16));
_Pragma("unroll 1")
    for (int spin = 0; spin < SPIN_CAP && !__all(tok(qa, tag) & tok(qb, tag)); ++spin) { MEMCLOB(); qa = ldsc(rws, qoff); qb = ldsc(rws, qoff + 2048); }
    const float q0 = x32_sum(lo16(qa.x) + lo16(qb.x)), q1 = x32_sum(hi16(qa.x) + hi16(qb.x)), q2 = x32_sum(lo16(qa.z) + lo16(qb.z)), q3 = x32_sum(hi16(qa.z) + hi16(qb.z));
    if (l < 32) lds_st<u32x2>(S, a_qw, u32x2{pk(q0, q1), pk(q2, q3)});
    if (l == 0) set_flag(S, F_Q + w, 1);
  }
  STAMP(3);
  {
    u32x4 fl4, bq0, bq1, bq2, bq3;
#pragma unroll 1
    for (int spin = 0; spin < LSPIN; ++spin) {
      asm volatile("ds_read_b128 %0, %5\n\tds_read_b128 %1, %6\n\tds_read_b128 %2, %6 offset:64\n\tds_read_b128 %3, %6 offset:128\n\tds_read_b128 %4, %6 offset:192\n\ts_waitcnt lgkmcnt(0)"
                   : "=&v"(fl4), "=&v"(bq0), "=&v"(bq1), "=&v"(bq2), "=&v"(bq3) : "v"(a_fq), "v"(a_qs) : "memory");
      if (all4(fl4)) break;
    }
    f32x4 d0 = mfma32(kf[0], bq0, f32x4{0.f, 0.f, 0.f, 0.f}), d1 = mfma32(kf[4], bq0, f32x4{0.f, 0.f, 0.f, 0.f});
    d0 = mfma32(kf[1], bq1, d0); d1 = mfma32(kf[5], bq1, d1);
    d0 = mfma32(kf[2], bq2, d0); d1 = mfma32(kf[6], bq2, d1);
    d0 = mfma32(kf[3], bq3, d0); d1 = mfma32(kf[7], bq3, d1);
    constexpr float C2 = 0.08838834764831845f * 1.4426950408889634f;
    float sc[8];
#pragma unroll
    for (int i = 0; i < 4; ++i) { sc[i] = d0[i] * C2; sc[4 + i] = d1[i] * C2; }
    const float mw = xr_max(fmaxf(fmaxf(fmaxf(sc[0], sc[1]), fmaxf(sc[2], sc[3])), fmaxf(fmaxf(sc[4], sc[5]), fmaxf(sc[6], sc[7]))));
    float p[8];
#pragma unroll
    for (int i = 0; i < 8; ++i) p[i] = ex2(sc[i] - mw);
    if (n16 < 4) {
      lds_st<u32x2>(S, a_pw, u32x2{pk(p[0], p[1]), pk(p[2], p[3])});
      lds_st<u32x2>(S, a_pw + 32, u32x2{pk(p[4], p[5]), pk(p[6], p[7])});
      if (l < 4) lds_st<float>(S, a_mw, mw);
    }
    if (l == 0) set_flag(S, F_P + w, 1);
  }
  STAMP(4);
  {
    u32x4 fl4, ap[4], m4u;
#pragma unroll 1
    for (int spin = 0; spin < LSPIN; ++spin) {
      asm volatile("ds_read_b128 %0, %6\n\tds_read_b128 %1, %7\n\tds_read_b128 %2, %7 offset:64\n\tds_read_b128 %3, %7 offset:128\n\tds_read_b128 %4, %7 offset:192\n\tds_read_b128 %5, %8\n\ts_waitcnt lgkmcnt(0)"
                   : "=&v"(fl4), "=&v"(ap[0]), "=&v"(ap[1]), "=&v"(ap[2]), "=&v"(ap[3]), "=&v"(m4u) : "v"(a_fp), "v"(a_pr), "v"(a_mr) : "memory");
      if (all4(fl4)) break;
    }
    const float m0 = f_of(m4u.x), m1 = f_of(m4u.y), m2 = f_of(m4u.z), m3 = f_of(m4u.w);
    const float M = vmax(vmax3(m0, m1, m2), m3);
    const float al[4] = {ex2(m0 - M), ex2(m1 - M), ex2(m2 - M), ex2(m3 - M)};
    const u32x4 ones = {0x3f803f80u, 0x3f803f80u, 0x3f803f80u, 0x3f803f80u};
    float L = 0;
#pragma unroll
    for (int ks = 0; ks < 4; ++ks) L = fmaf(al[ks], mfma32(ap[ks], ones, f32x4{0.f, 0.f, 0.f, 0.f})[0], L);
    stsc(rws, a_st, u32x4{u_of(M), tag, u_of(L), tag});
    float o0 = 0, o1 = 0;
    wait_landed(S, seen, N_K + 24);
    {
      u32x2 vf[12];
#pragma unroll
      for (int i = 0; i < 12; ++i) vf[i] = lds_tr(S, ((i & 2) ? ad1 : ad0) + (i >> 2) * 8192 + (i & 1) * 1024);
#pragma unroll
      for (int ks = 0; ks < 3; ++ks) {
        o0 = fmaf(al[ks], mfma32(ap[ks], u32x4{vf[4 * ks].x, vf[4 * ks].y, vf[4 * ks + 1].x, vf[4 * ks + 1].y}, f32x4{0.f, 0.f, 0.f, 0.f})[0], o0);
        o1 = fmaf(al[ks], mfma32(ap[ks], u32x4{vf[4 * ks + 2].x, vf[4 * ks + 2].y, vf[4 * ks + 3].x, vf[4 * ks + 3].y}, f32x4{0.f, 0.f, 0.f, 0.f})[0], o1);
      }
    }
    wait_landed(S, seen, ST_AT);
    STAMP(10);
    if (w == 0 && l < 32) arrive(ws + WS_HB + (g * 32 + l) * 128, 1u << s);
    wait_landed(S, seen, N_V);
    {
      const u32x2 a0 = lds_tr(S, ad0 + 3 * 8192), a1 = lds_tr(S, ad0 + 3 * 8192 + 1024), c0 = lds_tr(S, ad1 + 3 * 8192), c1 = lds_tr(S, ad1 + 3 * 8192 + 1024);
      o0 = fmaf(al[3], mfma32(ap[3], u32x4{a0.x, a0.y, a1.x, a1.y}, f32x4{0.f, 0.f, 0.f, 0.f})[0], o0);
      o1 = fmaf(al[3], mfma32(ap[3], u32x4{c0.x, c0.y, c1.x, c1.y}, f32x4{0.f, 0.f, 0.f, 0.f})[0], o1);
    }
    stsc(rws, a_op, u32x4{u_of(o0), tag, u_of(o1), tag});
  }
  STAMP(5);
  {
    wait_ge(S, F_OOK, 1);
    u32x4 ov = ld1(rws, ooff);
    wait_landed(S, seen, N_WO);
    u32x2 bw[8];
#pragma unroll
    for (int tt = 0; tt < 8; ++tt) bw[tt] = lds_tr(S, adw ^ (tt << 5));
    u32 bp = 0;
    if (hasM) { wait_ge(S, F_B + w, 1); bp = lds_ld<u32>(S, O_BP + w * 256 + l * 4); }
    wait_ge(S, F_WOK, 1);
    const float wn = lds_ld<float>(S, O_WT + (l >> 1) * 4);
_Pragma("unroll 1")
    for (int spin = 0; spin < SPIN_CAP && !__all(tok(ov, tag)); ++spin) { DIAG(c3++); MEMCLOB(); ov = ldsc(rws, ooff); }
    if (t == 0) set_flag(S, F_OG, 1);
    STAMP(6);
    float o0 = wn * f_of(ov.x), o1 = wn * f_of(ov.z);
    o0 += dpp<XOR2>(o0); o1 += dpp<XOR2>(o1); o0 += dpp<ROR4>(o0); o1 += dpp<ROR4>(o1); o0 += dpp<ROR8>(o0); o1 += dpp<ROR8>(o1);
    o0 = x16_sum(o0); o1 = x16_sum(o1); o0 = x32_sum(o0); o1 = x32_sum(o1);
    const u32 pr = pk(o0, o1), po = __builtin_amdgcn_mov_dpp(pr, XOR1, 0xf, 0xf, true);
    if (l == 0) { MEMCLOB(); *(volatile LAS u32x4*)(S + O_AV + w * 16) = u32x4{pr, po, 1u, 1u}; MEMCLOB(); }
    u32x4 av;
#pragma unroll 1
    for (int spin = 0; spin < LSPIN; ++spin) { av = *(volatile LAS u32x4*)(S + O_AV + j4 * 16); if (__all(av.z != 0)) break; }
    const u32x2 a = {av.x, av.y};
    f32x4 d[8];
#pragma unroll
    for (int tt = 0; tt < 8; ++tt) d[tt] = mfma16(a, bw[tt], f32x4{0.f, 0.f, 0.f, 0.f});
    const bool b0 = j4 & 1, b1_ = j4 & 2;
    const float e0 = b1_ ? (b0 ? d[6][0] : d[4][0]) : (b0 ? d[2][0] : d[0][0]), e1 = b1_ ? (b0 ? d[7][0] : d[5][0]) : (b0 ? d[3][0] : d[1][0]);
    stsc(rws, a_sp, u32x4{pk(e0, e1), tag, bp, tag});
  }
  STAMP(7);
  if (b < NC) {
    const float b2f = f_of(b2h << 16), xff = f_of(xfh << 16);
    u32 a_fw = O_FIN + (l & 3) * 64 + w * 4;
    u32 a_ff = lb + O_FL + 4 * F_F, a_fr = lb + O_FIN + (l & 3) * 64 + ((l >> 2) & 1) * 16;
    asm volatile("" : "+v"(a_fw), "+v"(a_ff), "+v"(a_fr));
    wait_ge(S, F_FOK, 1);
    u32x4 v[4];
#pragma unroll
    for (int i = 0; i < 4; ++i) v[i] = ld1(rws, off0 + i * 1024);
_Pragma("unroll 1")
    for (int spin = 0; spin < SPIN_CAP && !__all(tok(v[0], tag) & tok(v[1], tag) & tok(v[2], tag) & tok(v[3], tag)); ++spin) {
      DIAG(c4++);
      MEMCLOB();
#pragma unroll
      for (int i = 0; i < 4; ++i) v[i] = ldsc(rws, off0 + i * 1024);
    }
    STAMP(8);
    float r[4];
    r[0] = (lo16(v[0].x) + lo16(v[1].x)) + (lo16(v[2].x) + lo16(v[3].x)); r[1] = (hi16(v[0].x) + hi16(v[1].x)) + (hi16(v[2].x) + hi16(v[3].x));
    r[2] = (lo16(v[0].z) + lo16(v[1].z)) + (lo16(v[2].z) + lo16(v[3].z)); r[3] = (hi16(v[0].z) + hi16(v[1].z)) + (hi16(v[2].z) + hi16(v[3].z));
#pragma unroll
    for (int i = 0; i < 4; ++i) r[i] += dpp<ROR4>(r[i]);
#pragma unroll
    for (int i = 0; i < 4; ++i) r[i] += dpp<ROR8>(r[i]);
#pragma unroll
    for (int i = 0; i < 4; ++i) r[i] = x16_sum(r[i]);
#pragma unroll
    for (int i = 0; i < 4; ++i) r[i] = x32_sum(r[i]);
    if (l < 4) {
#pragma unroll
      for (int i = 0; i < 4; ++i) lds_st<float>(S, a_fw + i * 16, r[i]);
    }
    if (l == 0) set_flag(S, F_F + w, 1);
    if (w == 0) {
      u32x4 fl4, s4, m4;
#pragma unroll 1
      for (int spin = 0; spin < LSPIN; ++spin) {
        asm volatile("ds_read_b128 %0, %3\n\tds_read_b128 %1, %4\n\tds_read_b128 %2, %4 offset:32\n\ts_waitcnt lgkmcnt(0)"
                     : "=&v"(fl4), "=&v"(s4), "=&v"(m4) : "v"(a_ff), "v"(a_fr) : "memory");
        if (all4(fl4)) break;
      }
      const float sv = (f_of(s4.x) + f_of(s4.y)) + (f_of(s4.z) + f_of(s4.w)), mv = (f_of(m4.x) + f_of(m4.y)) + (f_of(m4.z) + f_of(m4.w));
      const float B = rbf(rbf(mv) + b2f);
      const float gt = rbf(B * rcp(1.f + ex2(-B * 1.4426950408889634f)));
      const float o = xff + rbf(rbf(sv) * gt);
      if (l < 8) out[fcol] = (u16)pk(o, o);
      if (b == 0 && l == 0) __builtin_amdgcn_raw_buffer_store_b32(epoch + 1u, rws, WS_EPOCH, 0, AUX_SC1);
    }
  }
  STAMP(9);
  __builtin_amdgcn_endpgm();
  }

  __builtin_amdgcn_s_setprio(3);
  const u32 fl = (u32)(size_t)(S + O_FL + 4 * F_LANDED);
  asm volatile("" :: "s"(kc), "s"(vc), "s"(w1), "s"(w2), "s"(wo), "s"(x), "s"(ws), "s"(b1), "s"(b2));
  STAMP(3);
  {
    __builtin_amdgcn_raw_ptr_buffer_load_lds(mk(x, 1024), (LAS void*)(S + O_X), 16, l * 16, 0, 0, 0);
    const u32 f0 = g * 256 + (s >> 2) * 32 + (s & 3) * 4, c0 = ((b & 63) >> 4) * 128 + ((b >> 2) & 3) * 32 + (b & 3) * 4;
    const char* p = (const char*)ws;
    if (l == 1 || l == 2) p = (const char*)b1 + (((hasM ? f0 : 0) + (l - 1) * 16) * 2 & ~15u);
    if (l == 3 || l == 4) p = (const char*)b2 + ((c0 + (l - 3) * 16) * 2 & ~15u);
    __builtin_amdgcn_global_load_lds((const __attribute__((address_space(1))) void*)p, (LAS void*)(S + O_MISC), 16, 0, 0);
  }
  {
    const rsrc_t r1 = mk(w1, hasM ? 512 * FF * 2 : 0);
    const u32 hb = l >> 5, bl = hb * (FF * 2) + (((l & 31) ^ (hb << 1)) * 16);
    const u32 base = (s * 16) * (FF * 2) + g * 512;
#pragma unroll 1
    for (int G = 0; G < 2; ++G) dma4(r1, S, O_W1 + G * 4096, bl, (bl ^ 64) + 6144, (bl ^ 128) + 2 * 6144, (bl ^ 192) + 3 * 6144, base + G * 8 * (FF * 2));
  }
  const size_t kvoff = ((size_t)g * SK + (SK - WIN) + s * 128) * 256;
  {
    const rsrc_t rk = mk((const char*)kc + kvoff, 32768);
    const u32 v0 = j4 * 256 + ((n16 ^ j4) * 16);
    wait_until(tstart + K_T);
#pragma unroll 1
    for (int G = 0; G < 8; ++G) { landed<WINDOW - 4>(fl, N_W1 + 4 * G - (WINDOW - 4)); dma4(rk, S, O_K + G * 4096, v0, v0 ^ 64, v0 ^ 128, v0 ^ 192, G * 4096); }
  }
  STAMP(4);
  {
    const rsrc_t rv = mk((const char*)vc + kvoff, 32768);
    const u32 v0 = j4 * 256 + ((n16 ^ (j4 << 1)) * 16);
#pragma unroll 1
    for (int G = 0; G < 8; ++G) { landed<WINDOW - 4>(fl, N_K + 4 * G - (WINDOW - 4)); dma4(rv, S, O_V + G * 4096, v0, v0, v0 ^ 128, v0 ^ 128, G * 4096); }
  }
  STAMP(5);
  {
    const rsrc_t r2 = mk((const char*)w2 - 1024, hasM ? 512 * FF * 2 + 1024 : 0);
    const u32 f0 = g * 256 + (s >> 2) * 32 + (s & 3) * 4;
    const rsrc_t ro = mk((const char*)wo - 1024, 4096 * 512 * 2 + 1024);
    const u32 r0 = g * 512 + (s >> 3) * 128 + ((s >> 1) & 3) * 32 + (s & 1) * 8;
#pragma unroll 1
    for (int k = 0; k < 6; ++k) {
      if (POSTMODE == 0) landed<WINDOW2 - 4>(fl, N_V + 4 * k - (WINDOW2 - 4));
      const bool is_w2 = WO_FIRST ? k >= 4 : k < 2;
      const int G = WO_FIRST ? (k & 3) : (k < 2 ? k : k - 2);
      if (is_w2) {
        const int G2 = G & 1;
        const u32 v = (l * 16) ^ (G2 << 7);
        dma4(r2, S, O_W2 + G2 * 4096, v + 1024, (v ^ 32) + 16384, v ^ 64, (v ^ 96) + 15360, (f0 + 2 * G2) * 1024);
      } else {
        const u32 v = (l * 16) ^ ((G & 1) << 7);
        dma4(ro, S, O_WO + G * 4096, v + 1024, (v ^ 32) + 16384, v ^ 64, (v ^ 96) + 15360, (r0 + 2 * G) * 1024);
      }
    }
  }
  STAMP(1);
  drain<20>(fl, N_END);
  STAMP(2);
}

extern "C" void kernel_launch(void* const* d_in, const int* in_sizes, int n_in, void* d_out, int out_size, void* d_ws, size_t ws_size, hipStream_t stream) {
  fused_decode<<<NWG, NT, 0, stream>>>((const u16*)d_in[0], (const u16*)d_in[1], (const u16*)d_in[2], (const u16*)d_in[3], (const u16*)d_in[4],
                                       (const u16*)d_in[6], (const u16*)d_in[7], (const u16*)d_in[8], (const u16*)d_in[9], (u16*)d_out, (unsigned char*)d_ws);
}
```

```cpp
#include <hip/hip_runtime.h>
#include <stdint.h>

typedef unsigned short u16;
typedef unsigned u32;
typedef unsigned long long u64;
typedef u32 u32x4 __attribute__((ext_vector_type(4)));
typedef u32 u32x2 __attribute__((ext_vector_type(2)));
typedef float f32x4 __attribute__((ext_vector_type(4)));
typedef float f32x2 __attribute__((ext_vector_type(2)));
typedef short s16x4 __attribute__((ext_vector_type(4)));
typedef __bf16 bf16x8 __attribute__((ext_vector_type(8)));
typedef __bf16 bf16x2 __attribute__((ext_vector_type(2)));
typedef __amdgpu_buffer_rsrc_t rsrc_t;
#define LAS __attribute__((address_space(3)))

constexpr int SK = 65536, WIN = 4096, FF = 1792;
constexpr int NWG = 256, NT = 640;
constexpr int NC = 64;
constexpr u32 WS_EPOCH = 0;
constexpr u32 WS_QP = 4096;
constexpr u32 WS_HB = WS_QP + 8 * 4 * 256 * 8;
constexpr u32 WS_ST = WS_HB + 256 * 128;
constexpr u32 WS_OP = WS_ST + 256 * 512;
constexpr u32 WS_HP = WS_OP + 256 * 32 * 128;
constexpr u32 WS_SB = WS_HP + 224 * 32 * 64;
constexpr u32 WS_END = WS_SB + NC * 256 * 64;
constexpr u32 OOB = 0x80000000u;
constexpr u32 O_WQ = 0, O_K = 16384, O_V = 49152, O_W1 = 81920, O_W2 = 90112, O_WO = 98304, O_S = 114688;
constexpr u32 O_X = O_S, O_MISC = O_X + 1024, O_S2 = O_MISC + 1024;
constexpr u32 O_XN = O_S2, O_Y = O_XN + 1024, O_QS = O_Y + 1024, O_P = O_QS + 1024, O_MW = O_P + 1024, O_LW = O_MW + 64, O_AV = O_LW + 64,
              O_EP = O_AV + 64, O_WT = O_EP + 16, O_HV = O_WT + 128, O_HX = O_HV + 64, O_FIN = O_HX + 128, O_BP = O_FIN + 512, O_FL = O_BP + 1024, LDS_BYTES = O_FL + 256;
constexpr u32 O_INIT = O_AV;
enum { F_LANDED = 0, F_OOK = 2, F_HOK = 3, F_FOK = 4, F_WOK = 5, F_OG = 6, F_XN = 7, F_Y = 8, F_INIT = 9, F_H = 32, F_Q = 12, F_P = 16, F_WQ = 20, F_F = 24, F_B = 28 };
constexpr int N_X = 2, N_W1 = N_X + 8, N_K = N_W1 + 32, N_V = N_K + 32, N_END = N_V + 24;
#define WO_FIRST 0
constexpr int N_W2 = WO_FIRST ? N_END : N_V + 8, N_WO = WO_FIRST ? N_V + 16 : N_END;
#define WINDOW 24
#define WINDOW2 24
#define Q_AT (N_X + 20)
#define ST_AT (N_V - 4)
#define HH_AT N_V
#define POSTMODE 0
#define HOLD 45
#define K_T 90
#define Q_D 75
#define H_D 100
#define POLL_AT N_V
#define CODE_BYTES 11520
#define SPINSLEEP 0
constexpr int AUX_NT = 2, AUX_SC1 = 16;
#define LSPIN (1 << 22)
#define GSPIN (1 << 14)
constexpr int SPIN_CAP = GSPIN;

#define DIAG(x)
#define KEEP(x) asm volatile("" :: "v"(x))
#define MEMCLOB() asm volatile("" ::: "memory")
#define STAMP(k) do { } while (0)

__device__ __forceinline__ rsrc_t mk(const void* p, u32 bytes) { return __builtin_amdgcn_make_buffer_rsrc((void*)p, 0, bytes, 0x00020000); }
__device__ __forceinline__ u32x4 ldsc(rsrc_t r, u32 voff) { return __builtin_amdgcn_raw_buffer_load_b128(r, voff, 0, AUX_SC1); }
#define FIRST_AUX 16
__device__ __forceinline__ u32x4 ld1(rsrc_t r, u32 voff) { return __builtin_amdgcn_raw_buffer_load_b128(r, voff, 0, FIRST_AUX); }
__device__ __forceinline__ void stsc(rsrc_t r, u32 voff, u32x4 v) { __builtin_amdgcn_raw_buffer_store_b128(v, r, voff, 0, AUX_SC1); }
__device__ __forceinline__ void stsc2(rsrc_t r, u32 voff, u32x2 v) { __builtin_amdgcn_raw_buffer_store_b64(v, r, voff, 0, AUX_SC1); }
__device__ __forceinline__ float f_of(u32 u) { return __builtin_bit_cast(float, u); }
__device__ __forceinline__ u32 u_of(float f) { return __builtin_bit_cast(u32, f); }
__device__ __forceinline__ float lo16(u32 w) { return f_of(w << 16); }
__device__ __forceinline__ float hi16(u32 w) { return f_of(w & 0xffff0000u); }
__device__ __forceinline__ u32 pk(float lo, float hi) { bf16x2 r = {(__bf16)lo, (__bf16)hi}; return __builtin_bit_cast(u32, r); }
__device__ __forceinline__ float rbf(float f) { return lo16(pk(f, f)); }
template <int CTRL> __device__ __forceinline__ float dpp(float x) { return f_of(__builtin_amdgcn_mov_dpp(u_of(x), CTRL, 0xf, 0xf, true)); }
constexpr int XOR1 = 0xB1, XOR2 = 0x4E, HMIR = 0x141, MIR = 0x140, ROR4 = 0x124, ROR8 = 0x128;
__device__ __forceinline__ float x16_sum(float x) { auto s = __builtin_amdgcn_permlane16_swap(u_of(x), u_of(x), false, false); return f_of(s[0]) + f_of(s[1]); }
__device__ __forceinline__ float x32_sum(float x) { auto s = __builtin_amdgcn_permlane32_swap(u_of(x), u_of(x), false, false); return f_of(s[0]) + f_of(s[1]); }
__device__ __forceinline__ float x16_max(float x) { auto s = __builtin_amdgcn_permlane16_swap(u_of(x), u_of(x), false, false); return fmaxf(f_of(s[0]), f_of(s[1])); }
__device__ __forceinline__ float x32_max(float x) { auto s = __builtin_amdgcn_permlane32_swap(u_of(x), u_of(x), false, false); return fmaxf(f_of(s[0]), f_of(s[1])); }
__device__ __forceinline__ float row_sum(float x) { x += dpp<XOR1>(x); x += dpp<XOR2>(x); x += dpp<HMIR>(x); x += dpp<MIR>(x); return x; }
__device__ __forceinline__ float wave_sum(float x) { return x32_sum(x16_sum(row_sum(x))); }
__device__ __forceinline__ float vmax(float a, float b) { float r; asm("v_max_f32 %0, %1, %2" : "=v"(r) : "v"(a), "v"(b)); return r; }
__device__ __forceinline__ float vmax3(float a, float b, float c) { float r; asm("v_max3_f32 %0, %1, %2, %3" : "=v"(r) : "v"(a), "v"(b), "v"(c)); return r; }
#define DPPMAX(x, ctrl) asm("s_nop 1\n\tv_max_f32_dpp %0, %0, %0 " ctrl " row_mask:0xf bank_mask:0xf" : "+v"(x))
__device__ __forceinline__ float xr_max(float x) {
  auto s = __builtin_amdgcn_permlane16_swap(u_of(x), u_of(x), false, false); x = vmax(f_of(s[0]), f_of(s[1]));
  auto t = __builtin_amdgcn_permlane32_swap(u_of(x), u_of(x), false, false); return vmax(f_of(t[0]), f_of(t[1]));
}
__device__ __forceinline__ float par_sum(float x) { x += dpp<XOR2>(x); x += dpp<ROR4>(x); x += dpp<ROR8>(x); return x32_sum(x16_sum(x)); }
__device__ __forceinline__ float par_max(float x) { DPPMAX(x, "quad_perm:[2,3,0,1]"); DPPMAX(x, "row_ror:4"); DPPMAX(x, "row_ror:8"); return xr_max(x); }
__device__ __forceinline__ float ex2(float x) { return __builtin_amdgcn_exp2f(x); }
__device__ __forceinline__ float rcp(float x) { return __builtin_amdgcn_rcpf(x); }
__device__ __forceinline__ f32x4 mfma32(u32x4 a, u32x4 b, f32x4 c) {
  return __builtin_amdgcn_mfma_f32_16x16x32_bf16(__builtin_bit_cast(bf16x8, a), __builtin_bit_cast(bf16x8, b), c, 0, 0, 0);
}
__device__ __forceinline__ f32x4 mfma16(u32x2 a, u32x2 b, f32x4 c) {
  return __builtin_amdgcn_mfma_f32_16x16x16bf16_1k(__builtin_bit_cast(s16x4, a), __builtin_bit_cast(s16x4, b), c, 0, 0, 0);
}
typedef LAS unsigned char* lds_t;
template <class T> __device__ __forceinline__ T lds_ld(lds_t s, u32 off) { return *(const LAS T*)(s + off); }
template <class T> __device__ __forceinline__ void lds_st(lds_t s, u32 off, T v) { *(LAS T*)(s + off) = v; }
__device__ __forceinline__ u32x2 lds_tr(lds_t s, u32 off) { return __builtin_bit_cast(u32x2, __builtin_amdgcn_ds_read_tr16_b64_v4i16((LAS s16x4*)(s + off))); }
__device__ __forceinline__ void set_flag(lds_t s, int f, u32 v) { MEMCLOB(); *(volatile LAS u32*)(s + O_FL + 4 * f) = v; MEMCLOB(); }
__device__ __forceinline__ void wait_ge(lds_t s, int f, int need) {
#pragma unroll 1
  for (int spin = 0; spin < LSPIN; ++spin) {
    const int v = __builtin_amdgcn_readfirstlane(*(volatile LAS int*)(s + O_FL + 4 * f));
    if (v >= need) break;
    if (SPINSLEEP) __builtin_amdgcn_s_sleep(SPINSLEEP);
  }
  MEMCLOB();
}
__device__ __forceinline__ void wait_landed(lds_t s, int& seen, int need) {
  if (seen < need) {
#pragma unroll 1
    for (int spin = 0; spin < LSPIN; ++spin) {
      seen = __builtin_amdgcn_readfirstlane(*(volatile LAS int*)(s + O_FL + 4 * F_LANDED));
      if (seen >= need) break;
      if (SPINSLEEP) __builtin_amdgcn_s_sleep(SPINSLEEP);
    }
  }
  MEMCLOB();
}
__device__ __forceinline__ bool all4(u32x4 f) { return __builtin_amdgcn_readfirstlane(f.x & f.y & f.z & f.w) != 0; }
__device__ __forceinline__ void wait4(lds_t s, int f) {
#pragma unroll 1
  for (int spin = 0; spin < LSPIN; ++spin) {
    const u32x4 v = *(volatile LAS u32x4*)(s + O_FL + 4 * f);
    if (__builtin_amdgcn_readfirstlane(v.x & v.y & v.z & v.w)) break;
  }
  MEMCLOB();
}
__device__ __forceinline__ u32x4 ln8(u32x4 xr) {
  float v[8];
#pragma unroll
  for (int i = 0; i < 4; ++i) { v[2 * i] = lo16(xr[i]); v[2 * i + 1] = hi16(xr[i]); }
  const float s = ((v[0] + v[1]) + (v[2] + v[3])) + ((v[4] + v[5]) + (v[6] + v[7]));
  const float m = wave_sum(s) * (1.f / 512);
  float q0 = 0, q1 = 0;
#pragma unroll
  for (int i = 0; i < 4; ++i) { const float d0 = v[2 * i] - m, d1 = v[2 * i + 1] - m; q0 = fmaf(d0, d0, q0); q1 = fmaf(d1, d1, q1); }
  const float var = wave_sum(q0 + q1) * (1.f / 512);
  const float mb = rbf(m);
  const float inv = rcp(rbf(__builtin_amdgcn_sqrtf(rbf(rbf(var) + 1e-5f))));
  u32x4 r;
#pragma unroll
  for (int i = 0; i < 4; ++i) { const u32 d = pk(v[2 * i] - mb, v[2 * i + 1] - mb); r[i] = pk(lo16(d) * inv, hi16(d) * inv); }
  return r;
}
__device__ __forceinline__ u64 poll64(const unsigned char* p) {
  u64 r; const u64 z = 0;
  asm volatile("global_atomic_or_x2 %0, %1, %2, off sc0 sc1\n\ts_waitcnt vmcnt(0)" : "=&v"(r) : "v"(p), "v"(z) : "memory");
  return r;
}
__device__ __forceinline__ void poll_word(const unsigned char* p, u32 tag, int& count) {
  int n;
  asm volatile(
    "v_mov_b32 v108, 0\n\tv_mov_b32 v109, 0\n\t"
    "s_mov_b64 exec, 1\n\t"
    "global_atomic_or_x2 v[104:105], %1, v[108:109], off sc0 sc1\n\t"
    "s_sleep 4\n\t"
    "global_atomic_or_x2 v[106:107], %1, v[108:109], off sc0 sc1\n\t"
    "s_mov_b32 %0, 0\n"
    "1:\n\t"
    "s_waitcnt vmcnt(1)\n\t"
    "v_not_b32 v104, v104\n\tv_xor_b32 v105, %2, v105\n\tv_or_b32 v104, v104, v105\n\t"
    "v_cmp_ne_u32 vcc, 0, v104\n\t"
    "s_cbranch_vccz 2f\n\t"
    "global_atomic_or_x2 v[104:105], %1, v[108:109], off sc0 sc1\n\t"
    "s_waitcnt vmcnt(1)\n\t"
    "v_not_b32 v106, v106\n\tv_xor_b32 v107, %2, v107\n\tv_or_b32 v106, v106, v107\n\t"
    "v_cmp_ne_u32 vcc, 0, v106\n\t"
    "s_cbranch_vccz 2f\n\t"
    "global_atomic_or_x2 v[106:107], %1, v[108:109], off sc0 sc1\n\t"
    "s_add_u32 %0, %0, 1\n\t"
    "s_cmp_lt_u32 %0, %3\n\t"
    "s_cbranch_scc1 1b\n"
    "2:\n\t"
    "s_mov_b64 exec, -1"
    : "=&s"(n) : "v"(p), "v"(tag), "s"(SPIN_CAP) : "vcc", "scc", "v104", "v105", "v106", "v107", "v108", "v109", "memory");
  DIAG(count += n);
}
__device__ __forceinline__ void arrive(unsigned char* p, u32 bit) {
  __hip_atomic_fetch_or((u64*)p, (u64)bit, __ATOMIC_RELAXED, __HIP_MEMORY_SCOPE_AGENT);
}
#define TSLEEP 0
__device__ __forceinline__ void wait_until(u32 t) { while ((int)((u32)__builtin_amdgcn_s_memrealtime() - t) < 0) { if (TSLEEP) __builtin_amdgcn_s_sleep(TSLEEP); } }
__device__ __forceinline__ void dma4(rsrc_t r, lds_t s, u32 ldsoff, u32 v0, u32 v1, u32 v2, u32 v3, u32 soff) {
  __builtin_amdgcn_raw_ptr_buffer_load_lds(r, (LAS void*)(s + ldsoff), 16, v0, soff, 0, AUX_NT);
  __builtin_amdgcn_raw_ptr_buffer_load_lds(r, (LAS void*)(s + ldsoff), 16, v1, soff, 1024, AUX_NT);
  __builtin_amdgcn_raw_ptr_buffer_load_lds(r, (LAS void*)(s + ldsoff), 16, v2, soff, 2048, AUX_NT);
  __builtin_amdgcn_raw_ptr_buffer_load_lds(r, (LAS void*)(s + ldsoff), 16, v3, soff, 3072, AUX_NT);
}
template <int N> __device__ __forceinline__ void landed(u32 flag_addr, int v) {
  asm volatile("s_waitcnt vmcnt(%0)\n\tds_write_b32 %1, %2" :: "i"(N), "v"(flag_addr), "v"(v) : "memory");
}
template <int N> __device__ __forceinline__ void drain(u32 flag_addr, int total) {
  landed<N>(flag_addr, total - N);
  if constexpr (N >= 4) drain<N - 4>(flag_addr, total);
}
__device__ __forceinline__ u32 msk(bool c) { return c ? 0u : OOB; }
__device__ __forceinline__ bool tok(u32x4 v, u32 tag) { return (v.y == tag) & (v.w == tag); }
__device__ __forceinline__ u32 mktag(u32 epoch) { return ((epoch + 1u) * 0x9E3779B1u) ^ 0x5bd1e995u; }

__global__ void __launch_bounds__(NT) fused_decode(const u16* __restrict__ x, const u16* __restrict__ kc, const u16* __restrict__ vc,
                                                   const u16* __restrict__ wq, const u16* __restrict__ wo, const u16* __restrict__ w1,
                                                   const u16* __restrict__ b1, const u16* __restrict__ w2, const u16* __restrict__ b2,
                                                   u16* __restrict__ out, unsigned char* __restrict__ ws) {
  __shared__ __attribute__((aligned(16))) unsigned char smem[LDS_BYTES];
  lds_t S = (lds_t)smem;
  STAMP(0);
  const u32 tstart = (u32)__builtin_amdgcn_s_memrealtime();
  const int t = threadIdx.x, l = t & 63, wv = __builtin_amdgcn_readfirstlane(t >> 6);
  if (wv == 0) {
    lds_st<u32>(S, O_FL + 4 * l, 0u); if (l < 16) lds_st<u32>(S, O_AV + 4 * l, 0u);
    asm volatile("s_waitcnt lgkmcnt(0)" ::: "memory");
  }
  asm volatile("s_load_dword s94, %0, 0x0\n\ts_load_dword s95, %0, 0x40" :: "s"(__builtin_amdgcn_kernarg_segment_ptr()) : "s94", "s95", "memory");
  const rsrc_t rcode = mk((const void*)(__builtin_amdgcn_s_getpc() & ~255ull), CODE_BYTES);
  {
    const u32 sel = (blockIdx.x >> 3) + wv;
    asm volatile("buffer_load_dwordx4 v[120:123], %0, %1, 0 offen\n\tbuffer_load_dwordx4 v[120:123], %2, %1, 0 offen"
                 :: "v"((l * 16 + (wv + 1) * 1024) | ((sel & 3) ? OOB : 0u)), "s"(rcode), "v"((l * 16 + (wv + 11) * 1024) | ((sel & 3) ? OOB : 0u))
                 : "v120", "v121", "v122", "v123", "memory");
  }
  asm volatile("s_barrier" ::: "memory");
  const int b = blockIdx.x, g = b & 7, s = b >> 3;
  const int n16 = l & 15, j4 = l >> 4, q4 = (l >> 2) & 3, p4 = l & 3;
  const bool hasM = g < 7;
  const int me = g * 32 + s;
  const rsrc_t rws = mk(ws, WS_END);
  if (wv < 4) {
    if (wv == 0) {
      const rsrc_t rx = mk(x, 1024);
      asm volatile("buffer_load_dwordx4 v[110:113], %0, %1, 0 offen\n\tbuffer_load_dword v114, off, %2, 0"
                   :: "v"(l * 16), "s"(rx), "s"(rws) : "v110", "v111", "v112", "v113", "v114", "memory");
    }
    {
      const rsrc_t rq = mk(wq, 512 * 4096 * 2);
      const u32 base = ((s & 3) * 128 + 32 * wv) * 8192 + (g * 512 + (s >> 2) * 64) * 2;
      const u32 v0 = (l >> 3) * 8192 + (((l & 7) ^ (((l >> 4) & 1) << 1)) * 16), v1 = (v0 ^ 64) + 64512;
      const u32 m = (u32)(size_t)(S + O_WQ) + wv * 4096;
      asm volatile("s_mov_b32 m0, %0\n\ts_nop 0\n\t"
                   "buffer_load_dwordx4 %1, %5, %6 offen nt lds\n\tbuffer_load_dwordx4 %2, %5, %6 offen offset:1024 nt lds\n\t"
                   "buffer_load_dwordx4 %3, %5, %6 offen offset:2048 nt lds\n\tbuffer_load_dwordx4 %4, %5, %6 offen offset:3072 nt lds"
                   :: "s"(m), "v"(v0), "v"(v1), "v"(v0 + 2 * 64512), "v"(v1 + 2 * 64512), "s"(rq), "s"(base) : "memory");
    }
  }

  if (wv != 8) {
  asm volatile("" :: "s"(out), "s"(kc), "s"(vc), "s"(wo), "s"(w2));

  if (wv == 9) {
    int c1 = 0, c2 = 0;
    u32 soff = (WS_ST + (me * 32 + (l & 31)) * 16) | msk(l < 32);
    const u32x4 warm = ldsc(rws, soff);
    wait_ge(S, F_XN, 1);
    const u32 tag = mktag(lds_ld<u32>(S, O_EP));
    KEEP(warm);
    stsc(rws, (WS_HB + ((l & 4) ? (b & 63) : me) * 128 + ((l & 4) ? 64 : 0) + (l & 3) * 16) | msk(l < 4 || (l < 8 && b < NC)), u32x4{0u, tag, 0u, tag});
    wait_ge(S, F_LANDED, POLL_AT);
    STAMP(3);
    poll_word(ws + WS_HB + me * 128, tag, c1);
    set_flag(S, F_OOK, 1);
    STAMP(4);
    {
      u32x4 sv = ldsc(rws, soff);
_Pragma("unroll 1")
      for (int spin = 0; spin < SPIN_CAP && !__all(tok(sv, tag) | (l >= 32)); ++spin) { MEMCLOB(); sv = ldsc(rws, soff); }
      float mp = f_of(sv.x);
      float Mx = mp;
      DPPMAX(Mx, "quad_perm:[1,0,3,2]"); DPPMAX(Mx, "quad_perm:[2,3,0,1]"); DPPMAX(Mx, "row_half_mirror"); DPPMAX(Mx, "row_mirror");
      { auto r = __builtin_amdgcn_permlane16_swap(u_of(Mx), u_of(Mx), false, false); Mx = vmax(f_of(r[0]), f_of(r[1])); }
      const float wp = ex2(mp - Mx);
      const float wn = wp * rcp(x16_sum(row_sum(wp * f_of(sv.z))));
      if (l < 32) lds_st<float>(S, O_WT + l * 4, wn);
    }
    set_flag(S, F_WOK, 1);
    STAMP(1);
    if ((s & 7) == 0) {
      wait_ge(S, F_OG, 1);
      arrive(ws + WS_HB + l * 128 + 64, 1u << (g * 4 + (s >> 3)));
    }
    if (b < NC) {
      poll_word(ws + WS_HB + b * 128 + 64, tag, c2);
      set_flag(S, F_FOK, 1);
    }
    STAMP(2);
    __builtin_amdgcn_endpgm();
  }

  if (wv >= 4 && !hasM) __builtin_amdgcn_endpgm();
  if (wv == 0) {
    u32x4 xr; u32 ep;
    asm volatile("s_waitcnt vmcnt(4)\n\tv_mov_b32 %0, v110\n\tv_mov_b32 %1, v111\n\tv_mov_b32 %2, v112\n\tv_mov_b32 %3, v113\n\tv_mov_b32 %4, v114"
                 : "=v"(xr.x), "=v"(xr.y), "=v"(xr.z), "=v"(xr.w), "=v"(ep) :: "memory");
    lds_st<u32x4>(S, O_XN + l * 16, ln8(xr));
    if (l == 0) { lds_st<u32>(S, O_EP, ep); set_flag(S, F_XN, 1); }
  }
  wait_ge(S, F_XN, 1);
  const u32 epoch = lds_ld<u32>(S, O_EP);

  if (wv >= 4) {
    const int w = wv - 4;
    const u32 f0 = g * 256 + (s >> 2) * 32 + (s & 3) * 4;
    if (w == 0) { lds_st<u32x4>(S, O_Y + l * 16, ln8(lds_ld<u32x4>(S, O_XN + l * 16))); if (l == 0) set_flag(S, F_Y, 1); }
    wait_ge(S, F_Y, 1);
    const u32x2 ay = lds_ld<u32x2>(S, O_Y + (s * 16 + 4 * j4) * 2);
    wait_ge(S, F_LANDED, N_X);
    const u32 b1v = lds_ld<u16>(S, O_MISC + 16 + ((l & 4) ? 16 : 0) + ((f0 * 2) & 15) + p4 * 2);
    const u32 tag = mktag(epoch);
    u32 a_hp = (WS_HP + ((g * 32 + (w * 2 + (j4 & 1)) * 4 + (n16 >> 2)) * 32 + s) * 64 + (n16 & 3) * 16) | msk(l < 32);
    u32 a_bp = O_BP + w * 256 + l * 4;
    u32 hoff = (WS_HP + (me * 32 + 8 * w + ((l >> 2) & 7)) * 64 + (l & 3) * 16) | msk(l < 32);
    u32 ad1 = O_W1 + (4 * j4 + q4) * 512 + (((8 * w + (p4 >> 1)) ^ (((4 * j4 + q4) & 7) << 1)) * 16) + (p4 & 1) * 8;
    u32 ad2 = O_W2 + ((4 * j4 + q4) & 7) * 1024 + (((16 * w + (p4 >> 1)) ^ (((4 * j4 + q4) & 7) << 1)) * 16) + (p4 & 1) * 8;
    float b1f = f_of(b1v << 16);
    asm volatile("" : "+v"(a_hp), "+v"(a_bp), "+v"(hoff), "+v"(ad1), "+v"(ad2), "+v"(b1f));
    wait_ge(S, F_LANDED, N_W1);
    {
      f32x4 d[4];
#pragma unroll
      for (int tt = 0; tt < 4; ++tt) d[tt] = mfma16(ay, lds_tr(S, ad1 ^ (tt << 5)), f32x4{0.f, 0.f, 0.f, 0.f});
      const bool up = j4 & 1;
      stsc(rws, a_hp, u32x4{u_of(up ? d[2][0] : d[0][0]), tag, u_of(up ? d[3][0] : d[1][0]), tag});
    }
    STAMP(3);
    wait_until((u32)__builtin_amdgcn_s_memrealtime() + H_D);
    u32x4 h0 = ld1(rws, hoff);
_Pragma("unroll 1")
    for (int spin = 0; spin < SPIN_CAP && !__all(tok(h0, tag) | (l >= 32)); ++spin) { MEMCLOB(); h0 = ldsc(rws, hoff); }
    STAMP(4);
    {
      float ha = f_of(h0.x), hb = f_of(h0.z);
      ha += dpp<ROR4>(ha); hb += dpp<ROR4>(hb); ha += dpp<ROR8>(ha); hb += dpp<ROR8>(hb);
      ha = x16_sum(ha); hb = x16_sum(hb);
      if (l < 4) { lds_st<float>(S, O_HX + l * 16 + w * 4, ha); lds_st<float>(S, O_HX + 64 + l * 16 + w * 4, hb); }
      if (l == 0) set_flag(S, F_H + w, 1);
      wait4(S, F_H);
      const f32x4 h4 = lds_ld<f32x4>(S, O_HX + ((l & 4) ? 64 : 0) + p4 * 16);
      hb = ha = (h4[0] + h4[1]) + (h4[2] + h4[3]);
      const float p = rbf(rbf((l & 4) ? hb : ha) + b1f);
      const float u = p * fmaf(0.044715f * 0.7978845608028654f * 2.8853900817779268f, p * p, 0.7978845608028654f * 2.8853900817779268f);
      const float h = p - p * rcp(ex2(u) + 1.f);
      if (l < 8) lds_st<u16>(S, O_HV + w * 16 + (p4 * 2 + (l >> 2)) * 2, (u16)pk(h, h));
    }
    u32x2 ah = lds_ld<u32x2>(S, O_HV + w * 16 + (j4 & 1) * 8);
    if (j4 >= 2) ah = u32x2{0u, 0u};
    wait_ge(S, F_LANDED, N_W2);
    {
      f32x4 d[8];
#pragma unroll
      for (int tt = 0; tt < 8; ++tt) d[tt] = mfma16(ah, lds_tr(S, ad2 ^ (tt << 5)), f32x4{0.f, 0.f, 0.f, 0.f});
      const bool b0 = j4 & 1, b1_ = j4 & 2;
      const float e0 = b1_ ? (b0 ? d[6][0] : d[4][0]) : (b0 ? d[2][0] : d[0][0]), e1 = b1_ ? (b0 ? d[7][0] : d[5][0]) : (b0 ? d[3][0] : d[1][0]);
      lds_st<u32>(S, a_bp, pk(e0, e1));
      if (l == 0) set_flag(S, F_B + w, 1);
    }
    STAMP(5);
    __builtin_amdgcn_endpgm();
  }

  const int w = wv;
  int c3 = 0, c4 = 0;
  int seen = -100;
  const int fcol = (b >> 4) * 128 + ((b >> 2) & 3) * 32 + (b & 3) * 4 + p4 + ((l >> 2) & 1) * 16;
  const u32 tag = mktag(epoch);
  STAMP(1);
  const u32 lb = (u32)(size_t)S;
  {
    u32x4 ax[4];
#pragma unroll
    for (int ks = 0; ks < 4; ++ks) ax[ks] = lds_ld<u32x4>(S, O_XN + ((s & 3) * 128 + 32 * ks + 8 * j4) * 2);
    const u32 gq = (q4 >> 1) | ((j4 & 1) << 1);
    u32 ad = O_WQ + (8 * j4 + q4) * 128 + (((2 * w + (p4 >> 1)) ^ (gq << 1)) * 16) + (p4 & 1) * 8;
    u32 a_qp = (WS_QP + ((g * 4 + (s & 3)) * 256 + (s >> 2) * 32 + w * 8 + (n16 >> 1)) * 8) | msk(l < 16 && (l & 1) == 0);
    asm volatile("" : "+v"(ad), "+v"(a_qp), "+v"(ax[0]), "+v"(ax[1]), "+v"(ax[2]), "+v"(ax[3]));
    asm volatile("s_waitcnt vmcnt(0)" ::: "memory");
    if (l == 0) set_flag(S, F_WQ + w, 1);
    wait4(S, F_WQ);
    u32x2 bb[8];
#pragma unroll
    for (int i = 0; i < 8; ++i) bb[i] = lds_tr(S, ad + (i >> 1) * 4096 + (i & 1) * 512);
    f32x4 d = {0.f, 0.f, 0.f, 0.f};
#pragma unroll
    for (int ks = 0; ks < 4; ++ks) d = mfma32(ax[ks], u32x4{bb[2 * ks].x, bb[2 * ks].y, bb[2 * ks + 1].x, bb[2 * ks + 1].y}, d);
    stsc2(rws, a_qp, u32x2{pk(d[0], dpp<XOR1>(d[0])), tag});
  }
  const u32 tpub = (u32)__builtin_amdgcn_s_memrealtime();
  STAMP(2);
  wait_landed(S, seen, N_X);
  const u32 b2h = lds_ld<u16>(S, O_MISC + 48 + ((l & 4) ? 16 : 0) + (((b & 3) * 8) & 15) + p4 * 2);
  const u32 xfh = lds_ld<u16>(S, O_X + (fcol & 511) * 2);
  u32 qoff = WS_QP + ((g * 4 + 2 * (l >> 5)) * 256 + w * 64 + 2 * (l & 31)) * 8;
  u32 adk = O_K + (32 * w + n16) * 256;
  u32 a_qs = lb + O_QS + (n16 & 3) * 256 + j4 * 16, a_qw = O_QS + w * 256 + (l & 31) * 8;
  u32 a_pw = O_P + (n16 & 3) * 256 + (32 * w + 4 * j4) * 2, a_pr = lb + O_P + (n16 >> 2) * 256 + j4 * 16;
  u32 a_mw = O_MW + (l & 3) * 16 + w * 4, a_mr = lb + O_MW + j4 * 16;
  u32 ad0 = O_V + (8 * j4 + q4) * 256 + (((4 * w + (p4 >> 1)) ^ ((q4 | ((j4 & 1) << 2)) << 1)) * 16) + (p4 & 1) * 8, ad1 = ad0 ^ 32;
  u32 a_op = WS_OP + ((g * 32 + (j4 * 4 + w) * 2 + (n16 >> 3)) * 32 + s) * 128 + (n16 & 7) * 16;
  u32 a_st = (WS_ST + ((g * 32 + j4 * 8 + 2 * w + (n16 & 1)) * 32 + s) * 16) | msk(n16 < 2);
  u32 ooff = WS_OP + (me * 32 + (l >> 1)) * 128 + (2 * w + (l & 1)) * 16;
  u32 adw = O_WO + (4 * j4 + q4) * 1024 + (((16 * w + (p4 >> 1)) ^ (((4 * j4 + q4) & 7) << 1)) * 16) + (p4 & 1) * 8;
  u32 a_sp = WS_SB + ((w * 16 + (l >> 2)) * 256 + me) * 64 + (l & 3) * 16;
  u32 off0 = WS_SB + ((b & 63) * 256 + 64 * w + (l >> 2)) * 64 + (l & 3) * 16;
  asm volatile("" : "+v"(qoff), "+v"(adk), "+v"(a_qs), "+v"(a_qw), "+v"(a_pw), "+v"(a_pr), "+v"(a_mw), "+v"(a_mr), "+v"(ad0), "+v"(ad1));
  asm volatile("" : "+v"(a_op), "+v"(a_st), "+v"(ooff), "+v"(adw), "+v"(a_sp), "+v"(off0));
  const u32 a_fq = lb + O_FL + 4 * F_Q, a_fp = lb + O_FL + 4 * F_P;
  u32x4 kf[8];
  {
    wait_until(tpub + Q_D);
    u32x4 qa = ld1(rws, qoff), qb = ld1(rws, qoff + 2048);
    wait_landed(S, seen, N_W1 + 8 * (w + 1));
#pragma unroll
    for (int i = 0; i < 8; ++i) kf[i] = lds_ld<u32x4>(S, adk + (i >> 2) * 4096 + (((4 * (i & 3) + j4) ^ n16) * 16));
_Pragma("unroll 1")
    for (int spin = 0; spin < SPIN_CAP && !__all(tok(qa, tag) & tok(qb, tag)); ++spin) { MEMCLOB(); qa = ldsc(rws, qoff); qb = ldsc(rws, qoff + 2048); }
    const float q0 = x32_sum(lo16(qa.x) + lo16(qb.x)), q1 = x32_sum(hi16(qa.x) + hi16(qb.x)), q2 = x32_sum(lo16(qa.z) + lo16(qb.z)), q3 = x32_sum(hi16(qa.z) + hi16(qb.z));
    if (l < 32) lds_st<u32x2>(S, a_qw, u32x2{pk(q0, q1), pk(q2, q3)});
    if (l == 0) set_flag(S, F_Q + w, 1);
  }
  STAMP(3);
  {
    u32x4 fl4, bq0, bq1, bq2, bq3;
#pragma unroll 1
    for (int spin = 0; spin < LSPIN; ++spin) {
      asm volatile("ds_read_b128 %0, %5\n\tds_read_b128 %1, %6\n\tds_read_b128 %2, %6 offset:64\n\tds_read_b128 %3, %6 offset:128\n\tds_read_b128 %4, %6 offset:192\n\ts_waitcnt lgkmcnt(0)"
                   : "=&v"(fl4), "=&v"(bq0), "=&v"(bq1), "=&v"(bq2), "=&v"(bq3) : "v"(a_fq), "v"(a_qs) : "memory");
      if (all4(fl4)) break;
    }
    f32x4 d0 = mfma32(kf[0], bq0, f32x4{0.f, 0.f, 0.f, 0.f}), d1 = mfma32(kf[4], bq0, f32x4{0.f, 0.f, 0.f, 0.f});
    d0 = mfma32(kf[1], bq1, d0); d1 = mfma32(kf[5], bq1, d1);
    d0 = mfma32(kf[2], bq2, d0); d1 = mfma32(kf[6], bq2, d1);
    d0 = mfma32(kf[3], bq3, d0); d1 = mfma32(kf[7], bq3, d1);
    constexpr float C2 = 0.08838834764831845f * 1.4426950408889634f;
    float sc[8];
#pragma unroll
    for (int i = 0; i < 4; ++i) { sc[i] = d0[i] * C2; sc[4 + i] = d1[i] * C2; }
    const float mw = xr_max(fmaxf(fmaxf(fmaxf(sc[0], sc[1]), fmaxf(sc[2], sc[3])), fmaxf(fmaxf(sc[4], sc[5]), fmaxf(sc[6], sc[7]))));
    float p[8];
#pragma unroll
    for (int i = 0; i < 8; ++i) p[i] = ex2(sc[i] - mw);
    if (n16 < 4) {
      lds_st<u32x2>(S, a_pw, u32x2{pk(p[0], p[1]), pk(p[2], p[3])});
      lds_st<u32x2>(S, a_pw + 32, u32x2{pk(p[4], p[5]), pk(p[6], p[7])});
      if (l < 4) lds_st<float>(S, a_mw, mw);
    }
    if (l == 0) set_flag(S, F_P + w, 1);
  }
  STAMP(4);
  {
    u32x4 fl4, ap[4], m4u;
#pragma unroll 1
    for (int spin = 0; spin < LSPIN; ++spin) {
      asm volatile("ds_read_b128 %0, %6\n\tds_read_b128 %1, %7\n\tds_read_b128 %2, %7 offset:64\n\tds_read_b128 %3, %7 offset:128\n\tds_read_b128 %4, %7 offset:192\n\tds_read_b128 %5, %8\n\ts_waitcnt lgkmcnt(0)"
                   : "=&v"(fl4), "=&v"(ap[0]), "=&v"(ap[1]), "=&v"(ap[2]), "=&v"(ap[3]), "=&v"(m4u) : "v"(a_fp), "v"(a_pr), "v"(a_mr) : "memory");
      if (all4(fl4)) break;
    }
    const float m0 = f_of(m4u.x), m1 = f_of(m4u.y), m2 = f_of(m4u.z), m3 = f_of(m4u.w);
    const float M = vmax(vmax3(m0, m1, m2), m3);
    const float al[4] = {ex2(m0 - M), ex2(m1 - M), ex2(m2 - M), ex2(m3 - M)};
    const u32x4 ones = {0x3f803f80u, 0x3f803f80u, 0x3f803f80u, 0x3f803f80u};
    float L = 0;
#pragma unroll
    for (int ks = 0; ks < 4; ++ks) L = fmaf(al[ks], mfma32(ap[ks], ones, f32x4{0.f, 0.f, 0.f, 0.f})[0], L);
    stsc(rws, a_st, u32x4{u_of(M), tag, u_of(L), tag});
    float o0 = 0, o1 = 0;
    wait_landed(S, seen, N_K + 24);
    {
      u32x2 vf[12];
#pragma unroll
      for (int i = 0; i < 12; ++i) vf[i] = lds_tr(S, ((i & 2) ? ad1 : ad0) + (i >> 2) * 8192 + (i & 1) * 1024);
#pragma unroll
      for (int ks = 0; ks < 3; ++ks) {
        o0 = fmaf(al[ks], mfma32(ap[ks], u32x4{vf[4 * ks].x, vf[4 * ks].y, vf[4 * ks + 1].x, vf[4 * ks + 1].y}, f32x4{0.f, 0.f, 0.f, 0.f})[0], o0);
        o1 = fmaf(al[ks], mfma32(ap[ks], u32x4{vf[4 * ks + 2].x, vf[4 * ks + 2].y, vf[4 * ks + 3].x, vf[4 * ks + 3].y}, f32x4{0.f, 0.f, 0.f, 0.f})[0], o1);
      }
    }
    wait_landed(S, seen, ST_AT);
    STAMP(10);
    if (w == 0 && l < 32) arrive(ws + WS_HB + (g * 32 + l) * 128, 1u << s);
    wait_landed(S, seen, N_V);
    {
      const u32x2 a0 = lds_tr(S, ad0 + 3 * 8192), a1 = lds_tr(S, ad0 + 3 * 8192 + 1024), c0 = lds_tr(S, ad1 + 3 * 8192), c1 = lds_tr(S, ad1 + 3 * 8192 + 1024);
      o0 = fmaf(al[3], mfma32(ap[3], u32x4{a0.x, a0.y, a1.x, a1.y}, f32x4{0.f, 0.f, 0.f, 0.f})[0], o0);
      o1 = fmaf(al[3], mfma32(ap[3], u32x4{c0.x, c0.y, c1.x, c1.y}, f32x4{0.f, 0.f, 0.f, 0.f})[0], o1);
    }
    stsc(rws, a_op, u32x4{u_of(o0), tag, u_of(o1), tag});
  }
  STAMP(5);
  {
    wait_ge(S, F_OOK, 1);
    u32x4 ov = ld1(rws, ooff);
    wait_landed(S, seen, N_WO);
    u32x2 bw[8];
#pragma unroll
    for (int tt = 0; tt < 8; ++tt) bw[tt] = lds_tr(S, adw ^ (tt << 5));
    u32 bp = 0;
    if (hasM) { wait_ge(S, F_B + w, 1); bp = lds_ld<u32>(S, O_BP + w * 256 + l * 4); }
    wait_ge(S, F_WOK, 1);
    const float wn = lds_ld<float>(S, O_WT + (l >> 1) * 4);
_Pragma("unroll 1")
    for (int spin = 0; spin < SPIN_CAP && !__all(tok(ov, tag)); ++spin) { DIAG(c3++); MEMCLOB(); ov = ldsc(rws, ooff); }
    if (t == 0) set_flag(S, F_OG, 1);
    STAMP(6);
    float o0 = wn * f_of(ov.x), o1 = wn * f_of(ov.z);
    o0 += dpp<XOR2>(o0); o1 += dpp<XOR2>(o1); o0 += dpp<ROR4>(o0); o1 += dpp<ROR4>(o1); o0 += dpp<ROR8>(o0); o1 += dpp<ROR8>(o1);
    o0 = x16_sum(o0); o1 = x16_sum(o1); o0 = x32_sum(o0); o1 = x32_sum(o1);
    const u32 pr = pk(o0, o1), po = __builtin_amdgcn_mov_dpp(pr, XOR1, 0xf, 0xf, true);
    if (l == 0) { MEMCLOB(); *(volatile LAS u32x4*)(S + O_AV + w * 16) = u32x4{pr, po, 1u, 1u}; MEMCLOB(); }
    u32x4 av;
#pragma unroll 1
    for (int spin = 0; spin < LSPIN; ++spin) { av = *(volatile LAS u32x4*)(S + O_AV + j4 * 16); if (__all(av.z != 0)) break; }
    const u32x2 a = {av.x, av.y};
    f32x4 d[8];
#pragma unroll
    for (int tt = 0; tt < 8; ++tt) d[tt] = mfma16(a, bw[tt], f32x4{0.f, 0.f, 0.f, 0.f});
    const bool b0 = j4 & 1, b1_ = j4 & 2;
    const float e0 = b1_ ? (b0 ? d[6][0] : d[4][0]) : (b0 ? d[2][0] : d[0][0]), e1 = b1_ ? (b0 ? d[7][0] : d[5][0]) : (b0 ? d[3][0] : d[1][0]);
    stsc(rws, a_sp, u32x4{pk(e0, e1), tag, bp, tag});
  }
  STAMP(7);
  if (b < NC) {
    const float b2f = f_of(b2h << 16), xff = f_of(xfh << 16);
    u32 a_fw = O_FIN + (l & 3) * 64 + w * 4;
    u32 a_ff = lb + O_FL + 4 * F_F, a_fr = lb + O_FIN + (l & 3) * 64 + ((l >> 2) & 1) * 16;
    asm volatile("" : "+v"(a_fw), "+v"(a_ff), "+v"(a_fr));
    wait_ge(S, F_FOK, 1);
    u32x4 v[4];
#pragma unroll
    for (int i = 0; i < 4; ++i) v[i] = ld1(rws, off0 + i * 1024);
_Pragma("unroll 1")
    for (int spin = 0; spin < SPIN_CAP && !__all(tok(v[0], tag) & tok(v[1], tag) & tok(v[2], tag) & tok(v[3], tag)); ++spin) {
      DIAG(c4++);
      MEMCLOB();
#pragma unroll
      for (int i = 0; i < 4; ++i) v[i] = ldsc(rws, off0 + i * 1024);
    }
    STAMP(8);
    float r[4];
    r[0] = (lo16(v[0].x) + lo16(v[1].x)) + (lo16(v[2].x) + lo16(v[3].x)); r[1] = (hi16(v[0].x) + hi16(v[1].x)) + (hi16(v[2].x) + hi16(v[3].x));
    r[2] = (lo16(v[0].z) + lo16(v[1].z)) + (lo16(v[2].z) + lo16(v[3].z)); r[3] = (hi16(v[0].z) + hi16(v[1].z)) + (hi16(v[2].z) + hi16(v[3].z));
#pragma unroll
    for (int i = 0; i < 4; ++i) r[i] += dpp<ROR4>(r[i]);
#pragma unroll
    for (int i = 0; i < 4; ++i) r[i] += dpp<ROR8>(r[i]);
#pragma unroll
    for (int i = 0; i < 4; ++i) r[i] = x16_sum(r[i]);
#pragma unroll
    for (int i = 0; i < 4; ++i) r[i] = x32_sum(r[i]);
    if (l < 4) {
#pragma unroll
      for (int i = 0; i < 4; ++i) lds_st<float>(S, a_fw + i * 16, r[i]);
    }
    if (l == 0) set_flag(S, F_F + w, 1);
    if (w == 0) {
      u32x4 fl4, s4, m4;
#pragma unroll 1
      for (int spin = 0; spin < LSPIN; ++spin) {
        asm volatile("ds_read_b128 %0, %3\n\tds_read_b128 %1, %4\n\tds_read_b128 %2, %4 offset:32\n\ts_waitcnt lgkmcnt(0)"
                     : "=&v"(fl4), "=&v"(s4), "=&v"(m4) : "v"(a_ff), "v"(a_fr) : "memory");
        if (all4(fl4)) break;
      }
      const float sv = (f_of(s4.x) + f_of(s4.y)) + (f_of(s4.z) + f_of(s4.w)), mv = (f_of(m4.x) + f_of(m4.y)) + (f_of(m4.z) + f_of(m4.w));
      const float B = rbf(rbf(mv) + b2f);
      const float gt = rbf(B * rcp(1.f + ex2(-B * 1.4426950408889634f)));
      const float o = xff + rbf(rbf(sv) * gt);
      if (l < 8) out[fcol] = (u16)pk(o, o);
      if (b == 0 && l == 0) __builtin_amdgcn_raw_buffer_store_b32(epoch + 1u, rws, WS_EPOCH, 0, AUX_SC1);
    }
  }
  STAMP(9);
  __builtin_amdgcn_endpgm();
  }

  __builtin_amdgcn_s_setprio(3);
  const u32 fl = (u32)(size_t)(S + O_FL + 4 * F_LANDED);
  asm volatile("" :: "s"(kc), "s"(vc), "s"(w1), "s"(w2), "s"(wo), "s"(x), "s"(ws), "s"(b1), "s"(b2));
  STAMP(3);
  {
    __builtin_amdgcn_raw_ptr_buffer_load_lds(mk(x, 1024), (LAS void*)(S + O_X), 16, l * 16, 0, 0, 0);
    const u32 f0 = g * 256 + (s >> 2) * 32 + (s & 3) * 4, c0 = ((b & 63) >> 4) * 128 + ((b >> 2) & 3) * 32 + (b & 3) * 4;
    const char* p = (const char*)ws;
    if (l == 1 || l == 2) p = (const char*)b1 + (((hasM ? f0 : 0) + (l - 1) * 16) * 2 & ~15u);
    if (l == 3 || l == 4) p = (const char*)b2 + ((c0 + (l - 3) * 16) * 2 & ~15u);
    __builtin_amdgcn_global_load_lds((const __attribute__((address_space(1))) void*)p, (LAS void*)(S + O_MISC), 16, 0, 0);
  }
  {
    const rsrc_t r1 = mk(w1, hasM ? 512 * FF * 2 : 0);
    const u32 hb = l >> 5, bl = hb * (FF * 2) + (((l & 31) ^ (hb << 1)) * 16);
    const u32 base = (s * 16) * (FF * 2) + g * 512;
#pragma unroll 1
    for (int G = 0; G < 2; ++G) dma4(r1, S, O_W1 + G * 4096, bl, (bl ^ 64) + 6144, (bl ^ 128) + 2 * 6144, (bl ^ 192) + 3 * 6144, base + G * 8 * (FF * 2));
  }
  const size_t kvoff = ((size_t)g * SK + (SK - WIN) + s * 128) * 256;
  {
    const rsrc_t rk = mk((const char*)kc + kvoff, 32768);
    const u32 v0 = j4 * 256 + ((n16 ^ j4) * 16);
    wait_until(tstart + K_T);
#pragma unroll 1
    for (int G = 0; G < 8; ++G) { landed<WINDOW - 4>(fl, N_W1 + 4 * G - (WINDOW - 4)); dma4(rk, S, O_K + G * 4096, v0, v0 ^ 64, v0 ^ 128, v0 ^ 192, G * 4096); }
  }
  STAMP(4);
  {
    const rsrc_t rv = mk((const char*)vc + kvoff, 32768);
    const u32 v0 = j4 * 256 + ((n16 ^ (j4 << 1)) * 16);
#pragma unroll 1
    for (int G = 0; G < 8; ++G) { landed<WINDOW - 4>(fl, N_K + 4 * G - (WINDOW - 4)); dma4(rv, S, O_V + G * 4096, v0, v0, v0 ^ 128, v0 ^ 128, G * 4096); }
  }
  STAMP(5);
  {
    const rsrc_t r2 = mk((const char*)w2 - 1024, hasM ? 512 * FF * 2 + 1024 : 0);
    const u32 f0 = g * 256 + (s >> 2) * 32 + (s & 3) * 4;
    const rsrc_t ro = mk((const char*)wo - 1024, 4096 * 512 * 2 + 1024);
    const u32 r0 = g * 512 + (s >> 3) * 128 + ((s >> 1) & 3) * 32 + (s & 1) * 8;
#pragma unroll 1
    for (int k = 0; k < 6; ++k) {
      if (POSTMODE == 0) landed<WINDOW2 - 4>(fl, N_V + 4 * k - (WINDOW2 - 4));
      const bool is_w2 = WO_FIRST ? k >= 4 : k < 2;
      const int G = WO_FIRST ? (k & 3) : (k < 2 ? k : k - 2);
      if (is_w2) {
        const int G2 = G & 1;
        const u32 v = (l * 16) ^ (G2 << 7);
        dma4(r2, S, O_W2 + G2 * 4096, v + 1024, (v ^ 32) + 16384, v ^ 64, (v ^ 96) + 15360, (f0 + 2 * G2) * 1024);
      } else {
        const u32 v = (l * 16) ^ ((G & 1) << 7);
        dma4(ro, S, O_WO + G * 4096, v + 1024, (v ^ 32) + 16384, v ^ 64, (v ^ 96) + 15360, (r0 + 2 * G) * 1024);
      }
    }
  }
  STAMP(1);
  drain<20>(fl, N_END);
  STAMP(2);
}

extern "C" void kernel_launch(void* const* d_in, const int* in_sizes, int n_in, void* d_out, int out_size, void* d_ws, size_t ws_size, hipStream_t stream) {
  fused_decode<<<NWG, NT, 0, stream>>>((const u16*)d_in[0], (const u16*)d_in[1], (const u16*)d_in[2], (const u16*)d_in[3], (const u16*)d_in[4],
                                       (const u16*)d_in[6], (const u16*)d_in[7], (const u16*)d_in[8], (const u16*)d_in[9], (u16*)d_out, (unsigned char*)d_ws);
}
```

```cpp
#include <hip/hip_runtime.h>
#include <stdint.h>

typedef unsigned short u16;
typedef unsigned u32;
typedef unsigned long long u64;
typedef u32 u32x4 __attribute__((ext_vector_type(4)));
typedef u32 u32x2 __attribute__((ext_vector_type(2)));
typedef float f32x4 __attribute__((ext_vector_type(4)));
typedef float f32x2 __attribute__((ext_vector_type(2)));
typedef short s16x4 __attribute__((ext_vector_type(4)));
typedef __bf16 bf16x8 __attribute__((ext_vector_type(8)));
typedef __bf16 bf16x2 __attribute__((ext_vector_type(2)));
typedef __amdgpu_buffer_rsrc_t rsrc_t;
#define LAS __attribute__((address_space(3)))

constexpr int SK = 65536, WIN = 4096, FF = 1792;
constexpr int NWG = 256, NT = 640;
constexpr int NC = 64;
constexpr u32 WS_EPOCH = 0;
constexpr u32 WS_QP = 4096;
constexpr u32 WS_HB = WS_QP + 8 * 4 * 256 * 8;
constexpr u32 WS_ST = WS_HB + 256 * 128;
constexpr u32 WS_OP = WS_ST + 256 * 2048;
constexpr u32 WS_HP = WS_OP + 256 * 32 * 128;
constexpr u32 WS_SB = WS_HP + 224 * 32 * 64;
constexpr u32 WS_END = WS_SB + NC * 256 * 64;
constexpr u32 OOB = 0x80000000u;
constexpr u32 O_WQ = 0, O_K = 16384, O_V = 49152, O_W1 = 81920, O_W2 = 90112, O_WO = 98304, O_S = 114688;
constexpr u32 O_X = O_S, O_MISC = O_X + 1024, O_S2 = O_MISC + 1024;
constexpr u32 O_XN = O_S2, O_Y = O_XN + 1024, O_QS = O_Y + 1024, O_P = O_QS + 1024, O_MW = O_P + 1024, O_LW = O_MW + 64, O_AV = O_LW + 64,
              O_EP = O_AV + 64, O_WT = O_EP + 16, O_HV = O_WT + 128, O_HX = O_HV + 64, O_FIN = O_HX + 128, O_BP = O_FIN + 512, O_FL = O_BP + 1024, LDS_BYTES = O_FL + 256;
constexpr u32 O_INIT = O_AV;
enum { F_LANDED = 0, F_OOK = 2, F_HOK = 3, F_FOK = 4, F_WOK = 5, F_OG = 6, F_XN = 7, F_Y = 8, F_INIT = 9, F_H = 32, F_Q = 12, F_P = 16, F_WQ = 20, F_F = 24, F_B = 28 };
constexpr int N_X = 2, N_W1 = N_X + 8, N_K = N_W1 + 32, N_V = N_K + 32, N_END = N_V + 24;
#define WO_FIRST 0
constexpr int N_W2 = WO_FIRST ? N_END : N_V + 8, N_WO = WO_FIRST ? N_V + 16 : N_END;
#define WINDOW 24
#define WINDOW2 24
#define Q_AT (N_X + 20)
#define ST_AT (N_V - 4)
#define HH_AT N_V
#define POSTMODE 0
#define HOLD 45
#define K_T 100
#define Q_D 75
#define H_D 100
#define POLL_AT N_V
#define CODE_BYTES 12048
#define SPINSLEEP 0
#define LAZY 6
constexpr int AUX_NT = 2, AUX_SC1 = 16;
#define LSPIN (1 << 22)
#define GSPIN (1 << 14)
constexpr int SPIN_CAP = GSPIN;

#define KEEP(x) asm volatile("" :: "v"(x))
#define MEMCLOB() asm volatile("" ::: "memory")

__device__ __forceinline__ rsrc_t mk(const void* p, u32 bytes) { return __builtin_amdgcn_make_buffer_rsrc((void*)p, 0, bytes, 0x00020000); }
__device__ __forceinline__ u32x4 ldsc(rsrc_t r, u32 voff) { return __builtin_amdgcn_raw_buffer_load_b128(r, voff, 0, AUX_SC1); }
#define FIRST_AUX 16
__device__ __forceinline__ u32x4 ld1(rsrc_t r, u32 voff) { return __builtin_amdgcn_raw_buffer_load_b128(r, voff, 0, FIRST_AUX); }
__device__ __forceinline__ void stsc(rsrc_t r, u32 voff, u32x4 v) { __builtin_amdgcn_raw_buffer_store_b128(v, r, voff, 0, AUX_SC1); }
__device__ __forceinline__ void stsc2(rsrc_t r, u32 voff, u32x2 v) { __builtin_amdgcn_raw_buffer_store_b64(v, r, voff, 0, AUX_SC1); }
__device__ __forceinline__ float f_of(u32 u) { return __builtin_bit_cast(float, u); }
__device__ __forceinline__ u32 u_of(float f) { return __builtin_bit_cast(u32, f); }
__device__ __forceinline__ float lo16(u32 w) { return f_of(w << 16); }
__device__ __forceinline__ float hi16(u32 w) { return f_of(w & 0xffff0000u); }
__device__ __forceinline__ u32 pk(float lo, float hi) { bf16x2 r = {(__bf16)lo, (__bf16)hi}; return __builtin_bit_cast(u32, r); }
__device__ __forceinline__ float rbf(float f) { return lo16(pk(f, f)); }
template <int CTRL> __device__ __forceinline__ float dpp(float x) { return f_of(__builtin_amdgcn_mov_dpp(u_of(x), CTRL, 0xf, 0xf, true)); }
constexpr int XOR1 = 0xB1, XOR2 = 0x4E, HMIR = 0x141, MIR = 0x140, ROR4 = 0x124, ROR8 = 0x128;
__device__ __forceinline__ float x16_sum(float x) { auto s = __builtin_amdgcn_permlane16_swap(u_of(x), u_of(x), false, false); return f_of(s[0]) + f_of(s[1]); }
__device__ __forceinline__ float x32_sum(float x) { auto s = __builtin_amdgcn_permlane32_swap(u_of(x), u_of(x), false, false); return f_of(s[0]) + f_of(s[1]); }
__device__ __forceinline__ float x16_max(float x) { auto s = __builtin_amdgcn_permlane16_swap(u_of(x), u_of(x), false, false); return fmaxf(f_of(s[0]), f_of(s[1])); }
__device__ __forceinline__ float x32_max(float x) { auto s = __builtin_amdgcn_permlane32_swap(u_of(x), u_of(x), false, false); return fmaxf(f_of(s[0]), f_of(s[1])); }
__device__ __forceinline__ float row_sum(float x) { x += dpp<XOR1>(x); x += dpp<XOR2>(x); x += dpp<HMIR>(x); x += dpp<MIR>(x); return x; }
__device__ __forceinline__ float wave_sum(float x) { return x32_sum(x16_sum(row_sum(x))); }
__device__ __forceinline__ float vmax(float a, float b) { float r; asm("v_max_f32 %0, %1, %2" : "=v"(r) : "v"(a), "v"(b)); return r; }
__device__ __forceinline__ float vmax3(float a, float b, float c) { float r; asm("v_max3_f32 %0, %1, %2, %3" : "=v"(r) : "v"(a), "v"(b), "v"(c)); return r; }
#define DPPMAX(x, ctrl) asm("s_nop 1\n\tv_max_f32_dpp %0, %0, %0 " ctrl " row_mask:0xf bank_mask:0xf" : "+v"(x))
__device__ __forceinline__ float xr_max(float x) {
  auto s = __builtin_amdgcn_permlane16_swap(u_of(x), u_of(x), false, false); x = vmax(f_of(s[0]), f_of(s[1]));
  auto t = __builtin_amdgcn_permlane32_swap(u_of(x), u_of(x), false, false); return vmax(f_of(t[0]), f_of(t[1]));
}
__device__ __forceinline__ float par_sum(float x) { x += dpp<XOR2>(x); x += dpp<ROR4>(x); x += dpp<ROR8>(x); return x32_sum(x16_sum(x)); }
__device__ __forceinline__ float par_max(float x) { DPPMAX(x, "quad_perm:[2,3,0,1]"); DPPMAX(x, "row_ror:4"); DPPMAX(x, "row_ror:8"); return xr_max(x); }
__device__ __forceinline__ float ex2(float x) { return __builtin_amdgcn_exp2f(x); }
__device__ __forceinline__ float rcp(float x) { return __builtin_amdgcn_rcpf(x); }
__device__ __forceinline__ f32x4 mfma32(u32x4 a, u32x4 b, f32x4 c) {
  return __builtin_amdgcn_mfma_f32_16x16x32_bf16(__builtin_bit_cast(bf16x8, a), __builtin_bit_cast(bf16x8, b), c, 0, 0, 0);
}
__device__ __forceinline__ f32x4 mfma16(u32x2 a, u32x2 b, f32x4 c) {
  return __builtin_amdgcn_mfma_f32_16x16x16bf16_1k(__builtin_bit_cast(s16x4, a), __builtin_bit_cast(s16x4, b), c, 0, 0, 0);
}
typedef LAS unsigned char* lds_t;
template <class T> __device__ __forceinline__ T lds_ld(lds_t s, u32 off) { return *(const LAS T*)(s + off); }
template <class T> __device__ __forceinline__ void lds_st(lds_t s, u32 off, T v) { *(LAS T*)(s + off) = v; }
__device__ __forceinline__ u32x2 lds_tr(lds_t s, u32 off) { return __builtin_bit_cast(u32x2, __builtin_amdgcn_ds_read_tr16_b64_v4i16((LAS s16x4*)(s + off))); }
__device__ __forceinline__ void set_flag(lds_t s, int f, u32 v) { MEMCLOB(); *(volatile LAS u32*)(s + O_FL + 4 * f) = v; MEMCLOB(); }
__device__ __forceinline__ void wait_ge(lds_t s, int f, int need) {
#pragma unroll 1
  for (int spin = 0; spin < LSPIN; ++spin) {
    const int v = __builtin_amdgcn_readfirstlane(*(volatile LAS int*)(s + O_FL + 4 * f));
    if (v >= need) break;
    if (SPINSLEEP) __builtin_amdgcn_s_sleep(SPINSLEEP);
  }
  MEMCLOB();
}
__device__ __forceinline__ void wait_lazy(lds_t s, int f, int need) {
#pragma unroll 1
  for (int spin = 0; spin < LSPIN; ++spin) {
    const int v = __builtin_amdgcn_readfirstlane(*(volatile LAS int*)(s + O_FL + 4 * f));
    if (v >= need) break;
    __builtin_amdgcn_s_sleep(LAZY);
  }
  MEMCLOB();
}
__device__ __forceinline__ void wait_landed(lds_t s, int& seen, int need) {
  if (seen < need) {
#pragma unroll 1
    for (int spin = 0; spin < LSPIN; ++spin) {
      seen = __builtin_amdgcn_readfirstlane(*(volatile LAS int*)(s + O_FL + 4 * F_LANDED));
      if (seen >= need) break;
      if (SPINSLEEP) __builtin_amdgcn_s_sleep(SPINSLEEP);
    }
  }
  MEMCLOB();
}
__device__ __forceinline__ bool all4(u32x4 f) { return __builtin_amdgcn_readfirstlane(f.x & f.y & f.z & f.w) != 0; }
__device__ __forceinline__ void wait4(lds_t s, int f) {
#pragma unroll 1
  for (int spin = 0; spin < LSPIN; ++spin) {
    const u32x4 v = *(volatile LAS u32x4*)(s + O_FL + 4 * f);
    if (__builtin_amdgcn_readfirstlane(v.x & v.y & v.z & v.w)) break;
  }
  MEMCLOB();
}
__device__ __forceinline__ u32x4 ln8(u32x4 xr) {
  float v[8];
#pragma unroll
  for (int i = 0; i < 4; ++i) { v[2 * i] = lo16(xr[i]); v[2 * i + 1] = hi16(xr[i]); }
  const float s = ((v[0] + v[1]) + (v[2] + v[3])) + ((v[4] + v[5]) + (v[6] + v[7]));
  const float m = wave_sum(s) * (1.f / 512);
  float q0 = 0, q1 = 0;
#pragma unroll
  for (int i = 0; i < 4; ++i) { const float d0 = v[2 * i] - m, d1 = v[2 * i + 1] - m; q0 = fmaf(d0, d0, q0); q1 = fmaf(d1, d1, q1); }
  const float var = wave_sum(q0 + q1) * (1.f / 512);
  const float mb = rbf(m);
  const float inv = rcp(rbf(__builtin_amdgcn_sqrtf(rbf(rbf(var) + 1e-5f))));
  u32x4 r;
#pragma unroll
  for (int i = 0; i < 4; ++i) { const u32 d = pk(v[2 * i] - mb, v[2 * i + 1] - mb); r[i] = pk(lo16(d) * inv, hi16(d) * inv); }
  return r;
}
__device__ __forceinline__ u64 poll64(const unsigned char* p) {
  u64 r; const u64 z = 0;
  asm volatile("global_atomic_or_x2 %0, %1, %2, off sc0 sc1\n\ts_waitcnt vmcnt(0)" : "=&v"(r) : "v"(p), "v"(z) : "memory");
  return r;
}
__device__ __forceinline__ void poll_word(const unsigned char* p, u32 tag) {
  int n;
  asm volatile(
    "v_mov_b32 v108, 0\n\tv_mov_b32 v109, 0\n\t"
    "s_mov_b64 exec, 1\n\t"
    "global_atomic_or_x2 v[104:105], %1, v[108:109], off sc0 sc1\n\t"
    "s_sleep 4\n\t"
    "global_atomic_or_x2 v[106:107], %1, v[108:109], off sc0 sc1\n\t"
    "s_mov_b32 %0, 0\n"
    "1:\n\t"
    "s_waitcnt vmcnt(1)\n\t"
    "v_not_b32 v104, v104\n\tv_xor_b32 v105, %2, v105\n\tv_or_b32 v104, v104, v105\n\t"
    "v_cmp_ne_u32 vcc, 0, v104\n\t"
    "s_cbranch_vccz 2f\n\t"
    "global_atomic_or_x2 v[104:105], %1, v[108:109], off sc0 sc1\n\t"
    "s_waitcnt vmcnt(1)\n\t"
    "v_not_b32 v106, v106\n\tv_xor_b32 v107, %2, v107\n\tv_or_b32 v106, v106, v107\n\t"
    "v_cmp_ne_u32 vcc, 0, v106\n\t"
    "s_cbranch_vccz 2f\n\t"
    "global_atomic_or_x2 v[106:107], %1, v[108:109], off sc0 sc1\n\t"
    "s_add_u32 %0, %0, 1\n\t"
    "s_cmp_lt_u32 %0, %3\n\t"
    "s_cbranch_scc1 1b\n"
    "2:\n\t"
    "s_mov_b64 exec, -1"
    : "=&s"(n) : "v"(p), "v"(tag), "s"(SPIN_CAP) : "vcc", "scc", "v104", "v105", "v106", "v107", "v108", "v109", "memory");
}
__device__ __forceinline__ void arrive(unsigned char* p, u32 bit) {
  __hip_atomic_fetch_or((u64*)p, (u64)bit, __ATOMIC_RELAXED, __HIP_MEMORY_SCOPE_AGENT);
}
#define TSLEEP 3
__device__ __forceinline__ void wait_until(u32 t) { while ((int)((u32)__builtin_amdgcn_s_memrealtime() - t) < 0) { if (TSLEEP) __builtin_amdgcn_s_sleep(TSLEEP); } }
__device__ __forceinline__ void dma4(rsrc_t r, lds_t s, u32 ldsoff, u32 v0, u32 v1, u32 v2, u32 v3, u32 soff) {
  __builtin_amdgcn_raw_ptr_buffer_load_lds(r, (LAS void*)(s + ldsoff), 16, v0, soff, 0, AUX_NT);
  __builtin_amdgcn_raw_ptr_buffer_load_lds(r, (LAS void*)(s + ldsoff), 16, v1, soff, 1024, AUX_NT);
  __builtin_amdgcn_raw_ptr_buffer_load_lds(r, (LAS void*)(s + ldsoff), 16, v2, soff, 2048, AUX_NT);
  __builtin_amdgcn_raw_ptr_buffer_load_lds(r, (LAS void*)(s + ldsoff), 16, v3, soff, 3072, AUX_NT);
}
template <int N> __device__ __forceinline__ void landed(u32 flag_addr, int v) {
  asm volatile("s_waitcnt vmcnt(%0)\n\tds_write_b32 %1, %2" :: "i"(N), "v"(flag_addr), "v"(v) : "memory");
}
template <int N> __device__ __forceinline__ void drain(u32 flag_addr, int total) {
  landed<N>(flag_addr, total - N);
  if constexpr (N >= 4) drain<N - 4>(flag_addr, total);
}
__device__ __forceinline__ u32 msk(bool c) { return c ? 0u : OOB; }
__device__ __forceinline__ bool tok(u32x4 v, u32 tag) { return (v.y == tag) & (v.w == tag); }
__device__ __forceinline__ u32 mktag(u32 epoch) { return ((epoch + 1u) * 0x9E3779B1u) ^ 0x5bd1e995u; }

__global__ void __launch_bounds__(NT) fused_decode(const u16* __restrict__ x, const u16* __restrict__ kc, const u16* __restrict__ vc,
                                                   const u16* __restrict__ wq, const u16* __restrict__ wo, const u16* __restrict__ w1,
                                                   const u16* __restrict__ b1, const u16* __restrict__ w2, const u16* __restrict__ b2,
                                                   u16* __restrict__ out, unsigned char* __restrict__ ws) {
  __shared__ __attribute__((aligned(16))) unsigned char smem[LDS_BYTES];
  lds_t S = (lds_t)smem;
  const u32 tstart = (u32)__builtin_amdgcn_s_memrealtime();
  const int t = threadIdx.x, l = t & 63, wv = __builtin_amdgcn_readfirstlane(t >> 6);
  if (wv == 0) {
    lds_st<u32>(S, O_FL + 4 * l, 0u); if (l < 16) lds_st<u32>(S, O_AV + 4 * l, 0u);
    asm volatile("s_waitcnt lgkmcnt(0)" ::: "memory");
  }
  asm volatile("s_load_dword s94, %0, 0x0\n\ts_load_dword s95, %0, 0x40" :: "s"(__builtin_amdgcn_kernarg_segment_ptr()) : "s94", "s95", "memory");
  const rsrc_t rcode = mk((const void*)(__builtin_amdgcn_s_getpc() & ~255ull), CODE_BYTES);
  {
    const u32 sel = (blockIdx.x >> 3) + wv;
    asm volatile("buffer_load_dwordx4 v[120:123], %0, %1, 0 offen\n\tbuffer_load_dwordx4 v[120:123], %2, %1, 0 offen"
                 :: "v"((l * 16 + (wv + 1) * 1024) | ((sel & 3) ? OOB : 0u)), "s"(rcode), "v"((l * 16 + (wv + 11) * 1024) | ((sel & 3) ? OOB : 0u))
                 : "v120", "v121", "v122", "v123", "memory");
  }
  asm volatile("s_barrier" ::: "memory");
  const int b = blockIdx.x, g = b & 7, s = b >> 3;
  const int n16 = l & 15, j4 = l >> 4, q4 = (l >> 2) & 3, p4 = l & 3;
  const bool hasM = g < 7;
  const int me = g * 32 + s;
  const rsrc_t rws = mk(ws, WS_END);
  if (wv < 4) {
    if (wv == 0) {
      const rsrc_t rx = mk(x, 1024);
      asm volatile("buffer_load_dwordx4 v[110:113], %0, %1, 0 offen\n\tbuffer_load_dword v114, off, %2, 0"
                   :: "v"(l * 16), "s"(rx), "s"(rws) : "v110", "v111", "v112", "v113", "v114", "memory");
    }
    {
      const rsrc_t rq = mk(wq, 512 * 4096 * 2);
      const u32 base = ((s & 3) * 128 + 32 * wv) * 8192 + (g * 512 + (s >> 2) * 64) * 2;
      const u32 v0 = (l >> 3) * 8192 + (((l & 7) ^ (((l >> 4) & 1) << 1)) * 16), v1 = (v0 ^ 64) + 64512;
      const u32 m = (u32)(size_t)(S + O_WQ) + wv * 4096;
      asm volatile("s_mov_b32 m0, %0\n\ts_nop 0\n\t"
                   "buffer_load_dwordx4 %1, %5, %6 offen nt lds\n\tbuffer_load_dwordx4 %2, %5, %6 offen offset:1024 nt lds\n\t"
                   "buffer_load_dwordx4 %3, %5, %6 offen offset:2048 nt lds\n\tbuffer_load_dwordx4 %4, %5, %6 offen offset:3072 nt lds"
                   :: "s"(m), "v"(v0), "v"(v1), "v"(v0 + 2 * 64512), "v"(v1 + 2 * 64512), "s"(rq), "s"(base) : "memory");
    }
  }

  if (wv != 8) {
  asm volatile("" :: "s"(out), "s"(kc), "s"(vc), "s"(wo), "s"(w2));

  if (wv == 9) {
    u32 soff = (WS_ST + (me * 32 + (l & 31)) * 64) | msk(l < 32);
    wait_ge(S, F_XN, 1);
    const u32 tag = mktag(lds_ld<u32>(S, O_EP));
    stsc(rws, (WS_HB + ((l & 4) ? (b & 63) : me) * 128 + ((l & 4) ? 64 : 0) + (l & 3) * 16) | msk(l < 4 || (l < 8 && b < NC)), u32x4{0u, tag, 0u, tag});
    wait_lazy(S, F_LANDED, POLL_AT - 8);
    wait_ge(S, F_LANDED, POLL_AT);
    poll_word(ws + WS_HB + me * 128, tag);
    set_flag(S, F_OOK, 1);
    {
      u32x4 sv = ldsc(rws, soff);
_Pragma("unroll 1")
      for (int spin = 0; spin < SPIN_CAP && !__all(tok(sv, tag) | (l >= 32)); ++spin) { MEMCLOB(); sv = ldsc(rws, soff); }
      float mp = f_of(sv.x);
      float Mx = mp;
      DPPMAX(Mx, "quad_perm:[1,0,3,2]"); DPPMAX(Mx, "quad_perm:[2,3,0,1]"); DPPMAX(Mx, "row_half_mirror"); DPPMAX(Mx, "row_mirror");
      { auto r = __builtin_amdgcn_permlane16_swap(u_of(Mx), u_of(Mx), false, false); Mx = vmax(f_of(r[0]), f_of(r[1])); }
      const float wp = ex2(mp - Mx);
      const float wn = wp * rcp(x16_sum(row_sum(wp * f_of(sv.z))));
      if (l < 32) lds_st<float>(S, O_WT + l * 4, wn);
    }
    set_flag(S, F_WOK, 1);
    if ((s & 7) == 0) {
      wait_ge(S, F_OG, 1);
      arrive(ws + WS_HB + l * 128 + 64, 1u << (g * 4 + (s >> 3)));
    }
    if (b < NC) {
      poll_word(ws + WS_HB + b * 128 + 64, tag);
      set_flag(S, F_FOK, 1);
    }
    __builtin_amdgcn_endpgm();
  }

  if (wv >= 4 && !hasM) __builtin_amdgcn_endpgm();
  if (wv == 0) {
    u32x4 xr; u32 ep;
    asm volatile("s_waitcnt vmcnt(4)\n\tv_mov_b32 %0, v110\n\tv_mov_b32 %1, v111\n\tv_mov_b32 %2, v112\n\tv_mov_b32 %3, v113\n\tv_mov_b32 %4, v114"
                 : "=v"(xr.x), "=v"(xr.y), "=v"(xr.z), "=v"(xr.w), "=v"(ep) :: "memory");
    lds_st<u32x4>(S, O_XN + l * 16, ln8(xr));
    if (l == 0) { lds_st<u32>(S, O_EP, ep); set_flag(S, F_XN, 1); }
  }
  wait_ge(S, F_XN, 1);
  const u32 epoch = lds_ld<u32>(S, O_EP);

  if (wv >= 4) {
    const int w = wv - 4;
    const u32 f0 = g * 256 + (s >> 2) * 32 + (s & 3) * 4;
    if (w == 0) { lds_st<u32x4>(S, O_Y + l * 16, ln8(lds_ld<u32x4>(S, O_XN + l * 16))); if (l == 0) set_flag(S, F_Y, 1); }
    wait_ge(S, F_Y, 1);
    const u32x2 ay = lds_ld<u32x2>(S, O_Y + (s * 16 + 4 * j4) * 2);
    wait_ge(S, F_LANDED, N_X);
    const u32 b1v = lds_ld<u16>(S, O_MISC + 16 + ((l & 4) ? 16 : 0) + ((f0 * 2) & 15) + p4 * 2);
    const u32 tag = mktag(epoch);
    u32 a_hp = (WS_HP + ((g * 32 + (w * 2 + (j4 & 1)) * 4 + (n16 >> 2)) * 32 + s) * 64 + (n16 & 3) * 16) | msk(l < 32);
    u32 a_bp = O_BP + w * 256 + l * 4;
    u32 hoff = (WS_HP + (me * 32 + 8 * w + ((l >> 2) & 7)) * 64 + (l & 3) * 16) | msk(l < 32);
    u32 ad1 = O_W1 + (4 * j4 + q4) * 512 + (((8 * w + (p4 >> 1)) ^ (((4 * j4 + q4) & 7) << 1)) * 16) + (p4 & 1) * 8;
    u32 ad2 = O_W2 + ((4 * j4 + q4) & 7) * 1024 + (((16 * w + (p4 >> 1)) ^ (((4 * j4 + q4) & 7) << 1)) * 16) + (p4 & 1) * 8;
    float b1f = f_of(b1v << 16);
    asm volatile("" : "+v"(a_hp), "+v"(a_bp), "+v"(hoff), "+v"(ad1), "+v"(ad2), "+v"(b1f));
    wait_ge(S, F_LANDED, N_W1);
    {
      f32x4 d[4];
#pragma unroll
      for (int tt = 0; tt < 4; ++tt) d[tt] = mfma16(ay, lds_tr(S, ad1 ^ (tt << 5)), f32x4{0.f, 0.f, 0.f, 0.f});
      const bool up = j4 & 1;
      stsc(rws, a_hp, u32x4{u_of(up ? d[2][0] : d[0][0]), tag, u_of(up ? d[3][0] : d[1][0]), tag});
    }
    wait_until((u32)__builtin_amdgcn_s_memrealtime() + H_D);
    u32x4 h0 = ld1(rws, hoff);
_Pragma("unroll 1")
    for (int spin = 0; spin < SPIN_CAP && !__all(tok(h0, tag) | (l >= 32)); ++spin) { MEMCLOB(); h0 = ldsc(rws, hoff); }
    {
      float ha = f_of(h0.x), hb = f_of(h0.z);
      ha += dpp<ROR4>(ha); hb += dpp<ROR4>(hb); ha += dpp<ROR8>(ha); hb += dpp<ROR8>(hb);
      ha = x16_sum(ha); hb = x16_sum(hb);
      if (l < 4) { lds_st<float>(S, O_HX + l * 16 + w * 4, ha); lds_st<float>(S, O_HX + 64 + l * 16 + w * 4, hb); }
      if (l == 0) set_flag(S, F_H + w, 1);
      wait4(S, F_H);
      const f32x4 h4 = lds_ld<f32x4>(S, O_HX + ((l & 4) ? 64 : 0) + p4 * 16);
      hb = ha = (h4[0] + h4[1]) + (h4[2] + h4[3]);
      const float p = rbf(rbf((l & 4) ? hb : ha) + b1f);
      const float u = p * fmaf(0.044715f * 0.7978845608028654f * 2.8853900817779268f, p * p, 0.7978845608028654f * 2.8853900817779268f);
      const float h = p - p * rcp(ex2(u) + 1.f);
      if (l < 8) lds_st<u16>(S, O_HV + w * 16 + (p4 * 2 + (l >> 2)) * 2, (u16)pk(h, h));
    }
    u32x2 ah = lds_ld<u32x2>(S, O_HV + w * 16 + (j4 & 1) * 8);
    if (j4 >= 2) ah = u32x2{0u, 0u};
    wait_lazy(S, F_LANDED, N_V - 4);
    wait_ge(S, F_LANDED, N_W2);
    {
      f32x4 d[8];
#pragma unroll
      for (int tt = 0; tt < 8; ++tt) d[tt] = mfma16(ah, lds_tr(S, ad2 ^ (tt << 5)), f32x4{0.f, 0.f, 0.f, 0.f});
      const bool b0 = j4 & 1, b1_ = j4 & 2;
      const float e0 = b1_ ? (b0 ? d[6][0] : d[4][0]) : (b0 ? d[2][0] : d[0][0]), e1 = b1_ ? (b0 ? d[7][0] : d[5][0]) : (b0 ? d[3][0] : d[1][0]);
      lds_st<u32>(S, a_bp, pk(e0, e1));
      if (l == 0) set_flag(S, F_B + w, 1);
    }
    __builtin_amdgcn_endpgm();
  }

  const int w = wv;
  int seen = -100;
  const int fcol = (b >> 4) * 128 + ((b >> 2) & 3) * 32 + (b & 3) * 4 + p4 + ((l >> 2) & 1) * 16;
  const u32 tag = mktag(epoch);
  const u32 lb = (u32)(size_t)S;
  {
    u32x4 ax[4];
#pragma unroll
    for (int ks = 0; ks < 4; ++ks) ax[ks] = lds_ld<u32x4>(S, O_XN + ((s & 3) * 128 + 32 * ks + 8 * j4) * 2);
    const u32 gq = (q4 >> 1) | ((j4 & 1) << 1);
    u32 ad = O_WQ + (8 * j4 + q4) * 128 + (((2 * w + (p4 >> 1)) ^ (gq << 1)) * 16) + (p4 & 1) * 8;
    u32 a_qp = (WS_QP + ((g * 4 + (s & 3)) * 256 + (s >> 2) * 32 + w * 8 + (n16 >> 1)) * 8) | msk(l < 16 && (l & 1) == 0);
    asm volatile("" : "+v"(ad), "+v"(a_qp), "+v"(ax[0]), "+v"(ax[1]), "+v"(ax[2]), "+v"(ax[3]));
    asm volatile("s_waitcnt vmcnt(0)" ::: "memory");
    if (l == 0) set_flag(S, F_WQ + w, 1);
    wait4(S, F_WQ);
    u32x2 bb[8];
#pragma unroll
    for (int i = 0; i < 8; ++i) bb[i] = lds_tr(S, ad + (i >> 1) * 4096 + (i & 1) * 512);
    f32x4 d = {0.f, 0.f, 0.f, 0.f};
#pragma unroll
    for (int ks = 0; ks < 4; ++ks) d = mfma32(ax[ks], u32x4{bb[2 * ks].x, bb[2 * ks].y, bb[2 * ks + 1].x, bb[2 * ks + 1].y}, d);
    stsc2(rws, a_qp, u32x2{pk(d[0], dpp<XOR1>(d[0])), tag});
  }
  const u32 tpub = (u32)__builtin_amdgcn_s_memrealtime();
  wait_landed(S, seen, N_X);
  const u32 b2h = lds_ld<u16>(S, O_MISC + 48 + ((l & 4) ? 16 : 0) + (((b & 3) * 8) & 15) + p4 * 2);
  const u32 xfh = lds_ld<u16>(S, O_X + (fcol & 511) * 2);
  u32 qoff = WS_QP + ((g * 4 + 2 * (l >> 5)) * 256 + w * 64 + 2 * (l & 31)) * 8;
  u32 adk = O_K + (32 * w + n16) * 256;
  u32 a_qs = lb + O_QS + (n16 & 3) * 256 + j4 * 16, a_qw = O_QS + w * 256 + (l & 31) * 8;
  u32 a_pw = O_P + (n16 & 3) * 256 + (32 * w + 4 * j4) * 2, a_pr = lb + O_P + (n16 >> 2) * 256 + j4 * 16;
  u32 a_mw = O_MW + (l & 3) * 16 + w * 4, a_mr = lb + O_MW + j4 * 16;
  u32 ad0 = O_V + (8 * j4 + q4) * 256 + (((4 * w + (p4 >> 1)) ^ ((q4 | ((j4 & 1) << 2)) << 1)) * 16) + (p4 & 1) * 8, ad1 = ad0 ^ 32;
  u32 a_op = WS_OP + ((g * 32 + (j4 * 4 + w) * 2 + (n16 >> 3)) * 32 + s) * 128 + (n16 & 7) * 16;
  u32 a_st = (WS_ST + ((g * 32 + j4 * 8 + 2 * w + ((n16 >> 2) & 1)) * 32 + s) * 64 + p4 * 16) | msk(n16 < 8);
  u32 ooff = WS_OP + (me * 32 + (l >> 1)) * 128 + (2 * w + (l & 1)) * 16;
  u32 adw = O_WO + (4 * j4 + q4) * 1024 + (((16 * w + (p4 >> 1)) ^ (((4 * j4 + q4) & 7) << 1)) * 16) + (p4 & 1) * 8;
  u32 a_sp = WS_SB + ((w * 16 + (l >> 2)) * 256 + me) * 64 + (l & 3) * 16;
  u32 off0 = WS_SB + ((b & 63) * 256 + 64 * w + (l >> 2)) * 64 + (l & 3) * 16;
  asm volatile("" : "+v"(qoff), "+v"(adk), "+v"(a_qs), "+v"(a_qw), "+v"(a_pw), "+v"(a_pr), "+v"(a_mw), "+v"(a_mr), "+v"(ad0), "+v"(ad1));
  asm volatile("" : "+v"(a_op), "+v"(a_st), "+v"(ooff), "+v"(adw), "+v"(a_sp), "+v"(off0));
  const u32 a_fq = lb + O_FL + 4 * F_Q, a_fp = lb + O_FL + 4 * F_P;
  u32x4 kf[8];
  {
    wait_until(tpub + Q_D);
    u32x4 qa = ld1(rws, qoff), qb = ld1(rws, qoff + 2048);
    wait_landed(S, seen, N_W1 + 8 * (w + 1));
#pragma unroll
    for (int i = 0; i < 8; ++i) kf[i] = lds_ld<u32x4>(S, adk + (i >> 2) * 4096 + (((4 * (i & 3) + j4) ^ n16) * 16));
_Pragma("unroll 1")
    for (int spin = 0; spin < SPIN_CAP && !__all(tok(qa, tag) & tok(qb, tag)); ++spin) { MEMCLOB(); qa = ldsc(rws, qoff); qb = ldsc(rws, qoff + 2048); }
    const float q0 = x32_sum(lo16(qa.x) + lo16(qb.x)), q1 = x32_sum(hi16(qa.x) + hi16(qb.x)), q2 = x32_sum(lo16(qa.z) + lo16(qb.z)), q3 = x32_sum(hi16(qa.z) + hi16(qb.z));
    if (l < 32) lds_st<u32x2>(S, a_qw, u32x2{pk(q0, q1), pk(q2, q3)});
    if (l == 0) set_flag(S, F_Q + w, 1);
  }
  {
    u32x4 fl4, bq0, bq1, bq2, bq3;
#pragma unroll 1
    for (int spin = 0; spin < LSPIN; ++spin) {
      asm volatile("ds_read_b128 %0, %5\n\tds_read_b128 %1, %6\n\tds_read_b128 %2, %6 offset:64\n\tds_read_b128 %3, %6 offset:128\n\tds_read_b128 %4, %6 offset:192\n\ts_waitcnt lgkmcnt(0)"
                   : "=&v"(fl4), "=&v"(bq0), "=&v"(bq1), "=&v"(bq2), "=&v"(bq3) : "v"(a_fq), "v"(a_qs) : "memory");
      if (all4(fl4)) break;
    }
    f32x4 d0 = mfma32(kf[0], bq0, f32x4{0.f, 0.f, 0.f, 0.f}), d1 = mfma32(kf[4], bq0, f32x4{0.f, 0.f, 0.f, 0.f});
    d0 = mfma32(kf[1], bq1, d0); d1 = mfma32(kf[5], bq1, d1);
    d0 = mfma32(kf[2], bq2, d0); d1 = mfma32(kf[6], bq2, d1);
    d0 = mfma32(kf[3], bq3, d0); d1 = mfma32(kf[7], bq3, d1);
    constexpr float C2 = 0.08838834764831845f * 1.4426950408889634f;
    float sc[8];
#pragma unroll
    for (int i = 0; i < 4; ++i) { sc[i] = d0[i] * C2; sc[4 + i] = d1[i] * C2; }
    const float mw = xr_max(fmaxf(fmaxf(fmaxf(sc[0], sc[1]), fmaxf(sc[2], sc[3])), fmaxf(fmaxf(sc[4], sc[5]), fmaxf(sc[6], sc[7]))));
    float p[8];
#pragma unroll
    for (int i = 0; i < 8; ++i) p[i] = ex2(sc[i] - mw);
    if (n16 < 4) {
      lds_st<u32x2>(S, a_pw, u32x2{pk(p[0], p[1]), pk(p[2], p[3])});
      lds_st<u32x2>(S, a_pw + 32, u32x2{pk(p[4], p[5]), pk(p[6], p[7])});
      if (l < 4) lds_st<float>(S, a_mw, mw);
    }
    if (l == 0) set_flag(S, F_P + w, 1);
  }
  {
    u32x4 fl4, ap[4], m4u;
#pragma unroll 1
    for (int spin = 0; spin < LSPIN; ++spin) {
      asm volatile("ds_read_b128 %0, %6\n\tds_read_b128 %1, %7\n\tds_read_b128 %2, %7 offset:64\n\tds_read_b128 %3, %7 offset:128\n\tds_read_b128 %4, %7 offset:192\n\tds_read_b128 %5, %8\n\ts_waitcnt lgkmcnt(0)"
                   : "=&v"(fl4), "=&v"(ap[0]), "=&v"(ap[1]), "=&v"(ap[2]), "=&v"(ap[3]), "=&v"(m4u) : "v"(a_fp), "v"(a_pr), "v"(a_mr) : "memory");
      if (all4(fl4)) break;
    }
    const float m0 = f_of(m4u.x), m1 = f_of(m4u.y), m2 = f_of(m4u.z), m3 = f_of(m4u.w);
    const float M = vmax(vmax3(m0, m1, m2), m3);
    const float al[4] = {ex2(m0 - M), ex2(m1 - M), ex2(m2 - M), ex2(m3 - M)};
    const u32x4 ones = {0x3f803f80u, 0x3f803f80u, 0x3f803f80u, 0x3f803f80u};
    float L = 0;
#pragma unroll
    for (int ks = 0; ks < 4; ++ks) L = fmaf(al[ks], mfma32(ap[ks], ones, f32x4{0.f, 0.f, 0.f, 0.f})[0], L);
    stsc(rws, a_st, u32x4{u_of(M), tag, u_of(L), tag});
    float o0 = 0, o1 = 0;
    wait_lazy(S, F_LANDED, N_K + 16);
    wait_landed(S, seen, N_K + 24);
    const bool early = seen >= ST_AT;
    if (early && w == 0 && l < 32) arrive(ws + WS_HB + (g * 32 + l) * 128, 1u << s);
    {
      u32x2 vf[12];
#pragma unroll
      for (int i = 0; i < 12; ++i) vf[i] = lds_tr(S, ((i & 2) ? ad1 : ad0) + (i >> 2) * 8192 + (i & 1) * 1024);
#pragma unroll
      for (int ks = 0; ks < 3; ++ks) {
        o0 = fmaf(al[ks], mfma32(ap[ks], u32x4{vf[4 * ks].x, vf[4 * ks].y, vf[4 * ks + 1].x, vf[4 * ks + 1].y}, f32x4{0.f, 0.f, 0.f, 0.f})[0], o0);
        o1 = fmaf(al[ks], mfma32(ap[ks], u32x4{vf[4 * ks + 2].x, vf[4 * ks + 2].y, vf[4 * ks + 3].x, vf[4 * ks + 3].y}, f32x4{0.f, 0.f, 0.f, 0.f})[0], o1);
      }
    }
    if (!early) {
      wait_landed(S, seen, ST_AT);
      if (w == 0 && l < 32) arrive(ws + WS_HB + (g * 32 + l) * 128, 1u << s);
    }
    wait_landed(S, seen, N_V);
    {
      const u32x2 a0 = lds_tr(S, ad0 + 3 * 8192), a1 = lds_tr(S, ad0 + 3 * 8192 + 1024), c0 = lds_tr(S, ad1 + 3 * 8192), c1 = lds_tr(S, ad1 + 3 * 8192 + 1024);
      o0 = fmaf(al[3], mfma32(ap[3], u32x4{a0.x, a0.y, a1.x, a1.y}, f32x4{0.f, 0.f, 0.f, 0.f})[0], o0);
      o1 = fmaf(al[3], mfma32(ap[3], u32x4{c0.x, c0.y, c1.x, c1.y}, f32x4{0.f, 0.f, 0.f, 0.f})[0], o1);
    }
    stsc(rws, a_op, u32x4{u_of(o0), tag, u_of(o1), tag});
  }
  {
    wait_ge(S, F_OOK, 1);
    u32x4 ov = ld1(rws, ooff);
    wait_landed(S, seen, N_WO);
    u32x2 bw[8];
#pragma unroll
    for (int tt = 0; tt < 8; ++tt) bw[tt] = lds_tr(S, adw ^ (tt << 5));
    u32 bp = 0;
    if (hasM) { wait_ge(S, F_B + w, 1); bp = lds_ld<u32>(S, O_BP + w * 256 + l * 4); }
    wait_ge(S, F_WOK, 1);
    const float wn = lds_ld<float>(S, O_WT + (l >> 1) * 4);
_Pragma("unroll 1")
    for (int spin = 0; spin < SPIN_CAP && !__all(tok(ov, tag)); ++spin) { MEMCLOB(); ov = ldsc(rws, ooff); }
    if (t == 0) set_flag(S, F_OG, 1);
    float o0 = wn * f_of(ov.x), o1 = wn * f_of(ov.z);
    o0 += dpp<XOR2>(o0); o1 += dpp<XOR2>(o1); o0 += dpp<ROR4>(o0); o1 += dpp<ROR4>(o1); o0 += dpp<ROR8>(o0); o1 += dpp<ROR8>(o1);
    o0 = x16_sum(o0); o1 = x16_sum(o1); o0 = x32_sum(o0); o1 = x32_sum(o1);
    const u32 pr = pk(o0, o1), po = __builtin_amdgcn_mov_dpp(pr, XOR1, 0xf, 0xf, true);
    if (l == 0) { MEMCLOB(); *(volatile LAS u32x4*)(S + O_AV + w * 16) = u32x4{pr, po, 1u, 1u}; MEMCLOB(); }
    u32x4 av;
#pragma unroll 1
    for (int spin = 0; spin < LSPIN; ++spin) { av = *(volatile LAS u32x4*)(S + O_AV + j4 * 16); if (__all(av.z != 0)) break; }
    const u32x2 a = {av.x, av.y};
    f32x4 d[8];
#pragma unroll
    for (int tt = 0; tt < 8; ++tt) d[tt] = mfma16(a, bw[tt], f32x4{0.f, 0.f, 0.f, 0.f});
    const bool b0 = j4 & 1, b1_ = j4 & 2;
    const float e0 = b1_ ? (b0 ? d[6][0] : d[4][0]) : (b0 ? d[2][0] : d[0][0]), e1 = b1_ ? (b0 ? d[7][0] : d[5][0]) : (b0 ? d[3][0] : d[1][0]);
    stsc(rws, a_sp, u32x4{pk(e0, e1), tag, bp, tag});
  }
  if (b < NC) {
    const float b2f = f_of(b2h << 16), xff = f_of(xfh << 16);
    u32 a_fw = O_FIN + (l & 3) * 64 + w * 4;
    u32 a_ff = lb + O_FL + 4 * F_F, a_fr = lb + O_FIN + (l & 3) * 64 + ((l >> 2) & 1) * 16;
    asm volatile("" : "+v"(a_fw), "+v"(a_ff), "+v"(a_fr));
    wait_ge(S, F_FOK, 1);
    u32x4 v[4];
#pragma unroll
    for (int i = 0; i < 4; ++i) v[i] = ld1(rws, off0 + i * 1024);
_Pragma("unroll 1")
    for (int spin = 0; spin < SPIN_CAP && !__all(tok(v[0], tag) & tok(v[1], tag) & tok(v[2], tag) & tok(v[3], tag)); ++spin) {
      MEMCLOB();
#pragma unroll
      for (int i = 0; i < 4; ++i) v[i] = ldsc(rws, off0 + i * 1024);
    }
    float r[4];
    r[0] = (lo16(v[0].x) + lo16(v[1].x)) + (lo16(v[2].x) + lo16(v[3].x)); r[1] = (hi16(v[0].x) + hi16(v[1].x)) + (hi16(v[2].x) + hi16(v[3].x));
    r[2] = (lo16(v[0].z) + lo16(v[1].z)) + (lo16(v[2].z) + lo16(v[3].z)); r[3] = (hi16(v[0].z) + hi16(v[1].z)) + (hi16(v[2].z) + hi16(v[3].z));
#pragma unroll
    for (int i = 0; i < 4; ++i) r[i] += dpp<ROR4>(r[i]);
#pragma unroll
    for (int i = 0; i < 4; ++i) r[i] += dpp<ROR8>(r[i]);
#pragma unroll
    for (int i = 0; i < 4; ++i) r[i] = x16_sum(r[i]);
#pragma unroll
    for (int i = 0; i < 4; ++i) r[i] = x32_sum(r[i]);
    if (l < 4) {
#pragma unroll
      for (int i = 0; i < 4; ++i) lds_st<float>(S, a_fw + i * 16, r[i]);
    }
    if (l == 0) set_flag(S, F_F + w, 1);
    if (w == 0) {
      u32x4 fl4, s4, m4;
#pragma unroll 1
      for (int spin = 0; spin < LSPIN; ++spin) {
        asm volatile("ds_read_b128 %0, %3\n\tds_read_b128 %1, %4\n\tds_read_b128 %2, %4 offset:32\n\ts_waitcnt lgkmcnt(0)"
                     : "=&v"(fl4), "=&v"(s4), "=&v"(m4) : "v"(a_ff), "v"(a_fr) : "memory");
        if (all4(fl4)) break;
      }
      const float sv = (f_of(s4.x) + f_of(s4.y)) + (f_of(s4.z) + f_of(s4.w)), mv = (f_of(m4.x) + f_of(m4.y)) + (f_of(m4.z) + f_of(m4.w));
      const float B = rbf(rbf(mv) + b2f);
      const float gt = rbf(B * rcp(1.f + ex2(-B * 1.4426950408889634f)));
      const float o = xff + rbf(rbf(sv) * gt);
      if (l < 8) out[fcol] = (u16)pk(o, o);
      if (b == 0 && l == 0) __builtin_amdgcn_raw_buffer_store_b32(epoch + 1u, rws, WS_EPOCH, 0, AUX_SC1);
    }
  }
  __builtin_amdgcn_endpgm();
  }

  __builtin_amdgcn_s_setprio(3);
  const u32 fl = (u32)(size_t)(S + O_FL + 4 * F_LANDED);
  asm volatile("" :: "s"(kc), "s"(vc), "s"(w1), "s"(w2), "s"(wo), "s"(x), "s"(ws), "s"(b1), "s"(b2));
  {
    __builtin_amdgcn_raw_ptr_buffer_load_lds(mk(x, 1024), (LAS void*)(S + O_X), 16, l * 16, 0, 0, 0);
    const u32 f0 = g * 256 + (s >> 2) * 32 + (s & 3) * 4, c0 = ((b & 63) >> 4) * 128 + ((b >> 2) & 3) * 32 + (b & 3) * 4;
    const char* p = (const char*)ws;
    if (l == 1 || l == 2) p = (const char*)b1 + (((hasM ? f0 : 0) + (l - 1) * 16) * 2 & ~15u);
    if (l == 3 || l == 4) p = (const char*)b2 + ((c0 + (l - 3) * 16) * 2 & ~15u);
    __builtin_amdgcn_global_load_lds((const __attribute__((address_space(1))) void*)p, (LAS void*)(S + O_MISC), 16, 0, 0);
  }
  {
    const rsrc_t r1 = mk(w1, hasM ? 512 * FF * 2 : 0);
    const u32 hb = l >> 5, bl = hb * (FF * 2) + (((l & 31) ^ (hb << 1)) * 16);
    const u32 base = (s * 16) * (FF * 2) + g * 512;
#pragma unroll 1
    for (int G = 0; G < 2; ++G) dma4(r1, S, O_W1 + G * 4096, bl, (bl ^ 64) + 6144, (bl ^ 128) + 2 * 6144, (bl ^ 192) + 3 * 6144, base + G * 8 * (FF * 2));
  }
  const size_t kvoff = ((size_t)g * SK + (SK - WIN) + s * 128) * 256;
  {
    const rsrc_t rk = mk((const char*)kc + kvoff, 32768);
    const u32 v0 = j4 * 256 + ((n16 ^ j4) * 16);
    wait_until(tstart + K_T);
#pragma unroll 1
    for (int G = 0; G < 8; ++G) { landed<WINDOW - 4>(fl, N_W1 + 4 * G - (WINDOW - 4)); dma4(rk, S, O_K + G * 4096, v0, v0 ^ 64, v0 ^ 128, v0 ^ 192, G * 4096); }
  }
  {
    const rsrc_t rv = mk((const char*)vc + kvoff, 32768);
    const u32 v0 = j4 * 256 + ((n16 ^ (j4 << 1)) * 16);
#pragma unroll 1
    for (int G = 0; G < 8; ++G) { landed<WINDOW - 4>(fl, N_K + 4 * G - (WINDOW - 4)); dma4(rv, S, O_V + G * 4096, v0, v0, v0 ^ 128, v0 ^ 128, G * 4096); }
  }
  {
    const rsrc_t r2 = mk((const char*)w2 - 1024, hasM ? 512 * FF * 2 + 1024 : 0);
    const u32 f0 = g * 256 + (s >> 2) * 32 + (s & 3) * 4;
    const rsrc_t ro = mk((const char*)wo - 1024, 4096 * 512 * 2 + 1024);
    const u32 r0 = g * 512 + (s >> 3) * 128 + ((s >> 1) & 3) * 32 + (s & 1) * 8;
#pragma unroll 1
    for (int k = 0; k < 6; ++k) {
      if (POSTMODE == 0) landed<WINDOW2 - 4>(fl, N_V + 4 * k - (WINDOW2 - 4));
      const bool is_w2 = WO_FIRST ? k >= 4 : k < 2;
      const int G = WO_FIRST ? (k & 3) : (k < 2 ? k : k - 2);
      if (is_w2) {
        const int G2 = G & 1;
        const u32 v = (l * 16) ^ (G2 << 7);
        dma4(r2, S, O_W2 + G2 * 4096, v + 1024, (v ^ 32) + 16384, v ^ 64, (v ^ 96) + 15360, (f0 + 2 * G2) * 1024);
      } else {
        const u32 v = (l * 16) ^ ((G & 1) << 7);
        dma4(ro, S, O_WO + G * 4096, v + 1024, (v ^ 32) + 16384, v ^ 64, (v ^ 96) + 15360, (r0 + 2 * G) * 1024);
      }
    }
  }
  drain<20>(fl, N_END);
}

extern "C" void kernel_launch(void* const* d_in, const int* in_sizes, int n_in, void* d_out, int out_size, void* d_ws, size_t ws_size, hipStream_t stream) {
  fused_decode<<<NWG, NT, 0, stream>>>((const u16*)d_in[0], (const u16*)d_in[1], (const u16*)d_in[2], (const u16*)d_in[3], (const u16*)d_in[4],
                                       (const u16*)d_in[6], (const u16*)d_in[7], (const u16*)d_in[8], (const u16*)d_in[9], (u16*)d_out, (unsigned char*)d_ws);
}
```

```cpp
#include <hip/hip_runtime.h>
#include <stdint.h>

typedef unsigned short u16;
typedef unsigned u32;
typedef unsigned long long u64;
typedef u32 u32x4 __attribute__((ext_vector_type(4)));
typedef u32 u32x2 __attribute__((ext_vector_type(2)));
typedef float f32x4 __attribute__((ext_vector_type(4)));
typedef float f32x2 __attribute__((ext_vector_type(2)));
typedef short s16x4 __attribute__((ext_vector_type(4)));
typedef __bf16 bf16x8 __attribute__((ext_vector_type(8)));
typedef __bf16 bf16x2 __attribute__((ext_vector_type(2)));
typedef __amdgpu_buffer_rsrc_t rsrc_t;
#define LAS __attribute__((address_space(3)))

constexpr int SK = 65536, WIN = 4096, FF = 1792;
constexpr int NWG = 256, NT = 640;
constexpr int NC = 64;
constexpr u32 WS_EPOCH = 0;
constexpr u32 WS_QP = 4096;
constexpr u32 WS_HB = WS_QP + 8 * 4 * 256 * 8;
constexpr u32 WS_ST = WS_HB + 256 * 128;
constexpr u32 WS_OP = WS_ST + 256 * 2048;
constexpr u32 WS_HP = WS_OP + 256 * 32 * 128;
constexpr u32 WS_SB = WS_HP + 224 * 32 * 64;
constexpr u32 WS_END = WS_SB + NC * 256 * 64;
constexpr u32 OOB = 0x80000000u;
constexpr u32 O_WQ = 0, O_K = 16384, O_V = 49152, O_W1 = 81920, O_W2 = 90112, O_WO = 98304, O_S = 114688;
constexpr u32 O_X = O_S, O_MISC = O_X + 1024, O_S2 = O_MISC + 1024;
constexpr u32 O_XN = O_S2, O_Y = O_XN + 1024, O_QS = O_Y + 1024, O_P = O_QS + 1024, O_MW = O_P + 1024, O_AV = O_MW + 128,
              O_EP = O_AV + 64, O_WT = O_EP + 16, O_HV = O_WT + 128, O_HX = O_HV + 64, O_FIN = O_HX + 128, O_BP = O_FIN + 512, O_FL = O_BP + 1024, LDS_BYTES = O_FL + 256;
enum { F_LANDED = 0, F_OOK = 2, F_FOK = 4, F_WOK = 5, F_OG = 6, F_XN = 7, F_Y = 8, F_Q = 12, F_P = 16, F_WQ = 20, F_F = 24, F_B = 28, F_H = 32 };
constexpr int N_X = 2, N_W1 = N_X + 8, N_K = N_W1 + 32, N_V = N_K + 32, N_W2 = N_V + 8, N_WO = N_W2 + 16;
constexpr int WINDOW = 24;
constexpr int ST_AT = N_V - 4;
constexpr int K_T = 100;
constexpr int Q_D = 80;
constexpr int H_D = 100;
constexpr int CODE_BYTES = 12016;
constexpr int AUX_NT = 2, AUX_SC1 = 16;
constexpr int LSPIN = 1 << 22, SPIN_CAP = 1 << 14;

#define MEMCLOB() asm volatile("" ::: "memory")

__device__ __forceinline__ rsrc_t mk(const void* p, u32 bytes) { return __builtin_amdgcn_make_buffer_rsrc((void*)p, 0, bytes, 0x00020000); }
__device__ __forceinline__ u32x4 ldsc(rsrc_t r, u32 voff) { return __builtin_amdgcn_raw_buffer_load_b128(r, voff, 0, AUX_SC1); }
__device__ __forceinline__ void stsc(rsrc_t r, u32 voff, u32x4 v) { __builtin_amdgcn_raw_buffer_store_b128(v, r, voff, 0, AUX_SC1); }
__device__ __forceinline__ void stsc2(rsrc_t r, u32 voff, u32x2 v) { __builtin_amdgcn_raw_buffer_store_b64(v, r, voff, 0, AUX_SC1); }
__device__ __forceinline__ float f_of(u32 u) { return __builtin_bit_cast(float, u); }
__device__ __forceinline__ u32 u_of(float f) { return __builtin_bit_cast(u32, f); }
__device__ __forceinline__ float lo16(u32 w) { return f_of(w << 16); }
__device__ __forceinline__ float hi16(u32 w) { return f_of(w & 0xffff0000u); }
__device__ __forceinline__ u32 pk(float lo, float hi) { bf16x2 r = {(__bf16)lo, (__bf16)hi}; return __builtin_bit_cast(u32, r); }
__device__ __forceinline__ float rbf(float f) { return lo16(pk(f, f)); }
template <int CTRL> __device__ __forceinline__ float dpp(float x) { return f_of(__builtin_amdgcn_mov_dpp(u_of(x), CTRL, 0xf, 0xf, true)); }
constexpr int XOR1 = 0xB1, XOR2 = 0x4E, HMIR = 0x141, MIR = 0x140, ROR4 = 0x124, ROR8 = 0x128;
__device__ __forceinline__ float x16_sum(float x) { auto s = __builtin_amdgcn_permlane16_swap(u_of(x), u_of(x), false, false); return f_of(s[0]) + f_of(s[1]); }
__device__ __forceinline__ float x32_sum(float x) { auto s = __builtin_amdgcn_permlane32_swap(u_of(x), u_of(x), false, false); return f_of(s[0]) + f_of(s[1]); }
__device__ __forceinline__ float row_sum(float x) { x += dpp<XOR1>(x); x += dpp<XOR2>(x); x += dpp<HMIR>(x); x += dpp<MIR>(x); return x; }
__device__ __forceinline__ float wave_sum(float x) { return x32_sum(x16_sum(row_sum(x))); }
__device__ __forceinline__ float vmax(float a, float b) { float r; asm("v_max_f32 %0, %1, %2" : "=v"(r) : "v"(a), "v"(b)); return r; }
__device__ __forceinline__ float vmax3(float a, float b, float c) { float r; asm("v_max3_f32 %0, %1, %2, %3" : "=v"(r) : "v"(a), "v"(b), "v"(c)); return r; }
#define DPPMAX(x, ctrl) asm("s_nop 1\n\tv_max_f32_dpp %0, %0, %0 " ctrl " row_mask:0xf bank_mask:0xf" : "+v"(x))
__device__ __forceinline__ float xr_max(float x) {
  auto s = __builtin_amdgcn_permlane16_swap(u_of(x), u_of(x), false, false); x = vmax(f_of(s[0]), f_of(s[1]));
  auto t = __builtin_amdgcn_permlane32_swap(u_of(x), u_of(x), false, false); return vmax(f_of(t[0]), f_of(t[1]));
}
__device__ __forceinline__ float ex2(float x) { return __builtin_amdgcn_exp2f(x); }
__device__ __forceinline__ float rcp(float x) { return __builtin_amdgcn_rcpf(x); }
__device__ __forceinline__ f32x4 mfma32(u32x4 a, u32x4 b, f32x4 c) {
  return __builtin_amdgcn_mfma_f32_16x16x32_bf16(__builtin_bit_cast(bf16x8, a), __builtin_bit_cast(bf16x8, b), c, 0, 0, 0);
}
__device__ __forceinline__ f32x4 mfma16(u32x2 a, u32x2 b, f32x4 c) {
  return __builtin_amdgcn_mfma_f32_16x16x16bf16_1k(__builtin_bit_cast(s16x4, a), __builtin_bit_cast(s16x4, b), c, 0, 0, 0);
}
typedef LAS unsigned char* lds_t;
template <class T> __device__ __forceinline__ T lds_ld(lds_t s, u32 off) { return *(const LAS T*)(s + off); }
template <class T> __device__ __forceinline__ void lds_st(lds_t s, u32 off, T v) { *(LAS T*)(s + off) = v; }
__device__ __forceinline__ u32x2 lds_tr(lds_t s, u32 off) { return __builtin_bit_cast(u32x2, __builtin_amdgcn_ds_read_tr16_b64_v4i16((LAS s16x4*)(s + off))); }
__device__ __forceinline__ void set_flag(lds_t s, int f, u32 v) { MEMCLOB(); *(volatile LAS u32*)(s + O_FL + 4 * f) = v; MEMCLOB(); }
__device__ __forceinline__ void wait_ge(lds_t s, int f, int need) {
#pragma unroll 1
  for (int spin = 0; spin < LSPIN; ++spin) {
    const int v = __builtin_amdgcn_readfirstlane(*(volatile LAS int*)(s + O_FL + 4 * f));
    if (v >= need) break;
  }
  MEMCLOB();
}
__device__ __forceinline__ void wait_lazy(lds_t s, int f, int need) {
#pragma unroll 1
  for (int spin = 0; spin < LSPIN; ++spin) {
    const int v = __builtin_amdgcn_readfirstlane(*(volatile LAS int*)(s + O_FL + 4 * f));
    if (v >= need) break;
    __builtin_amdgcn_s_sleep(6);
  }
  MEMCLOB();
}
__device__ __forceinline__ void wait_landed(lds_t s, int& seen, int need) {
  if (seen < need) {
#pragma unroll 1
    for (int spin = 0; spin < LSPIN; ++spin) {
      seen = __builtin_amdgcn_readfirstlane(*(volatile LAS int*)(s + O_FL + 4 * F_LANDED));
      if (seen >= need) break;
      }
  }
  MEMCLOB();
}
__device__ __forceinline__ bool all4(u32x4 f) { return __builtin_amdgcn_readfirstlane(f.x & f.y & f.z & f.w) != 0; }
__device__ __forceinline__ void wait4(lds_t s, int f) {
#pragma unroll 1
  for (int spin = 0; spin < LSPIN; ++spin) {
    const u32x4 v = *(volatile LAS u32x4*)(s + O_FL + 4 * f);
    if (__builtin_amdgcn_readfirstlane(v.x & v.y & v.z & v.w)) break;
  }
  MEMCLOB();
}
__device__ __forceinline__ u32x4 ln8(u32x4 xr) {
  float v[8];
#pragma unroll
  for (int i = 0; i < 4; ++i) { v[2 * i] = lo16(xr[i]); v[2 * i + 1] = hi16(xr[i]); }
  const float s = ((v[0] + v[1]) + (v[2] + v[3])) + ((v[4] + v[5]) + (v[6] + v[7]));
  const float m = wave_sum(s) * (1.f / 512);
  float q0 = 0, q1 = 0;
#pragma unroll
  for (int i = 0; i < 4; ++i) { const float d0 = v[2 * i] - m, d1 = v[2 * i + 1] - m; q0 = fmaf(d0, d0, q0); q1 = fmaf(d1, d1, q1); }
  const float var = wave_sum(q0 + q1) * (1.f / 512);
  const float mb = rbf(m);
  const float inv = rcp(rbf(__builtin_amdgcn_sqrtf(rbf(rbf(var) + 1e-5f))));
  u32x4 r;
#pragma unroll
  for (int i = 0; i < 4; ++i) { const u32 d = pk(v[2 * i] - mb, v[2 * i + 1] - mb); r[i] = pk(lo16(d) * inv, hi16(d) * inv); }
  return r;
}
__device__ __forceinline__ void poll_word(const unsigned char* p, u32 tag) {
  int n;
  asm volatile(
    "v_mov_b32 v108, 0\n\tv_mov_b32 v109, 0\n\t"
    "s_mov_b64 exec, 1\n\t"
    "global_atomic_or_x2 v[104:105], %1, v[108:109], off sc0 sc1\n\t"
    "s_sleep 4\n\t"
    "global_atomic_or_x2 v[106:107], %1, v[108:109], off sc0 sc1\n\t"
    "s_mov_b32 %0, 0\n"
    "1:\n\t"
    "s_waitcnt vmcnt(1)\n\t"
    "v_not_b32 v104, v104\n\tv_xor_b32 v105, %2, v105\n\tv_or_b32 v104, v104, v105\n\t"
    "v_cmp_ne_u32 vcc, 0, v104\n\t"
    "s_cbranch_vccz 2f\n\t"
    "global_atomic_or_x2 v[104:105], %1, v[108:109], off sc0 sc1\n\t"
    "s_waitcnt vmcnt(1)\n\t"
    "v_not_b32 v106, v106\n\tv_xor_b32 v107, %2, v107\n\tv_or_b32 v106, v106, v107\n\t"
    "v_cmp_ne_u32 vcc, 0, v106\n\t"
    "s_cbranch_vccz 2f\n\t"
    "global_atomic_or_x2 v[106:107], %1, v[108:109], off sc0 sc1\n\t"
    "s_add_u32 %0, %0, 1\n\t"
    "s_cmp_lt_u32 %0, %3\n\t"
    "s_cbranch_scc1 1b\n"
    "2:\n\t"
    "s_mov_b64 exec, -1"
    : "=&s"(n) : "v"(p), "v"(tag), "s"(SPIN_CAP) : "vcc", "scc", "v104", "v105", "v106", "v107", "v108", "v109", "memory");
}
__device__ __forceinline__ void arrive(unsigned char* p, u32 bit) {
  __hip_atomic_fetch_or((u64*)p, (u64)bit, __ATOMIC_RELAXED, __HIP_MEMORY_SCOPE_AGENT);
}
__device__ __forceinline__ void wait_until(u32 t) { while ((int)((u32)__builtin_amdgcn_s_memrealtime() - t) < 0) __builtin_amdgcn_s_sleep(3); }
__device__ __forceinline__ void dma4(rsrc_t r, lds_t s, u32 ldsoff, u32 v0, u32 v1, u32 v2, u32 v3, u32 soff) {
  __builtin_amdgcn_raw_ptr_buffer_load_lds(r, (LAS void*)(s + ldsoff), 16, v0, soff, 0, AUX_NT);
  __builtin_amdgcn_raw_ptr_buffer_load_lds(r, (LAS void*)(s + ldsoff), 16, v1, soff, 1024, AUX_NT);
  __builtin_amdgcn_raw_ptr_buffer_load_lds(r, (LAS void*)(s + ldsoff), 16, v2, soff, 2048, AUX_NT);
  __builtin_amdgcn_raw_ptr_buffer_load_lds(r, (LAS void*)(s + ldsoff), 16, v3, soff, 3072, AUX_NT);
}
template <int N> __device__ __forceinline__ void landed(u32 flag_addr, int v) {
  asm volatile("s_waitcnt vmcnt(%0)\n\tds_write_b32 %1, %2" :: "i"(N), "v"(flag_addr), "v"(v) : "memory");
}
template <int N> __device__ __forceinline__ void drain(u32 flag_addr, int total) {
  landed<N>(flag_addr, total - N);
  if constexpr (N >= 4) drain<N - 4>(flag_addr, total);
}
__device__ __forceinline__ u32 msk(bool c) { return c ? 0u : OOB; }
__device__ __forceinline__ bool tok(u32x4 v, u32 tag) { return (v.y == tag) & (v.w == tag); }
__device__ __forceinline__ u32 mktag(u32 epoch) { return ((epoch + 1u) * 0x9E3779B1u) ^ 0x5bd1e995u; }

__global__ void __launch_bounds__(NT) fused_decode(const u16* __restrict__ x, const u16* __restrict__ kc, const u16* __restrict__ vc,
                                                   const u16* __restrict__ wq, const u16* __restrict__ wo, const u16* __restrict__ w1,
                                                   const u16* __restrict__ b1, const u16* __restrict__ w2, const u16* __restrict__ b2,
                                                   u16* __restrict__ out, unsigned char* __restrict__ ws) {
  __shared__ __attribute__((aligned(16))) unsigned char smem[LDS_BYTES];
  lds_t S = (lds_t)smem;
  const u32 tstart = (u32)__builtin_amdgcn_s_memrealtime();
  const int t = threadIdx.x, l = t & 63, wv = __builtin_amdgcn_readfirstlane(t >> 6);
  if (wv == 0) {
    lds_st<u32>(S, O_FL + 4 * l, 0u); if (l < 16) lds_st<u32>(S, O_AV + 4 * l, 0u);
    asm volatile("s_waitcnt lgkmcnt(0)" ::: "memory");
  }
  asm volatile("s_load_dword s94, %0, 0x0\n\ts_load_dword s95, %0, 0x40" :: "s"(__builtin_amdgcn_kernarg_segment_ptr()) : "s94", "s95", "memory");
  const rsrc_t rcode = mk((const void*)(__builtin_amdgcn_s_getpc() & ~255ull), CODE_BYTES);
  {
    const u32 sel = (blockIdx.x >> 3) + wv;
    asm volatile("buffer_load_dwordx4 v[120:123], %0, %1, 0 offen\n\tbuffer_load_dwordx4 v[120:123], %2, %1, 0 offen"
                 :: "v"((l * 16 + (wv + 1) * 1024) | ((sel & 3) ? OOB : 0u)), "s"(rcode), "v"((l * 16 + (wv + 11) * 1024) | ((sel & 3) ? OOB : 0u))
                 : "v120", "v121", "v122", "v123", "memory");
  }
  asm volatile("s_barrier" ::: "memory");
  const int b = blockIdx.x, g = b & 7, s = b >> 3;
  const int n16 = l & 15, j4 = l >> 4, q4 = (l >> 2) & 3, p4 = l & 3;
  const bool hasM = g < 7;
  const int me = g * 32 + s;
  const rsrc_t rws = mk(ws, WS_END);
  if (wv < 4) {
    if (wv == 0) {
      const rsrc_t rx = mk(x, 1024);
      asm volatile("buffer_load_dwordx4 v[110:113], %0, %1, 0 offen\n\tbuffer_load_dword v114, off, %2, 0"
                   :: "v"(l * 16), "s"(rx), "s"(rws) : "v110", "v111", "v112", "v113", "v114", "memory");
    }
    {
      const rsrc_t rq = mk(wq, 512 * 4096 * 2);
      const u32 base = ((s & 3) * 128 + 32 * wv) * 8192 + (g * 512 + (s >> 2) * 64) * 2;
      const u32 v0 = (l >> 3) * 8192 + (((l & 7) ^ (((l >> 4) & 1) << 1)) * 16), v1 = (v0 ^ 64) + 64512;
      const u32 m = (u32)(size_t)(S + O_WQ) + wv * 4096;
      asm volatile("s_mov_b32 m0, %0\n\ts_nop 0\n\t"
                   "buffer_load_dwordx4 %1, %5, %6 offen nt lds\n\tbuffer_load_dwordx4 %2, %5, %6 offen offset:1024 nt lds\n\t"
                   "buffer_load_dwordx4 %3, %5, %6 offen offset:2048 nt lds\n\tbuffer_load_dwordx4 %4, %5, %6 offen offset:3072 nt lds"
                   :: "s"(m), "v"(v0), "v"(v1), "v"(v0 + 2 * 64512), "v"(v1 + 2 * 64512), "s"(rq), "s"(base) : "memory");
    }
  }

  if (wv != 8) {
  asm volatile("" :: "s"(out));

  if (wv == 9) {
    u32 soff = (WS_ST + (me * 32 + (l & 31)) * 64) | msk(l < 32);
    wait_ge(S, F_XN, 1);
    const u32 tag = mktag(lds_ld<u32>(S, O_EP));
    stsc(rws, (WS_HB + ((l & 4) ? (b & 63) : me) * 128 + ((l & 4) ? 64 : 0) + (l & 3) * 16) | msk(l < 4 || (l < 8 && b < NC)), u32x4{0u, tag, 0u, tag});
    wait_lazy(S, F_LANDED, N_V - 8);
    wait_ge(S, F_LANDED, N_V);
    poll_word(ws + WS_HB + me * 128, tag);
    set_flag(S, F_OOK, 1);
    {
      u32x4 sv = ldsc(rws, soff);
#pragma unroll 1
      for (int spin = 0; spin < SPIN_CAP && !__all(tok(sv, tag) | (l >= 32)); ++spin) { MEMCLOB(); sv = ldsc(rws, soff); }
      float mp = f_of(sv.x);
      float Mx = mp;
      DPPMAX(Mx, "quad_perm:[1,0,3,2]"); DPPMAX(Mx, "quad_perm:[2,3,0,1]"); DPPMAX(Mx, "row_half_mirror"); DPPMAX(Mx, "row_mirror");
      { auto r = __builtin_amdgcn_permlane16_swap(u_of(Mx), u_of(Mx), false, false); Mx = vmax(f_of(r[0]), f_of(r[1])); }
      const float wp = ex2(mp - Mx);
      const float wn = wp * rcp(x16_sum(row_sum(wp * f_of(sv.z))));
      if (l < 32) lds_st<float>(S, O_WT + l * 4, wn);
    }
    set_flag(S, F_WOK, 1);
    if ((s & 7) == 0) {
      wait_ge(S, F_OG, 1);
      arrive(ws + WS_HB + l * 128 + 64, 1u << (g * 4 + (s >> 3)));
    }
    if (b < NC) {
      poll_word(ws + WS_HB + b * 128 + 64, tag);
      set_flag(S, F_FOK, 1);
    }
    __builtin_amdgcn_endpgm();
  }

  if (wv >= 4 && !hasM) __builtin_amdgcn_endpgm();
  if (wv == 0) {
    u32x4 xr; u32 ep;
    asm volatile("s_waitcnt vmcnt(4)\n\tv_mov_b32 %0, v110\n\tv_mov_b32 %1, v111\n\tv_mov_b32 %2, v112\n\tv_mov_b32 %3, v113\n\tv_mov_b32 %4, v114"
                 : "=v"(xr.x), "=v"(xr.y), "=v"(xr.z), "=v"(xr.w), "=v"(ep) :: "memory");
    lds_st<u32x4>(S, O_XN + l * 16, ln8(xr));
    if (l == 0) { lds_st<u32>(S, O_EP, ep); set_flag(S, F_XN, 1); }
  }
  wait_ge(S, F_XN, 1);
  const u32 epoch = lds_ld<u32>(S, O_EP);

  if (wv >= 4) {
    const int w = wv - 4;
    const u32 f0 = g * 256 + (s >> 2) * 32 + (s & 3) * 4;
    if (w == 0) { lds_st<u32x4>(S, O_Y + l * 16, ln8(lds_ld<u32x4>(S, O_XN + l * 16))); if (l == 0) set_flag(S, F_Y, 1); }
    wait_ge(S, F_Y, 1);
    const u32x2 ay = lds_ld<u32x2>(S, O_Y + (s * 16 + 4 * j4) * 2);
    wait_ge(S, F_LANDED, N_X);
    const u32 b1v = lds_ld<u16>(S, O_MISC + 16 + ((l & 4) ? 16 : 0) + ((f0 * 2) & 15) + p4 * 2);
    const u32 tag = mktag(epoch);
    u32 a_hp = (WS_HP + ((g * 32 + (w * 2 + (j4 & 1)) * 4 + (n16 >> 2)) * 32 + s) * 64 + (n16 & 3) * 16) | msk(l < 32);
    u32 a_bp = O_BP + w * 256 + l * 4;
    u32 hoff = (WS_HP + (me * 32 + 8 * w + ((l >> 2) & 7)) * 64 + (l & 3) * 16) | msk(l < 32);
    u32 ad1 = O_W1 + (4 * j4 + q4) * 512 + (((8 * w + (p4 >> 1)) ^ (((4 * j4 + q4) & 7) << 1)) * 16) + (p4 & 1) * 8;
    u32 ad2 = O_W2 + ((4 * j4 + q4) & 7) * 1024 + (((16 * w + (p4 >> 1)) ^ (((4 * j4 + q4) & 7) << 1)) * 16) + (p4 & 1) * 8;
    float b1f = f_of(b1v << 16);
    asm volatile("" : "+v"(a_hp), "+v"(a_bp), "+v"(hoff), "+v"(ad1), "+v"(ad2), "+v"(b1f));
    wait_ge(S, F_LANDED, N_W1);
    {
      f32x4 d[4];
#pragma unroll
      for (int tt = 0; tt < 4; ++tt) d[tt] = mfma16(ay, lds_tr(S, ad1 ^ (tt << 5)), f32x4{0.f, 0.f, 0.f, 0.f});
      const bool up = j4 & 1;
      stsc(rws, a_hp, u32x4{u_of(up ? d[2][0] : d[0][0]), tag, u_of(up ? d[3][0] : d[1][0]), tag});
    }
    wait_until((u32)__builtin_amdgcn_s_memrealtime() + H_D);
    u32x4 h0 = ldsc(rws, hoff);
#pragma unroll 1
    for (int spin = 0; spin < SPIN_CAP && !__all(tok(h0, tag) | (l >= 32)); ++spin) { MEMCLOB(); h0 = ldsc(rws, hoff); }
    {
      float ha = f_of(h0.x), hb = f_of(h0.z);
      ha += dpp<ROR4>(ha); hb += dpp<ROR4>(hb); ha += dpp<ROR8>(ha); hb += dpp<ROR8>(hb);
      ha = x16_sum(ha); hb = x16_sum(hb);
      if (l < 4) { lds_st<float>(S, O_HX + l * 16 + w * 4, ha); lds_st<float>(S, O_HX + 64 + l * 16 + w * 4, hb); }
      if (l == 0) set_flag(S, F_H + w, 1);
      wait4(S, F_H);
      const f32x4 h4 = lds_ld<f32x4>(S, O_HX + ((l & 4) ? 64 : 0) + p4 * 16);
      hb = ha = (h4[0] + h4[1]) + (h4[2] + h4[3]);
      const float p = rbf(rbf((l & 4) ? hb : ha) + b1f);
      const float u = p * fmaf(0.044715f * 0.7978845608028654f * 2.8853900817779268f, p * p, 0.7978845608028654f * 2.8853900817779268f);
      const float h = p - p * rcp(ex2(u) + 1.f);
      if (l < 8) lds_st<u16>(S, O_HV + w * 16 + (p4 * 2 + (l >> 2)) * 2, (u16)pk(h, h));
    }
    u32x2 ah = lds_ld<u32x2>(S, O_HV + w * 16 + (j4 & 1) * 8);
    if (j4 >= 2) ah = u32x2{0u, 0u};
    wait_lazy(S, F_LANDED, N_V - 4);
    wait_ge(S, F_LANDED, N_W2);
    {
      f32x4 d[8];
#pragma unroll
      for (int tt = 0; tt < 8; ++tt) d[tt] = mfma16(ah, lds_tr(S, ad2 ^ (tt << 5)), f32x4{0.f, 0.f, 0.f, 0.f});
      const bool b0 = j4 & 1, b1_ = j4 & 2;
      const float e0 = b1_ ? (b0 ? d[6][0] : d[4][0]) : (b0 ? d[2][0] : d[0][0]), e1 = b1_ ? (b0 ? d[7][0] : d[5][0]) : (b0 ? d[3][0] : d[1][0]);
      lds_st<u32>(S, a_bp, pk(e0, e1));
      if (l == 0) set_flag(S, F_B + w, 1);
    }
    __builtin_amdgcn_endpgm();
  }

  const int w = wv;
  int seen = -100;
  const int fcol = (b >> 4) * 128 + ((b >> 2) & 3) * 32 + (b & 3) * 4 + p4 + ((l >> 2) & 1) * 16;
  const u32 tag = mktag(epoch);
  const u32 lb = (u32)(size_t)S;
  {
    u32x4 ax[4];
#pragma unroll
    for (int ks = 0; ks < 4; ++ks) ax[ks] = lds_ld<u32x4>(S, O_XN + ((s & 3) * 128 + 32 * ks + 8 * j4) * 2);
    const u32 gq = (q4 >> 1) | ((j4 & 1) << 1);
    u32 ad = O_WQ + (8 * j4 + q4) * 128 + (((2 * w + (p4 >> 1)) ^ (gq << 1)) * 16) + (p4 & 1) * 8;
    u32 a_qp = (WS_QP + ((g * 4 + (s & 3)) * 256 + (s >> 2) * 32 + w * 8 + (n16 >> 1)) * 8) | msk(l < 16 && (l & 1) == 0);
    asm volatile("" : "+v"(ad), "+v"(a_qp), "+v"(ax[0]), "+v"(ax[1]), "+v"(ax[2]), "+v"(ax[3]));
    asm volatile("s_waitcnt vmcnt(0)" ::: "memory");
    if (l == 0) set_flag(S, F_WQ + w, 1);
    wait4(S, F_WQ);
    u32x2 bb[8];
#pragma unroll
    for (int i = 0; i < 8; ++i) bb[i] = lds_tr(S, ad + (i >> 1) * 4096 + (i & 1) * 512);
    f32x4 d = {0.f, 0.f, 0.f, 0.f};
#pragma unroll
    for (int ks = 0; ks < 4; ++ks) d = mfma32(ax[ks], u32x4{bb[2 * ks].x, bb[2 * ks].y, bb[2 * ks + 1].x, bb[2 * ks + 1].y}, d);
    stsc2(rws, a_qp, u32x2{pk(d[0], dpp<XOR1>(d[0])), tag});
  }
  const u32 tpub = (u32)__builtin_amdgcn_s_memrealtime();
  wait_landed(S, seen, N_X);
  const u32 b2h = lds_ld<u16>(S, O_MISC + 48 + ((l & 4) ? 16 : 0) + (((b & 3) * 8) & 15) + p4 * 2);
  const u32 xfh = lds_ld<u16>(S, O_X + (fcol & 511) * 2);
  u32 qoff = WS_QP + ((g * 4 + 2 * (l >> 5)) * 256 + w * 64 + 2 * (l & 31)) * 8;
  u32 adk = O_K + (32 * w + n16) * 256;
  u32 a_qs = lb + O_QS + (n16 & 3) * 256 + j4 * 16, a_qw = O_QS + w * 256 + (l & 31) * 8;
  u32 a_pw = O_P + (n16 & 3) * 256 + (32 * w + 4 * j4) * 2, a_pr = lb + O_P + (n16 >> 2) * 256 + j4 * 16;
  u32 a_mw = O_MW + (l & 3) * 16 + w * 4, a_mr = lb + O_MW + j4 * 16;
  u32 ad0 = O_V + (8 * j4 + q4) * 256 + (((4 * w + (p4 >> 1)) ^ ((q4 | ((j4 & 1) << 2)) << 1)) * 16) + (p4 & 1) * 8, ad1 = ad0 ^ 32;
  u32 a_op = WS_OP + ((g * 32 + (j4 * 4 + w) * 2 + (n16 >> 3)) * 32 + s) * 128 + (n16 & 7) * 16;
  u32 a_st = (WS_ST + ((g * 32 + j4 * 8 + 2 * w + ((n16 >> 2) & 1)) * 32 + s) * 64 + p4 * 16) | msk(n16 < 8);
  u32 ooff = WS_OP + (me * 32 + (l >> 1)) * 128 + (2 * w + (l & 1)) * 16;
  u32 adw = O_WO + (4 * j4 + q4) * 1024 + (((16 * w + (p4 >> 1)) ^ (((4 * j4 + q4) & 7) << 1)) * 16) + (p4 & 1) * 8;
  u32 a_sp = WS_SB + ((w * 16 + (l >> 2)) * 256 + me) * 64 + (l & 3) * 16;
  u32 off0 = WS_SB + ((b & 63) * 256 + 64 * w + (l >> 2)) * 64 + (l & 3) * 16;
  asm volatile("" : "+v"(qoff), "+v"(adk), "+v"(a_qs), "+v"(a_qw), "+v"(a_pw), "+v"(a_pr), "+v"(a_mw), "+v"(a_mr), "+v"(ad0), "+v"(ad1));
  asm volatile("" : "+v"(a_op), "+v"(a_st), "+v"(ooff), "+v"(adw), "+v"(a_sp), "+v"(off0));
  const u32 a_fq = lb + O_FL + 4 * F_Q, a_fp = lb + O_FL + 4 * F_P;
  u32x4 kf[8];
  {
    wait_until(tpub + Q_D);
    u32x4 qa = ldsc(rws, qoff), qb = ldsc(rws, qoff + 2048);
    wait_landed(S, seen, N_W1 + 8 * (w + 1));
#pragma unroll
    for (int i = 0; i < 8; ++i) kf[i] = lds_ld<u32x4>(S, adk + (i >> 2) * 4096 + (((4 * (i & 3) + j4) ^ n16) * 16));
#pragma unroll 1
    for (int spin = 0; spin < SPIN_CAP && !__all(tok(qa, tag) & tok(qb, tag)); ++spin) { MEMCLOB(); qa = ldsc(rws, qoff); qb = ldsc(rws, qoff + 2048); }
    const float q0 = x32_sum(lo16(qa.x) + lo16(qb.x)), q1 = x32_sum(hi16(qa.x) + hi16(qb.x)), q2 = x32_sum(lo16(qa.z) + lo16(qb.z)), q3 = x32_sum(hi16(qa.z) + hi16(qb.z));
    if (l < 32) lds_st<u32x2>(S, a_qw, u32x2{pk(q0, q1), pk(q2, q3)});
    if (l == 0) set_flag(S, F_Q + w, 1);
  }
  {
    u32x4 fl4, bq0, bq1, bq2, bq3;
#pragma unroll 1
    for (int spin = 0; spin < LSPIN; ++spin) {
      asm volatile("ds_read_b128 %0, %5\n\tds_read_b128 %1, %6\n\tds_read_b128 %2, %6 offset:64\n\tds_read_b128 %3, %6 offset:128\n\tds_read_b128 %4, %6 offset:192\n\ts_waitcnt lgkmcnt(0)"
                   : "=&v"(fl4), "=&v"(bq0), "=&v"(bq1), "=&v"(bq2), "=&v"(bq3) : "v"(a_fq), "v"(a_qs) : "memory");
      if (all4(fl4)) break;
    }
    f32x4 d0 = mfma32(kf[0], bq0, f32x4{0.f, 0.f, 0.f, 0.f}), d1 = mfma32(kf[4], bq0, f32x4{0.f, 0.f, 0.f, 0.f});
    d0 = mfma32(kf[1], bq1, d0); d1 = mfma32(kf[5], bq1, d1);
    d0 = mfma32(kf[2], bq2, d0); d1 = mfma32(kf[6], bq2, d1);
    d0 = mfma32(kf[3], bq3, d0); d1 = mfma32(kf[7], bq3, d1);
    constexpr float C2 = 0.08838834764831845f * 1.4426950408889634f;
    float sc[8];
#pragma unroll
    for (int i = 0; i < 4; ++i) { sc[i] = d0[i] * C2; sc[4 + i] = d1[i] * C2; }
    const float mw = xr_max(fmaxf(fmaxf(fmaxf(sc[0], sc[1]), fmaxf(sc[2], sc[3])), fmaxf(fmaxf(sc[4], sc[5]), fmaxf(sc[6], sc[7]))));
    float p[8];
#pragma unroll
    for (int i = 0; i < 8; ++i) p[i] = ex2(sc[i] - mw);
    if (n16 < 4) {
      lds_st<u32x2>(S, a_pw, u32x2{pk(p[0], p[1]), pk(p[2], p[3])});
      lds_st<u32x2>(S, a_pw + 32, u32x2{pk(p[4], p[5]), pk(p[6], p[7])});
      if (l < 4) lds_st<float>(S, a_mw, mw);
    }
    if (l == 0) set_flag(S, F_P + w, 1);
  }
  {
    u32x4 fl4, ap[4], m4u;
#pragma unroll 1
    for (int spin = 0; spin < LSPIN; ++spin) {
      asm volatile("ds_read_b128 %0, %6\n\tds_read_b128 %1, %7\n\tds_read_b128 %2, %7 offset:64\n\tds_read_b128 %3, %7 offset:128\n\tds_read_b128 %4, %7 offset:192\n\tds_read_b128 %5, %8\n\ts_waitcnt lgkmcnt(0)"
                   : "=&v"(fl4), "=&v"(ap[0]), "=&v"(ap[1]), "=&v"(ap[2]), "=&v"(ap[3]), "=&v"(m4u) : "v"(a_fp), "v"(a_pr), "v"(a_mr) : "memory");
      if (all4(fl4)) break;
    }
    const float m0 = f_of(m4u.x), m1 = f_of(m4u.y), m2 = f_of(m4u.z), m3 = f_of(m4u.w);
    const float M = vmax(vmax3(m0, m1, m2), m3);
    const float al[4] = {ex2(m0 - M), ex2(m1 - M), ex2(m2 - M), ex2(m3 - M)};
    const u32x4 ones = {0x3f803f80u, 0x3f803f80u, 0x3f803f80u, 0x3f803f80u};
    float L = 0;
#pragma unroll
    for (int ks = 0; ks < 4; ++ks) L = fmaf(al[ks], mfma32(ap[ks], ones, f32x4{0.f, 0.f, 0.f, 0.f})[0], L);
    stsc(rws, a_st, u32x4{u_of(M), tag, u_of(L), tag});
    float o0 = 0, o1 = 0;
    wait_lazy(S, F_LANDED, N_K + 16);
    wait_landed(S, seen, N_K + 24);
    const bool early = seen >= ST_AT;
    if (early && w == 0 && l < 32) arrive(ws + WS_HB + (g * 32 + l) * 128, 1u << s);
    {
      u32x2 vf[12];
#pragma unroll
      for (int i = 0; i < 12; ++i) vf[i] = lds_tr(S, ((i & 2) ? ad1 : ad0) + (i >> 2) * 8192 + (i & 1) * 1024);
#pragma unroll
      for (int ks = 0; ks < 3; ++ks) {
        o0 = fmaf(al[ks], mfma32(ap[ks], u32x4{vf[4 * ks].x, vf[4 * ks].y, vf[4 * ks + 1].x, vf[4 * ks + 1].y}, f32x4{0.f, 0.f, 0.f, 0.f})[0], o0);
        o1 = fmaf(al[ks], mfma32(ap[ks], u32x4{vf[4 * ks + 2].x, vf[4 * ks + 2].y, vf[4 * ks + 3].x, vf[4 * ks + 3].y}, f32x4{0.f, 0.f, 0.f, 0.f})[0], o1);
      }
    }
    if (!early) {
      wait_landed(S, seen, ST_AT);
      if (w == 0 && l < 32) arrive(ws + WS_HB + (g * 32 + l) * 128, 1u << s);
    }
    wait_landed(S, seen, N_V);
    {
      const u32x2 a0 = lds_tr(S, ad0 + 3 * 8192), a1 = lds_tr(S, ad0 + 3 * 8192 + 1024), c0 = lds_tr(S, ad1 + 3 * 8192), c1 = lds_tr(S, ad1 + 3 * 8192 + 1024);
      o0 = fmaf(al[3], mfma32(ap[3], u32x4{a0.x, a0.y, a1.x, a1.y}, f32x4{0.f, 0.f, 0.f, 0.f})[0], o0);
      o1 = fmaf(al[3], mfma32(ap[3], u32x4{c0.x, c0.y, c1.x, c1.y}, f32x4{0.f, 0.f, 0.f, 0.f})[0], o1);
    }
    stsc(rws, a_op, u32x4{u_of(o0), tag, u_of(o1), tag});
  }
  {
    wait_ge(S, F_OOK, 1);
    u32x4 ov = ldsc(rws, ooff);
    wait_landed(S, seen, N_WO);
    u32x2 bw[8];
#pragma unroll
    for (int tt = 0; tt < 8; ++tt) bw[tt] = lds_tr(S, adw ^ (tt << 5));
    u32 bp = 0;
    if (hasM) { wait_ge(S, F_B + w, 1); bp = lds_ld<u32>(S, O_BP + w * 256 + l * 4); }
    wait_ge(S, F_WOK, 1);
    const float wn = lds_ld<float>(S, O_WT + (l >> 1) * 4);
#pragma unroll 1
    for (int spin = 0; spin < SPIN_CAP && !__all(tok(ov, tag)); ++spin) { MEMCLOB(); ov = ldsc(rws, ooff); }
    if (t == 0) set_flag(S, F_OG, 1);
    float o0 = wn * f_of(ov.x), o1 = wn * f_of(ov.z);
    o0 += dpp<XOR2>(o0); o1 += dpp<XOR2>(o1); o0 += dpp<ROR4>(o0); o1 += dpp<ROR4>(o1); o0 += dpp<ROR8>(o0); o1 += dpp<ROR8>(o1);
    o0 = x16_sum(o0); o1 = x16_sum(o1); o0 = x32_sum(o0); o1 = x32_sum(o1);
    const u32 pr = pk(o0, o1), po = __builtin_amdgcn_mov_dpp(pr, XOR1, 0xf, 0xf, true);
    if (l == 0) { MEMCLOB(); *(volatile LAS u32x4*)(S + O_AV + w * 16) = u32x4{pr, po, 1u, 1u}; MEMCLOB(); }
    u32x4 av;
#pragma unroll 1
    for (int spin = 0; spin < LSPIN; ++spin) { av = *(volatile LAS u32x4*)(S + O_AV + j4 * 16); if (__all(av.z != 0)) break; }
    const u32x2 a = {av.x, av.y};
    f32x4 d[8];
#pragma unroll
    for (int tt = 0; tt < 8; ++tt) d[tt] = mfma16(a, bw[tt], f32x4{0.f, 0.f, 0.f, 0.f});
    const bool b0 = j4 & 1, b1_ = j4 & 2;
    const float e0 = b1_ ? (b0 ? d[6][0] : d[4][0]) : (b0 ? d[2][0] : d[0][0]), e1 = b1_ ? (b0 ? d[7][0] : d[5][0]) : (b0 ? d[3][0] : d[1][0]);
    stsc(rws, a_sp, u32x4{pk(e0, e1), tag, bp, tag});
  }
  if (b < NC) {
    const float b2f = f_of(b2h << 16), xff = f_of(xfh << 16);
    u32 a_fw = O_FIN + (l & 3) * 64 + w * 4;
    u32 a_ff = lb + O_FL + 4 * F_F, a_fr = lb + O_FIN + (l & 3) * 64 + ((l >> 2) & 1) * 16;
    asm volatile("" : "+v"(a_fw), "+v"(a_ff), "+v"(a_fr));
    wait_ge(S, F_FOK, 1);
    u32x4 v[4];
#pragma unroll
    for (int i = 0; i < 4; ++i) v[i] = ldsc(rws, off0 + i * 1024);
#pragma unroll 1
    for (int spin = 0; spin < SPIN_CAP && !__all(tok(v[0], tag) & tok(v[1], tag) & tok(v[2], tag) & tok(v[3], tag)); ++spin) {
      MEMCLOB();
#pragma unroll
      for (int i = 0; i < 4; ++i) v[i] = ldsc(rws, off0 + i * 1024);
    }
    float r[4];
    r[0] = (lo16(v[0].x) + lo16(v[1].x)) + (lo16(v[2].x) + lo16(v[3].x)); r[1] = (hi16(v[0].x) + hi16(v[1].x)) + (hi16(v[2].x) + hi16(v[3].x));
    r[2] = (lo16(v[0].z) + lo16(v[1].z)) + (lo16(v[2].z) + lo16(v[3].z)); r[3] = (hi16(v[0].z) + hi16(v[1].z)) + (hi16(v[2].z) + hi16(v[3].z));
#pragma unroll
    for (int i = 0; i < 4; ++i) r[i] += dpp<ROR4>(r[i]);
#pragma unroll
    for (int i = 0; i < 4; ++i) r[i] += dpp<ROR8>(r[i]);
#pragma unroll
    for (int i = 0; i < 4; ++i) r[i] = x16_sum(r[i]);
#pragma unroll
    for (int i = 0; i < 4; ++i) r[i] = x32_sum(r[i]);
    if (l < 4) {
#pragma unroll
      for (int i = 0; i < 4; ++i) lds_st<float>(S, a_fw + i * 16, r[i]);
    }
    if (l == 0) set_flag(S, F_F + w, 1);
    if (w == 0) {
      u32x4 fl4, s4, m4;
#pragma unroll 1
      for (int spin = 0; spin < LSPIN; ++spin) {
        asm volatile("ds_read_b128 %0, %3\n\tds_read_b128 %1, %4\n\tds_read_b128 %2, %4 offset:32\n\ts_waitcnt lgkmcnt(0)"
                     : "=&v"(fl4), "=&v"(s4), "=&v"(m4) : "v"(a_ff), "v"(a_fr) : "memory");
        if (all4(fl4)) break;
      }
      const float sv = (f_of(s4.x) + f_of(s4.y)) + (f_of(s4.z) + f_of(s4.w)), mv = (f_of(m4.x) + f_of(m4.y)) + (f_of(m4.z) + f_of(m4.w));
      const float B = rbf(rbf(mv) + b2f);
      const float gt = rbf(B * rcp(1.f + ex2(-B * 1.4426950408889634f)));
      const float o = xff + rbf(rbf(sv) * gt);
      if (l < 8) out[fcol] = (u16)pk(o, o);
      if (b == 0 && l == 0) __builtin_amdgcn_raw_buffer_store_b32(epoch + 1u, rws, WS_EPOCH, 0, AUX_SC1);
    }
  }
  __builtin_amdgcn_endpgm();
  }

  __builtin_amdgcn_s_setprio(3);
  const u32 fl = (u32)(size_t)(S + O_FL + 4 * F_LANDED);
  asm volatile("" :: "s"(kc), "s"(vc), "s"(w1), "s"(w2), "s"(wo), "s"(x), "s"(ws), "s"(b1), "s"(b2));
  {
    __builtin_amdgcn_raw_ptr_buffer_load_lds(mk(x, 1024), (LAS void*)(S + O_X), 16, l * 16, 0, 0, 0);
    const u32 f0 = g * 256 + (s >> 2) * 32 + (s & 3) * 4, c0 = ((b & 63) >> 4) * 128 + ((b >> 2) & 3) * 32 + (b & 3) * 4;
    const char* p = (const char*)ws;
    if (l == 1 || l == 2) p = (const char*)b1 + (((hasM ? f0 : 0) + (l - 1) * 16) * 2 & ~15u);
    if (l == 3 || l == 4) p = (const char*)b2 + ((c0 + (l - 3) * 16) * 2 & ~15u);
    __builtin_amdgcn_global_load_lds((const __attribute__((address_space(1))) void*)p, (LAS void*)(S + O_MISC), 16, 0, 0);
  }
  {
    const rsrc_t r1 = mk(w1, hasM ? 512 * FF * 2 : 0);
    const u32 hb = l >> 5, bl = hb * (FF * 2) + (((l & 31) ^ (hb << 1)) * 16);
    const u32 base = (s * 16) * (FF * 2) + g * 512;
#pragma unroll 1
    for (int G = 0; G < 2; ++G) dma4(r1, S, O_W1 + G * 4096, bl, (bl ^ 64) + 6144, (bl ^ 128) + 2 * 6144, (bl ^ 192) + 3 * 6144, base + G * 8 * (FF * 2));
  }
  const size_t kvoff = ((size_t)g * SK + (SK - WIN) + s * 128) * 256;
  {
    const rsrc_t rk = mk((const char*)kc + kvoff, 32768);
    const u32 v0 = j4 * 256 + ((n16 ^ j4) * 16);
    wait_until(tstart + K_T);
#pragma unroll 1
    for (int G = 0; G < 8; ++G) { landed<WINDOW - 4>(fl, N_W1 + 4 * G - (WINDOW - 4)); dma4(rk, S, O_K + G * 4096, v0, v0 ^ 64, v0 ^ 128, v0 ^ 192, G * 4096); }
  }
  {
    const rsrc_t rv = mk((const char*)vc + kvoff, 32768);
    const u32 v0 = j4 * 256 + ((n16 ^ (j4 << 1)) * 16);
#pragma unroll 1
    for (int G = 0; G < 8; ++G) { landed<WINDOW - 4>(fl, N_K + 4 * G - (WINDOW - 4)); dma4(rv, S, O_V + G * 4096, v0, v0, v0 ^ 128, v0 ^ 128, G * 4096); }
  }
  {
    const rsrc_t r2 = mk((const char*)w2 - 1024, hasM ? 512 * FF * 2 + 1024 : 0);
    const u32 f0 = g * 256 + (s >> 2) * 32 + (s & 3) * 4;
    const rsrc_t ro = mk((const char*)wo - 1024, 4096 * 512 * 2 + 1024);
    const u32 r0 = g * 512 + (s >> 3) * 128 + ((s >> 1) & 3) * 32 + (s & 1) * 8;
#pragma unroll 1
    for (int k = 0; k < 6; ++k) {
      landed<WINDOW - 4>(fl, N_V + 4 * k - (WINDOW - 4));
      if (k < 2) {
        const u32 v = (l * 16) ^ (k << 7);
        dma4(r2, S, O_W2 + k * 4096, v + 1024, (v ^ 32) + 16384, v ^ 64, (v ^ 96) + 15360, (f0 + 2 * k) * 1024);
      } else {
        const int G = k - 2;
        const u32 v = (l * 16) ^ ((G & 1) << 7);
        dma4(ro, S, O_WO + G * 4096, v + 1024, (v ^ 32) + 16384, v ^ 64, (v ^ 96) + 15360, (r0 + 2 * G) * 1024);
      }
    }
  }
  drain<WINDOW - 4>(fl, N_WO);
}

extern "C" void kernel_launch(void* const* d_in, const int* in_sizes, int n_in, void* d_out, int out_size, void* d_ws, size_t ws_size, hipStream_t stream) {
  fused_decode<<<NWG, NT, 0, stream>>>((const u16*)d_in[0], (const u16*)d_in[1], (const u16*)d_in[2], (const u16*)d_in[3], (const u16*)d_in[4],
                                       (const u16*)d_in[6], (const u16*)d_in[7], (const u16*)d_in[8], (const u16*)d_in[9], (u16*)d_out, (unsigned char*)d_ws);
}
```

```cpp
#include <hip/hip_runtime.h>
#include <stdint.h>

typedef unsigned short u16;
typedef unsigned u32;
typedef unsigned long long u64;
typedef u32 u32x4 __attribute__((ext_vector_type(4)));
typedef u32 u32x2 __attribute__((ext_vector_type(2)));
typedef float f32x4 __attribute__((ext_vector_type(4)));
typedef float f32x2 __attribute__((ext_vector_type(2)));
typedef short s16x4 __attribute__((ext_vector_type(4)));
typedef __bf16 bf16x8 __attribute__((ext_vector_type(8)));
typedef __bf16 bf16x2 __attribute__((ext_vector_type(2)));
typedef __amdgpu_buffer_rsrc_t rsrc_t;
#define LAS __attribute__((address_space(3)))

constexpr int SK = 65536, WIN = 4096, FF = 1792;
constexpr int NWG = 256, NT = 640;
constexpr int NC = 64;
constexpr u32 WS_EPOCH = 0;
constexpr u32 WS_QP = 4096;
constexpr u32 WS_HB = WS_QP + 8 * 4 * 256 * 8;
constexpr u32 WS_ST = WS_HB + 256 * 128;
constexpr u32 WS_OP = WS_ST + 256 * 2048;
constexpr u32 WS_HP = WS_OP + 256 * 32 * 128;
constexpr u32 WS_SB = WS_HP + 224 * 32 * 64;
constexpr u32 WS_END = WS_SB + NC * 256 * 64;
constexpr u32 OOB = 0x80000000u;
constexpr u32 O_WQ = 0, O_K = 16384, O_V = 49152, O_W1 = 81920, O_W2 = 90112, O_WO = 98304, O_S = 114688;
constexpr u32 O_X = O_S, O_MISC = O_X + 1024, O_S2 = O_MISC + 1024;
constexpr u32 O_XN = O_S2, O_Y = O_XN + 1024, O_QS = O_Y + 1024, O_P = O_QS + 1024, O_MW = O_P + 1024, O_AV = O_MW + 128,
              O_EP = O_AV + 64, O_WT = O_EP + 16, O_HV = O_WT + 128, O_HX = O_HV + 64, O_FIN = O_HX + 128, O_BP = O_FIN + 512, O_FL = O_BP + 1024, LDS_BYTES = O_FL + 256;
enum { F_LANDED = 0, F_OOK = 2, F_FOK = 4, F_WOK = 5, F_OG = 6, F_XN = 7, F_Y = 8, F_Q = 12, F_P = 16, F_WQ = 20, F_F = 24, F_B = 28, F_H = 32 };
constexpr int N_X = 2, N_W1 = N_X + 8, N_K = N_W1 + 32, N_V = N_K + 32, N_W2 = N_V + 8, N_WO = N_W2 + 16;
constexpr int WINDOW = 24;
constexpr int ST_AT = N_V - 4;
constexpr int K_T = 100;
constexpr int Q_D = 80;
constexpr int H_D = 100;
constexpr int CODE_BYTES = 12000;
constexpr int AUX_NT = 2, AUX_SC1 = 16;
constexpr int LSPIN = 1 << 22, SPIN_CAP = 1 << 16;

#define MEMCLOB() asm volatile("" ::: "memory")

__device__ __forceinline__ rsrc_t mk(const void* p, u32 bytes) { return __builtin_amdgcn_make_buffer_rsrc((void*)p, 0, bytes, 0x00020000); }
__device__ __forceinline__ u32x4 ldsc(rsrc_t r, u32 voff) { return __builtin_amdgcn_raw_buffer_load_b128(r, voff, 0, AUX_SC1); }
__device__ __forceinline__ void stsc(rsrc_t r, u32 voff, u32x4 v) { __builtin_amdgcn_raw_buffer_store_b128(v, r, voff, 0, AUX_SC1); }
__device__ __forceinline__ void stsc2(rsrc_t r, u32 voff, u32x2 v) { __builtin_amdgcn_raw_buffer_store_b64(v, r, voff, 0, AUX_SC1); }
__device__ __forceinline__ float f_of(u32 u) { return __builtin_bit_cast(float, u); }
__device__ __forceinline__ u32 u_of(float f) { return __builtin_bit_cast(u32, f); }
__device__ __forceinline__ float lo16(u32 w) { return f_of(w << 16); }
__device__ __forceinline__ float hi16(u32 w) { return f_of(w & 0xffff0000u); }
__device__ __forceinline__ u32 pk(float lo, float hi) { bf16x2 r = {(__bf16)lo, (__bf16)hi}; return __builtin_bit_cast(u32, r); }
__device__ __forceinline__ float rbf(float f) { return lo16(pk(f, f)); }
template <int CTRL> __device__ __forceinline__ float dpp(float x) { return f_of(__builtin_amdgcn_mov_dpp(u_of(x), CTRL, 0xf, 0xf, true)); }
constexpr int XOR1 = 0xB1, XOR2 = 0x4E, HMIR = 0x141, MIR = 0x140, ROR4 = 0x124, ROR8 = 0x128;
__device__ __forceinline__ float x16_sum(float x) { auto s = __builtin_amdgcn_permlane16_swap(u_of(x), u_of(x), false, false); return f_of(s[0]) + f_of(s[1]); }
__device__ __forceinline__ float x32_sum(float x) { auto s = __builtin_amdgcn_permlane32_swap(u_of(x), u_of(x), false, false); return f_of(s[0]) + f_of(s[1]); }
__device__ __forceinline__ float row_sum(float x) { x += dpp<XOR1>(x); x += dpp<XOR2>(x); x += dpp<HMIR>(x); x += dpp<MIR>(x); return x; }
__device__ __forceinline__ float wave_sum(float x) { return x32_sum(x16_sum(row_sum(x))); }
__device__ __forceinline__ float vmax(float a, float b) { float r; asm("v_max_f32 %0, %1, %2" : "=v"(r) : "v"(a), "v"(b)); return r; }
__device__ __forceinline__ float vmax3(float a, float b, float c) { float r; asm("v_max3_f32 %0, %1, %2, %3" : "=v"(r) : "v"(a), "v"(b), "v"(c)); return r; }
#define DPPMAX(x, ctrl) asm("s_nop 1\n\tv_max_f32_dpp %0, %0, %0 " ctrl " row_mask:0xf bank_mask:0xf" : "+v"(x))
__device__ __forceinline__ float xr_max(float x) {
  auto s = __builtin_amdgcn_permlane16_swap(u_of(x), u_of(x), false, false); x = vmax(f_of(s[0]), f_of(s[1]));
  auto t = __builtin_amdgcn_permlane32_swap(u_of(x), u_of(x), false, false); return vmax(f_of(t[0]), f_of(t[1]));
}
__device__ __forceinline__ float ex2(float x) { return __builtin_amdgcn_exp2f(x); }
__device__ __forceinline__ float rcp(float x) { return __builtin_amdgcn_rcpf(x); }
__device__ __forceinline__ f32x4 mfma32(u32x4 a, u32x4 b, f32x4 c) {
  return __builtin_amdgcn_mfma_f32_16x16x32_bf16(__builtin_bit_cast(bf16x8, a), __builtin_bit_cast(bf16x8, b), c, 0, 0, 0);
}
__device__ __forceinline__ f32x4 mfma16(u32x2 a, u32x2 b, f32x4 c) {
  return __builtin_amdgcn_mfma_f32_16x16x16bf16_1k(__builtin_bit_cast(s16x4, a), __builtin_bit_cast(s16x4, b), c, 0, 0, 0);
}
typedef LAS unsigned char* lds_t;
template <class T> __device__ __forceinline__ T lds_ld(lds_t s, u32 off) { return *(const LAS T*)(s + off); }
template <class T> __device__ __forceinline__ void lds_st(lds_t s, u32 off, T v) { *(LAS T*)(s + off) = v; }
__device__ __forceinline__ u32x2 lds_tr(lds_t s, u32 off) { return __builtin_bit_cast(u32x2, __builtin_amdgcn_ds_read_tr16_b64_v4i16((LAS s16x4*)(s + off))); }
__device__ __forceinline__ void set_flag(lds_t s, int f, u32 v) { MEMCLOB(); *(volatile LAS u32*)(s + O_FL + 4 * f) = v; MEMCLOB(); }
__device__ __forceinline__ void wait_ge(lds_t s, int f, int need) {
#pragma unroll 1
  for (int spin = 0; spin < LSPIN; ++spin) {
    const int v = __builtin_amdgcn_readfirstlane(*(volatile LAS int*)(s + O_FL + 4 * f));
    if (v >= need) break;
  }
  MEMCLOB();
}
__device__ __forceinline__ void wait_lazy(lds_t s, int f, int need) {
#pragma unroll 1
  for (int spin = 0; spin < LSPIN; ++spin) {
    const int v = __builtin_amdgcn_readfirstlane(*(volatile LAS int*)(s + O_FL + 4 * f));
    if (v >= need) break;
    __builtin_amdgcn_s_sleep(6);
  }
  MEMCLOB();
}
__device__ __forceinline__ void wait_landed(lds_t s, int& seen, int need) {
  if (seen < need) {
#pragma unroll 1
    for (int spin = 0; spin < LSPIN; ++spin) {
      seen = __builtin_amdgcn_readfirstlane(*(volatile LAS int*)(s + O_FL + 4 * F_LANDED));
      if (seen >= need) break;
      }
  }
  MEMCLOB();
}
__device__ __forceinline__ bool all4(u32x4 f) { return __builtin_amdgcn_readfirstlane(f.x & f.y & f.z & f.w) != 0; }
__device__ __forceinline__ void wait4(lds_t s, int f) {
#pragma unroll 1
  for (int spin = 0; spin < LSPIN; ++spin) {
    const u32x4 v = *(volatile LAS u32x4*)(s + O_FL + 4 * f);
    if (__builtin_amdgcn_readfirstlane(v.x & v.y & v.z & v.w)) break;
  }
  MEMCLOB();
}
__device__ __forceinline__ u32x4 ln8(u32x4 xr) {
  float v[8];
#pragma unroll
  for (int i = 0; i < 4; ++i) { v[2 * i] = lo16(xr[i]); v[2 * i + 1] = hi16(xr[i]); }
  const float s = ((v[0] + v[1]) + (v[2] + v[3])) + ((v[4] + v[5]) + (v[6] + v[7]));
  const float m = wave_sum(s) * (1.f / 512);
  float q0 = 0, q1 = 0;
#pragma unroll
  for (int i = 0; i < 4; ++i) { const float d0 = v[2 * i] - m, d1 = v[2 * i + 1] - m; q0 = fmaf(d0, d0, q0); q1 = fmaf(d1, d1, q1); }
  const float var = wave_sum(q0 + q1) * (1.f / 512);
  const float mb = rbf(m);
  const float inv = rcp(rbf(__builtin_amdgcn_sqrtf(rbf(rbf(var) + 1e-5f))));
  u32x4 r;
#pragma unroll
  for (int i = 0; i < 4; ++i) { const u32 d = pk(v[2 * i] - mb, v[2 * i + 1] - mb); r[i] = pk(lo16(d) * inv, hi16(d) * inv); }
  return r;
}
__device__ __forceinline__ void poll_word(const unsigned char* p, u32 tag) {
  int n;
  asm volatile(
    "v_mov_b32 v108, 0\n\tv_mov_b32 v109, 0\n\t"
    "s_mov_b64 exec, 1\n\t"
    "global_atomic_or_x2 v[104:105], %1, v[108:109], off sc0 sc1\n\t"
    "s_sleep 4\n\t"
    "global_atomic_or_x2 v[106:107], %1, v[108:109], off sc0 sc1\n\t"
    "s_mov_b32 %0, 0\n"
    "1:\n\t"
    "s_waitcnt vmcnt(1)\n\t"
    "v_not_b32 v104, v104\n\tv_xor_b32 v105, %2, v105\n\tv_or_b32 v104, v104, v105\n\t"
    "v_cmp_ne_u32 vcc, 0, v104\n\t"
    "s_cbranch_vccz 2f\n\t"
    "global_atomic_or_x2 v[104:105], %1, v[108:109], off sc0 sc1\n\t"
    "s_waitcnt vmcnt(1)\n\t"
    "v_not_b32 v106, v106\n\tv_xor_b32 v107, %2, v107\n\tv_or_b32 v106, v106, v107\n\t"
    "v_cmp_ne_u32 vcc, 0, v106\n\t"
    "s_cbranch_vccz 2f\n\t"
    "global_atomic_or_x2 v[106:107], %1, v[108:109], off sc0 sc1\n\t"
    "s_add_u32 %0, %0, 1\n\t"
    "s_cmp_lt_u32 %0, %3\n\t"
    "s_cbranch_scc1 1b\n"
    "2:\n\t"
    "s_mov_b64 exec, -1"
    : "=&s"(n) : "v"(p), "v"(tag), "s"(SPIN_CAP) : "vcc", "scc", "v104", "v105", "v106", "v107", "v108", "v109", "memory");
}
__device__ __forceinline__ void arrive(unsigned char* p, u32 bit) {
  __hip_atomic_fetch_or((u64*)p, (u64)bit, __ATOMIC_RELAXED, __HIP_MEMORY_SCOPE_AGENT);
}
__device__ __forceinline__ void wait_until(u32 t) { while ((int)((u32)__builtin_amdgcn_s_memrealtime() - t) < 0) __builtin_amdgcn_s_sleep(3); }
__device__ __forceinline__ void dma4(rsrc_t r, lds_t s, u32 ldsoff, u32 v0, u32 v1, u32 v2, u32 v3, u32 soff) {
  __builtin_amdgcn_raw_ptr_buffer_load_lds(r, (LAS void*)(s + ldsoff), 16, v0, soff, 0, AUX_NT);
  __builtin_amdgcn_raw_ptr_buffer_load_lds(r, (LAS void*)(s + ldsoff), 16, v1, soff, 1024, AUX_NT);
  __builtin_amdgcn_raw_ptr_buffer_load_lds(r, (LAS void*)(s + ldsoff), 16, v2, soff, 2048, AUX_NT);
  __builtin_amdgcn_raw_ptr_buffer_load_lds(r, (LAS void*)(s + ldsoff), 16, v3, soff, 3072, AUX_NT);
}
template <int N> __device__ __forceinline__ void landed(u32 flag_addr, int v) {
  asm volatile("s_waitcnt vmcnt(%0)\n\tds_write_b32 %1, %2" :: "i"(N), "v"(flag_addr), "v"(v) : "memory");
}
template <int N> __device__ __forceinline__ void drain(u32 flag_addr, int total) {
  landed<N>(flag_addr, total - N);
  if constexpr (N >= 4) drain<N - 4>(flag_addr, total);
}
__device__ __forceinline__ u32 msk(bool c) { return c ? 0u : OOB; }
__device__ __forceinline__ bool tok(u32x4 v, u32 tag) { return (v.y == tag) & (v.w == tag); }
__device__ __forceinline__ u32 mktag(u32 epoch) { return ((epoch + 1u) * 0x9E3779B1u) ^ 0x5bd1e995u; }

__global__ void __launch_bounds__(NT) fused_decode(const u16* __restrict__ x, const u16* __restrict__ kc, const u16* __restrict__ vc,
                                                   const u16* __restrict__ wq, const u16* __restrict__ wo, const u16* __restrict__ w1,
                                                   const u16* __restrict__ b1, const u16* __restrict__ w2, const u16* __restrict__ b2,
                                                   u16* __restrict__ out, unsigned char* __restrict__ ws) {
  __shared__ __attribute__((aligned(16))) unsigned char smem[LDS_BYTES];
  lds_t S = (lds_t)smem;
  const u32 tstart = (u32)__builtin_amdgcn_s_memrealtime();
  const int t = threadIdx.x, l = t & 63, wv = __builtin_amdgcn_readfirstlane(t >> 6);
  if (wv == 0) {
    lds_st<u32>(S, O_FL + 4 * l, 0u); if (l < 16) lds_st<u32>(S, O_AV + 4 * l, 0u);
    asm volatile("s_waitcnt lgkmcnt(0)" ::: "memory");
  }
  asm volatile("s_load_dword s94, %0, 0x0\n\ts_load_dword s95, %0, 0x40" :: "s"(__builtin_amdgcn_kernarg_segment_ptr()) : "s94", "s95", "memory");
  const rsrc_t rcode = mk((const void*)(__builtin_amdgcn_s_getpc() & ~255ull), CODE_BYTES);
  {
    const u32 sel = (blockIdx.x >> 3) + wv;
    asm volatile("buffer_load_dwordx4 v[120:123], %0, %1, 0 offen\n\tbuffer_load_dwordx4 v[120:123], %2, %1, 0 offen"
                 :: "v"((l * 16 + (wv + 1) * 1024) | ((sel & 3) ? OOB : 0u)), "s"(rcode), "v"((l * 16 + (wv + 11) * 1024) | ((sel & 3) ? OOB : 0u))
                 : "v120", "v121", "v122", "v123", "memory");
  }
  asm volatile("s_barrier" ::: "memory");
  const int b = blockIdx.x, g = b & 7, s = b >> 3;
  const int n16 = l & 15, j4 = l >> 4, q4 = (l >> 2) & 3, p4 = l & 3;
  const bool hasM = g < 7;
  const int me = g * 32 + s;
  const rsrc_t rws = mk(ws, WS_END);
  if (wv < 4) {
    if (wv == 0) {
      const rsrc_t rx = mk(x, 1024);
      asm volatile("buffer_load_dwordx4 v[110:113], %0, %1, 0 offen\n\tbuffer_load_dword v114, off, %2, 0"
                   :: "v"(l * 16), "s"(rx), "s"(rws) : "v110", "v111", "v112", "v113", "v114", "memory");
    }
    {
      const rsrc_t rq = mk(wq, 512 * 4096 * 2);
      const u32 base = ((s & 3) * 128 + 32 * wv) * 8192 + (g * 512 + (s >> 2) * 64) * 2;
      const u32 v0 = (l >> 3) * 8192 + (((l & 7) ^ (((l >> 4) & 1) << 1)) * 16), v1 = (v0 ^ 64) + 64512;
      const u32 m = (u32)(size_t)(S + O_WQ) + wv * 4096;
      asm volatile("s_mov_b32 m0, %0\n\ts_nop 0\n\t"
                   "buffer_load_dwordx4 %1, %5, %6 offen nt lds\n\tbuffer_load_dwordx4 %2, %5, %6 offen offset:1024 nt lds\n\t"
                   "buffer_load_dwordx4 %3, %5, %6 offen offset:2048 nt lds\n\tbuffer_load_dwordx4 %4, %5, %6 offen offset:3072 nt lds"
                   :: "s"(m), "v"(v0), "v"(v1), "v"(v0 + 2 * 64512), "v"(v1 + 2 * 64512), "s"(rq), "s"(base) : "memory");
    }
  }

  if (wv != 8) {
  asm volatile("" :: "s"(out));

  if (wv == 9) {
    u32 soff = (WS_ST + (me * 32 + (l & 31)) * 64) | msk(l < 32);
    wait_ge(S, F_XN, 1);
    const u32 tag = mktag(lds_ld<u32>(S, O_EP));
    stsc(rws, (WS_HB + ((l & 4) ? (b & 63) : me) * 128 + ((l & 4) ? 64 : 0) + (l & 3) * 16) | msk(l < 4 || (l < 8 && b < NC)), u32x4{0u, tag, 0u, tag});
    wait_lazy(S, F_LANDED, N_V - 8);
    wait_ge(S, F_LANDED, N_V);
    poll_word(ws + WS_HB + me * 128, tag);
    set_flag(S, F_OOK, 1);
    {
      u32x4 sv = ldsc(rws, soff);
#pragma unroll 1
      for (int spin = 0; spin < SPIN_CAP && !__all(tok(sv, tag) | (l >= 32)); ++spin) { MEMCLOB(); sv = ldsc(rws, soff); }
      float mp = f_of(sv.x);
      float Mx = mp;
      DPPMAX(Mx, "quad_perm:[1,0,3,2]"); DPPMAX(Mx, "quad_perm:[2,3,0,1]"); DPPMAX(Mx, "row_half_mirror"); DPPMAX(Mx, "row_mirror");
      { auto r = __builtin_amdgcn_permlane16_swap(u_of(Mx), u_of(Mx), false, false); Mx = vmax(f_of(r[0]), f_of(r[1])); }
      const float wp = ex2(mp - Mx);
      const float wn = wp * rcp(x16_sum(row_sum(wp * f_of(sv.z))));
      if (l < 32) lds_st<float>(S, O_WT + l * 4, wn);
    }
    set_flag(S, F_WOK, 1);
    if ((s & 7) == 0) {
      wait_ge(S, F_OG, 1);
      arrive(ws + WS_HB + l * 128 + 64, 1u << (g * 4 + (s >> 3)));
    }
    if (b < NC) {
      poll_word(ws + WS_HB + b * 128 + 64, tag);
      set_flag(S, F_FOK, 1);
    }
    __builtin_amdgcn_endpgm();
  }

  if (wv >= 4 && !hasM) __builtin_amdgcn_endpgm();
  if (wv == 0) {
    u32x4 xr; u32 ep;
    asm volatile("s_waitcnt vmcnt(4)\n\tv_mov_b32 %0, v110\n\tv_mov_b32 %1, v111\n\tv_mov_b32 %2, v112\n\tv_mov_b32 %3, v113\n\tv_mov_b32 %4, v114"
                 : "=v"(xr.x), "=v"(xr.y), "=v"(xr.z), "=v"(xr.w), "=v"(ep) :: "memory");
    lds_st<u32x4>(S, O_XN + l * 16, ln8(xr));
    if (l == 0) { lds_st<u32>(S, O_EP, ep); set_flag(S, F_XN, 1); }
  }
  wait_ge(S, F_XN, 1);
  const u32 epoch = lds_ld<u32>(S, O_EP);

  if (wv >= 4) {
    const int w = wv - 4;
    const u32 f0 = g * 256 + (s >> 2) * 32 + (s & 3) * 4;
    if (w == 0) { lds_st<u32x4>(S, O_Y + l * 16, ln8(lds_ld<u32x4>(S, O_XN + l * 16))); if (l == 0) set_flag(S, F_Y, 1); }
    wait_ge(S, F_Y, 1);
    const u32x2 ay = lds_ld<u32x2>(S, O_Y + (s * 16 + 4 * j4) * 2);
    wait_ge(S, F_LANDED, N_X);
    const u32 b1v = lds_ld<u16>(S, O_MISC + 16 + ((l & 4) ? 16 : 0) + ((f0 * 2) & 15) + p4 * 2);
    const u32 tag = mktag(epoch);
    u32 a_hp = (WS_HP + ((g * 32 + (w * 2 + (j4 & 1)) * 4 + (n16 >> 2)) * 32 + s) * 64 + (n16 & 3) * 16) | msk(l < 32);
    u32 a_bp = O_BP + w * 256 + l * 4;
    u32 hoff = (WS_HP + (me * 32 + 8 * w + ((l >> 2) & 7)) * 64 + (l & 3) * 16) | msk(l < 32);
    u32 ad1 = O_W1 + (4 * j4 + q4) * 512 + (((8 * w + (p4 >> 1)) ^ (((4 * j4 + q4) & 7) << 1)) * 16) + (p4 & 1) * 8;
    u32 ad2 = O_W2 + ((4 * j4 + q4) & 7) * 1024 + (((16 * w + (p4 >> 1)) ^ (((4 * j4 + q4) & 7) << 1)) * 16) + (p4 & 1) * 8;
    float b1f = f_of(b1v << 16);
    asm volatile("" : "+v"(a_hp), "+v"(a_bp), "+v"(hoff), "+v"(ad1), "+v"(ad2), "+v"(b1f));
    wait_ge(S, F_LANDED, N_W1);
    {
      f32x4 d[4];
#pragma unroll
      for (int tt = 0; tt < 4; ++tt) d[tt] = mfma16(ay, lds_tr(S, ad1 ^ (tt << 5)), f32x4{0.f, 0.f, 0.f, 0.f});
      const bool up = j4 & 1;
      stsc(rws, a_hp, u32x4{u_of(up ? d[2][0] : d[0][0]), tag, u_of(up ? d[3][0] : d[1][0]), tag});
    }
    wait_until((u32)__builtin_amdgcn_s_memrealtime() + H_D);
    u32x4 h0 = ldsc(rws, hoff);
#pragma unroll 1
    for (int spin = 0; spin < SPIN_CAP && !__all(tok(h0, tag) | (l >= 32)); ++spin) { MEMCLOB(); h0 = ldsc(rws, hoff); }
    {
      float ha = f_of(h0.x), hb = f_of(h0.z);
      ha += dpp<ROR4>(ha); hb += dpp<ROR4>(hb); ha += dpp<ROR8>(ha); hb += dpp<ROR8>(hb);
      ha = x16_sum(ha); hb = x16_sum(hb);
      if (l < 4) { lds_st<float>(S, O_HX + l * 16 + w * 4, ha); lds_st<float>(S, O_HX + 64 + l * 16 + w * 4, hb); }
      if (l == 0) set_flag(S, F_H + w, 1);
      wait4(S, F_H);
      const f32x4 h4 = lds_ld<f32x4>(S, O_HX + ((l & 4) ? 64 : 0) + p4 * 16);
      hb = ha = (h4[0] + h4[1]) + (h4[2] + h4[3]);
      const float p = rbf(rbf((l & 4) ? hb : ha) + b1f);
      const float u = p * fmaf(0.044715f * 0.7978845608028654f * 2.8853900817779268f, p * p, 0.7978845608028654f * 2.8853900817779268f);
      const float h = p - p * rcp(ex2(u) + 1.f);
      if (l < 8) lds_st<u16>(S, O_HV + w * 16 + (p4 * 2 + (l >> 2)) * 2, (u16)pk(h, h));
    }
    u32x2 ah = lds_ld<u32x2>(S, O_HV + w * 16 + (j4 & 1) * 8);
    if (j4 >= 2) ah = u32x2{0u, 0u};
    wait_lazy(S, F_LANDED, N_V - 4);
    wait_ge(S, F_LANDED, N_W2);
    {
      f32x4 d[8];
#pragma unroll
      for (int tt = 0; tt < 8; ++tt) d[tt] = mfma16(ah, lds_tr(S, ad2 ^ (tt << 5)), f32x4{0.f, 0.f, 0.f, 0.f});
      const bool b0 = j4 & 1, b1_ = j4 & 2;
      const float e0 = b1_ ? (b0 ? d[6][0] : d[4][0]) : (b0 ? d[2][0] : d[0][0]), e1 = b1_ ? (b0 ? d[7][0] : d[5][0]) : (b0 ? d[3][0] : d[1][0]);
      lds_st<u32>(S, a_bp, pk(e0, e1));
      if (l == 0) set_flag(S, F_B + w, 1);
    }
    __builtin_amdgcn_endpgm();
  }

  const int w = wv;
  int seen = -100;
  const int fcol = (b >> 4) * 128 + ((b >> 2) & 3) * 32 + (b & 3) * 4 + p4 + ((l >> 2) & 1) * 16;
  const u32 tag = mktag(epoch);
  const u32 lb = (u32)(size_t)S;
  {
    u32x4 ax[4];
#pragma unroll
    for (int ks = 0; ks < 4; ++ks) ax[ks] = lds_ld<u32x4>(S, O_XN + ((s & 3) * 128 + 32 * ks + 8 * j4) * 2);
    const u32 gq = (q4 >> 1) | ((j4 & 1) << 1);
    u32 ad = O_WQ + (8 * j4 + q4) * 128 + (((2 * w + (p4 >> 1)) ^ (gq << 1)) * 16) + (p4 & 1) * 8;
    u32 a_qp = (WS_QP + ((g * 4 + (s & 3)) * 256 + (s >> 2) * 32 + w * 8 + (n16 >> 1)) * 8) | msk(l < 16 && (l & 1) == 0);
    asm volatile("" : "+v"(ad), "+v"(a_qp), "+v"(ax[0]), "+v"(ax[1]), "+v"(ax[2]), "+v"(ax[3]));
    asm volatile("s_waitcnt vmcnt(0)" ::: "memory");
    if (l == 0) set_flag(S, F_WQ + w, 1);
    wait4(S, F_WQ);
    u32x2 bb[8];
#pragma unroll
    for (int i = 0; i < 8; ++i) bb[i] = lds_tr(S, ad + (i >> 1) * 4096 + (i & 1) * 512);
    f32x4 d = {0.f, 0.f, 0.f, 0.f};
#pragma unroll
    for (int ks = 0; ks < 4; ++ks) d = mfma32(ax[ks], u32x4{bb[2 * ks].x, bb[2 * ks].y, bb[2 * ks + 1].x, bb[2 * ks + 1].y}, d);
    stsc2(rws, a_qp, u32x2{pk(d[0], dpp<XOR1>(d[0])), tag});
  }
  const u32 tpub = (u32)__builtin_amdgcn_s_memrealtime();
  wait_landed(S, seen, N_X);
  const u32 b2h = lds_ld<u16>(S, O_MISC + 48 + ((l & 4) ? 16 : 0) + (((b & 3) * 8) & 15) + p4 * 2);
  const u32 xfh = lds_ld<u16>(S, O_X + (fcol & 511) * 2);
  u32 qoff = WS_QP + ((g * 4 + 2 * (l >> 5)) * 256 + w * 64 + 2 * (l & 31)) * 8;
  u32 adk = O_K + (32 * w + n16) * 256;
  u32 a_qs = lb + O_QS + (n16 & 3) * 256 + j4 * 16, a_qw = O_QS + w * 256 + (l & 31) * 8;
  u32 a_pw = O_P + (n16 & 3) * 256 + (32 * w + 4 * j4) * 2, a_pr = lb + O_P + (n16 >> 2) * 256 + j4 * 16;
  u32 a_mw = O_MW + (l & 3) * 16 + w * 4, a_mr = lb + O_MW + j4 * 16;
  u32 ad0 = O_V + (8 * j4 + q4) * 256 + (((4 * w + (p4 >> 1)) ^ ((q4 | ((j4 & 1) << 2)) << 1)) * 16) + (p4 & 1) * 8, ad1 = ad0 ^ 32;
  u32 a_op = WS_OP + ((g * 32 + (j4 * 4 + w) * 2 + (n16 >> 3)) * 32 + s) * 128 + (n16 & 7) * 16;
  u32 a_st = (WS_ST + ((g * 32 + j4 * 8 + 2 * w + ((n16 >> 2) & 1)) * 32 + s) * 64 + p4 * 16) | msk(n16 < 8);
  u32 ooff = WS_OP + (me * 32 + (l >> 1)) * 128 + (2 * w + (l & 1)) * 16;
  u32 adw = O_WO + (4 * j4 + q4) * 1024 + (((16 * w + (p4 >> 1)) ^ (((4 * j4 + q4) & 7) << 1)) * 16) + (p4 & 1) * 8;
  u32 a_sp = WS_SB + ((w * 16 + (l >> 2)) * 256 + me) * 64 + (l & 3) * 16;
  u32 off0 = WS_SB + ((b & 63) * 256 + 64 * w + (l >> 2)) * 64 + (l & 3) * 16;
  asm volatile("" : "+v"(qoff), "+v"(adk), "+v"(a_qs), "+v"(a_qw), "+v"(a_pw), "+v"(a_pr), "+v"(a_mw), "+v"(a_mr), "+v"(ad0), "+v"(ad1));
  asm volatile("" : "+v"(a_op), "+v"(a_st), "+v"(ooff), "+v"(adw), "+v"(a_sp), "+v"(off0));
  const u32 a_fq = lb + O_FL + 4 * F_Q, a_fp = lb + O_FL + 4 * F_P;
  u32x4 kf[8];
  {
    wait_until(tpub + Q_D);
    u32x4 qa = ldsc(rws, qoff), qb = ldsc(rws, qoff + 2048);
    wait_landed(S, seen, N_W1 + 8 * (w + 1));
#pragma unroll
    for (int i = 0; i < 8; ++i) kf[i] = lds_ld<u32x4>(S, adk + (i >> 2) * 4096 + (((4 * (i & 3) + j4) ^ n16) * 16));
#pragma unroll 1
    for (int spin = 0; spin < SPIN_CAP && !__all(tok(qa, tag) & tok(qb, tag)); ++spin) { MEMCLOB(); qa = ldsc(rws, qoff); qb = ldsc(rws, qoff + 2048); }
    const float q0 = x32_sum(lo16(qa.x) + lo16(qb.x)), q1 = x32_sum(hi16(qa.x) + hi16(qb.x)), q2 = x32_sum(lo16(qa.z) + lo16(qb.z)), q3 = x32_sum(hi16(qa.z) + hi16(qb.z));
    if (l < 32) lds_st<u32x2>(S, a_qw, u32x2{pk(q0, q1), pk(q2, q3)});
    if (l == 0) set_flag(S, F_Q + w, 1);
  }
  {
    u32x4 fl4, bq0, bq1, bq2, bq3;
#pragma unroll 1
    for (int spin = 0; spin < LSPIN; ++spin) {
      asm volatile("ds_read_b128 %0, %5\n\tds_read_b128 %1, %6\n\tds_read_b128 %2, %6 offset:64\n\tds_read_b128 %3, %6 offset:128\n\tds_read_b128 %4, %6 offset:192\n\ts_waitcnt lgkmcnt(0)"
                   : "=&v"(fl4), "=&v"(bq0), "=&v"(bq1), "=&v"(bq2), "=&v"(bq3) : "v"(a_fq), "v"(a_qs) : "memory");
      if (all4(fl4)) break;
    }
    f32x4 d0 = mfma32(kf[0], bq0, f32x4{0.f, 0.f, 0.f, 0.f}), d1 = mfma32(kf[4], bq0, f32x4{0.f, 0.f, 0.f, 0.f});
    d0 = mfma32(kf[1], bq1, d0); d1 = mfma32(kf[5], bq1, d1);
    d0 = mfma32(kf[2], bq2, d0); d1 = mfma32(kf[6], bq2, d1);
    d0 = mfma32(kf[3], bq3, d0); d1 = mfma32(kf[7], bq3, d1);
    constexpr float C2 = 0.08838834764831845f * 1.4426950408889634f;
    float sc[8];
#pragma unroll
    for (int i = 0; i < 4; ++i) { sc[i] = d0[i] * C2; sc[4 + i] = d1[i] * C2; }
    const float mw = xr_max(fmaxf(fmaxf(fmaxf(sc[0], sc[1]), fmaxf(sc[2], sc[3])), fmaxf(fmaxf(sc[4], sc[5]), fmaxf(sc[6], sc[7]))));
    float p[8];
#pragma unroll
    for (int i = 0; i < 8; ++i) p[i] = ex2(sc[i] - mw);
    if (n16 < 4) {
      lds_st<u32x2>(S, a_pw, u32x2{pk(p[0], p[1]), pk(p[2], p[3])});
      lds_st<u32x2>(S, a_pw + 32, u32x2{pk(p[4], p[5]), pk(p[6], p[7])});
      if (l < 4) lds_st<float>(S, a_mw, mw);
    }
    if (l == 0) set_flag(S, F_P + w, 1);
  }
  {
    u32x4 fl4, ap[4], m4u;
#pragma unroll 1
    for (int spin = 0; spin < LSPIN; ++spin) {
      asm volatile("ds_read_b128 %0, %6\n\tds_read_b128 %1, %7\n\tds_read_b128 %2, %7 offset:64\n\tds_read_b128 %3, %7 offset:128\n\tds_read_b128 %4, %7 offset:192\n\tds_read_b128 %5, %8\n\ts_waitcnt lgkmcnt(0)"
                   : "=&v"(fl4), "=&v"(ap[0]), "=&v"(ap[1]), "=&v"(ap[2]), "=&v"(ap[3]), "=&v"(m4u) : "v"(a_fp), "v"(a_pr), "v"(a_mr) : "memory");
      if (all4(fl4)) break;
    }
    const float m0 = f_of(m4u.x), m1 = f_of(m4u.y), m2 = f_of(m4u.z), m3 = f_of(m4u.w);
    const float M = vmax(vmax3(m0, m1, m2), m3);
    const float al[4] = {ex2(m0 - M), ex2(m1 - M), ex2(m2 - M), ex2(m3 - M)};
    const u32x4 ones = {0x3f803f80u, 0x3f803f80u, 0x3f803f80u, 0x3f803f80u};
    float L = 0;
#pragma unroll
    for (int ks = 0; ks < 4; ++ks) L = fmaf(al[ks], mfma32(ap[ks], ones, f32x4{0.f, 0.f, 0.f, 0.f})[0], L);
    stsc(rws, a_st, u32x4{u_of(M), tag, u_of(L), tag});
    float o0 = 0, o1 = 0;
    wait_lazy(S, F_LANDED, N_K + 16);
    wait_landed(S, seen, N_K + 24);
    const bool early = seen >= ST_AT;
    if (early && w == 0 && l < 32) arrive(ws + WS_HB + (g * 32 + l) * 128, 1u << s);
    {
      u32x2 vf[12];
#pragma unroll
      for (int i = 0; i < 12; ++i) vf[i] = lds_tr(S, ((i & 2) ? ad1 : ad0) + (i >> 2) * 8192 + (i & 1) * 1024);
#pragma unroll
      for (int ks = 0; ks < 3; ++ks) {
        o0 = fmaf(al[ks], mfma32(ap[ks], u32x4{vf[4 * ks].x, vf[4 * ks].y, vf[4 * ks + 1].x, vf[4 * ks + 1].y}, f32x4{0.f, 0.f, 0.f, 0.f})[0], o0);
        o1 = fmaf(al[ks], mfma32(ap[ks], u32x4{vf[4 * ks + 2].x, vf[4 * ks + 2].y, vf[4 * ks + 3].x, vf[4 * ks + 3].y}, f32x4{0.f, 0.f, 0.f, 0.f})[0], o1);
      }
    }
    if (!early) {
      wait_landed(S, seen, ST_AT);
      if (w == 0 && l < 32) arrive(ws + WS_HB + (g * 32 + l) * 128, 1u << s);
    }
    wait_landed(S, seen, N_V);
    {
      const u32x2 a0 = lds_tr(S, ad0 + 3 * 8192), a1 = lds_tr(S, ad0 + 3 * 8192 + 1024), c0 = lds_tr(S, ad1 + 3 * 8192), c1 = lds_tr(S, ad1 + 3 * 8192 + 1024);
      o0 = fmaf(al[3], mfma32(ap[3], u32x4{a0.x, a0.y, a1.x, a1.y}, f32x4{0.f, 0.f, 0.f, 0.f})[0], o0);
      o1 = fmaf(al[3], mfma32(ap[3], u32x4{c0.x, c0.y, c1.x, c1.y}, f32x4{0.f, 0.f, 0.f, 0.f})[0], o1);
    }
    stsc(rws, a_op, u32x4{u_of(o0), tag, u_of(o1), tag});
  }
  {
    wait_ge(S, F_OOK, 1);
    u32x4 ov = ldsc(rws, ooff);
    wait_landed(S, seen, N_WO);
    u32x2 bw[8];
#pragma unroll
    for (int tt = 0; tt < 8; ++tt) bw[tt] = lds_tr(S, adw ^ (tt << 5));
    u32 bp = 0;
    if (hasM) { wait_ge(S, F_B + w, 1); bp = lds_ld<u32>(S, O_BP + w * 256 + l * 4); }
    wait_ge(S, F_WOK, 1);
    const float wn = lds_ld<float>(S, O_WT + (l >> 1) * 4);
#pragma unroll 1
    for (int spin = 0; spin < SPIN_CAP && !__all(tok(ov, tag)); ++spin) { MEMCLOB(); ov = ldsc(rws, ooff); }
    if (t == 0) set_flag(S, F_OG, 1);
    float o0 = wn * f_of(ov.x), o1 = wn * f_of(ov.z);
    o0 += dpp<XOR2>(o0); o1 += dpp<XOR2>(o1); o0 += dpp<ROR4>(o0); o1 += dpp<ROR4>(o1); o0 += dpp<ROR8>(o0); o1 += dpp<ROR8>(o1);
    o0 = x16_sum(o0); o1 = x16_sum(o1); o0 = x32_sum(o0); o1 = x32_sum(o1);
    const u32 pr = pk(o0, o1), po = __builtin_amdgcn_mov_dpp(pr, XOR1, 0xf, 0xf, true);
    if (l == 0) { MEMCLOB(); *(volatile LAS u32x4*)(S + O_AV + w * 16) = u32x4{pr, po, 1u, 1u}; MEMCLOB(); }
    u32x4 av;
#pragma unroll 1
    for (int spin = 0; spin < LSPIN; ++spin) { av = *(volatile LAS u32x4*)(S + O_AV + j4 * 16); if (__all(av.z != 0)) break; }
    const u32x2 a = {av.x, av.y};
    f32x4 d[8];
#pragma unroll
    for (int tt = 0; tt < 8; ++tt) d[tt] = mfma16(a, bw[tt], f32x4{0.f, 0.f, 0.f, 0.f});
    const bool b0 = j4 & 1, b1_ = j4 & 2;
    const float e0 = b1_ ? (b0 ? d[6][0] : d[4][0]) : (b0 ? d[2][0] : d[0][0]), e1 = b1_ ? (b0 ? d[7][0] : d[5][0]) : (b0 ? d[3][0] : d[1][0]);
    stsc(rws, a_sp, u32x4{pk(e0, e1), tag, bp, tag});
  }
  if (b < NC) {
    const float b2f = f_of(b2h << 16), xff = f_of(xfh << 16);
    u32 a_fw = O_FIN + (l & 3) * 64 + w * 4;
    u32 a_ff = lb + O_FL + 4 * F_F, a_fr = lb + O_FIN + (l & 3) * 64 + ((l >> 2) & 1) * 16;
    asm volatile("" : "+v"(a_fw), "+v"(a_ff), "+v"(a_fr));
    wait_ge(S, F_FOK, 1);
    u32x4 v[4];
#pragma unroll
    for (int i = 0; i < 4; ++i) v[i] = ldsc(rws, off0 + i * 1024);
#pragma unroll 1
    for (int spin = 0; spin < SPIN_CAP && !__all(tok(v[0], tag) & tok(v[1], tag) & tok(v[2], tag) & tok(v[3], tag)); ++spin) {
      MEMCLOB();
#pragma unroll
      for (int i = 0; i < 4; ++i) v[i] = ldsc(rws, off0 + i * 1024);
    }
    const u32 pick = (n16 & 1) ? 0x3f800000u : 0x00003f80u;
    const u32x4 sel = {pick, pick, pick, pick};
    const f32x4 ds = mfma32(sel, u32x4{v[0].x, v[1].x, v[2].x, v[3].x}, f32x4{0.f, 0.f, 0.f, 0.f});
    const f32x4 dm = mfma32(sel, u32x4{v[0].z, v[1].z, v[2].z, v[3].z}, f32x4{0.f, 0.f, 0.f, 0.f});
    float r[4] = {ds[0], ds[1], dm[0], dm[1]};
#pragma unroll
    for (int i = 0; i < 4; ++i) r[i] += dpp<ROR4>(r[i]);
#pragma unroll
    for (int i = 0; i < 4; ++i) r[i] += dpp<ROR8>(r[i]);
    if (l < 4) {
#pragma unroll
      for (int i = 0; i < 4; ++i) lds_st<float>(S, a_fw + i * 16, r[i]);
    }
    if (l == 0) set_flag(S, F_F + w, 1);
    if (w == 0) {
      u32x4 fl4, s4, m4;
#pragma unroll 1
      for (int spin = 0; spin < LSPIN; ++spin) {
        asm volatile("ds_read_b128 %0, %3\n\tds_read_b128 %1, %4\n\tds_read_b128 %2, %4 offset:32\n\ts_waitcnt lgkmcnt(0)"
                     : "=&v"(fl4), "=&v"(s4), "=&v"(m4) : "v"(a_ff), "v"(a_fr) : "memory");
        if (all4(fl4)) break;
      }
      const float sv = (f_of(s4.x) + f_of(s4.y)) + (f_of(s4.z) + f_of(s4.w)), mv = (f_of(m4.x) + f_of(m4.y)) + (f_of(m4.z) + f_of(m4.w));
      const float B = rbf(rbf(mv) + b2f);
      const float gt = rbf(B * rcp(1.f + ex2(-B * 1.4426950408889634f)));
      const float o = xff + rbf(rbf(sv) * gt);
      if (l < 8) out[fcol] = (u16)pk(o, o);
      if (b == 0 && l == 0) __builtin_amdgcn_raw_buffer_store_b32(epoch + 1u, rws, WS_EPOCH, 0, AUX_SC1);
    }
  }
  __builtin_amdgcn_endpgm();
  }

  __builtin_amdgcn_s_setprio(3);
  const u32 fl = (u32)(size_t)(S + O_FL + 4 * F_LANDED);
  asm volatile("" :: "s"(kc), "s"(vc), "s"(w1), "s"(w2), "s"(wo), "s"(x), "s"(ws), "s"(b1), "s"(b2));
  {
    __builtin_amdgcn_raw_ptr_buffer_load_lds(mk(x, 1024), (LAS void*)(S + O_X), 16, l * 16, 0, 0, 0);
    const u32 f0 = g * 256 + (s >> 2) * 32 + (s & 3) * 4, c0 = ((b & 63) >> 4) * 128 + ((b >> 2) & 3) * 32 + (b & 3) * 4;
    const char* p = (const char*)ws;
    if (l == 1 || l == 2) p = (const char*)b1 + (((hasM ? f0 : 0) + (l - 1) * 16) * 2 & ~15u);
    if (l == 3 || l == 4) p = (const char*)b2 + ((c0 + (l - 3) * 16) * 2 & ~15u);
    __builtin_amdgcn_global_load_lds((const __attribute__((address_space(1))) void*)p, (LAS void*)(S + O_MISC), 16, 0, 0);
  }
  {
    const rsrc_t r1 = mk(w1, hasM ? 512 * FF * 2 : 0);
    const u32 hb = l >> 5, bl = hb * (FF * 2) + (((l & 31) ^ (hb << 1)) * 16);
    const u32 base = (s * 16) * (FF * 2) + g * 512;
#pragma unroll 1
    for (int G = 0; G < 2; ++G) dma4(r1, S, O_W1 + G * 4096, bl, (bl ^ 64) + 6144, (bl ^ 128) + 2 * 6144, (bl ^ 192) + 3 * 6144, base + G * 8 * (FF * 2));
  }
  const size_t kvoff = ((size_t)g * SK + (SK - WIN) + s * 128) * 256;
  {
    const rsrc_t rk = mk((const char*)kc + kvoff, 32768);
    const u32 v0 = j4 * 256 + ((n16 ^ j4) * 16);
    wait_until(tstart + K_T);
#pragma unroll 1
    for (int G = 0; G < 8; ++G) { landed<WINDOW - 4>(fl, N_W1 + 4 * G - (WINDOW - 4)); dma4(rk, S, O_K + G * 4096, v0, v0 ^ 64, v0 ^ 128, v0 ^ 192, G * 4096); }
  }
  {
    const rsrc_t rv = mk((const char*)vc + kvoff, 32768);
    const u32 v0 = j4 * 256 + ((n16 ^ (j4 << 1)) * 16);
#pragma unroll 1
    for (int G = 0; G < 8; ++G) { landed<WINDOW - 4>(fl, N_K + 4 * G - (WINDOW - 4)); dma4(rv, S, O_V + G * 4096, v0, v0, v0 ^ 128, v0 ^ 128, G * 4096); }
  }
  {
    const rsrc_t r2 = mk((const char*)w2 - 1024, hasM ? 512 * FF * 2 + 1024 : 0);
    const u32 f0 = g * 256 + (s >> 2) * 32 + (s & 3) * 4;
    const rsrc_t ro = mk((const char*)wo - 1024, 4096 * 512 * 2 + 1024);
    const u32 r0 = g * 512 + (s >> 3) * 128 + ((s >> 1) & 3) * 32 + (s & 1) * 8;
#pragma unroll 1
    for (int k = 0; k < 6; ++k) {
      landed<WINDOW - 4>(fl, N_V + 4 * k - (WINDOW - 4));
      if (k < 2) {
        const u32 v = (l * 16) ^ (k << 7);
        dma4(r2, S, O_W2 + k * 4096, v + 1024, (v ^ 32) + 16384, v ^ 64, (v ^ 96) + 15360, (f0 + 2 * k) * 1024);
      } else {
        const int G = k - 2;
        const u32 v = (l * 16) ^ ((G & 1) << 7);
        dma4(ro, S, O_WO + G * 4096, v + 1024, (v ^ 32) + 16384, v ^ 64, (v ^ 96) + 15360, (r0 + 2 * G) * 1024);
      }
    }
  }
  drain<WINDOW - 4>(fl, N_WO);
}

extern "C" void kernel_launch(void* const* d_in, const int* in_sizes, int n_in, void* d_out, int out_size, void* d_ws, size_t ws_size, hipStream_t stream) {
  fused_decode<<<NWG, NT, 0, stream>>>((const u16*)d_in[0], (const u16*)d_in[1], (const u16*)d_in[2], (const u16*)d_in[3], (const u16*)d_in[4],
                                       (const u16*)d_in[6], (const u16*)d_in[7], (const u16*)d_in[8], (const u16*)d_in[9], (u16*)d_out, (unsigned char*)d_ws);
}
```
